```python
import torch

def _softmax(x, axis=-1):
    return torch.softmax(x, dim=axis)
def _gelu(x):
    return torch.nn.functional.gelu(x, approximate="tanh")
def _silu(x):
    return torch.nn.functional.silu(x)
def _sigmoid(x):
    return torch.sigmoid(x)
def _layernorm(x, axis=-1, eps=1e-5):
    m = x.mean(dim=axis, keepdim=True)
    v = x.var(dim=axis, keepdim=True, unbiased=False)
    return (x - m) / torch.sqrt(v + eps)
def _scatter_add(tgt, idx, src, axis=0):
    return tgt.clone().index_add_(axis, idx, src)
def _segment_sum(src, seg, n):
    out = torch.zeros((n,) + src.shape[1:], dtype=src.dtype)
    return out.index_add_(0, seg, src)
def _pad(x, pw):
    flat = tuple(v for pair in reversed(pw) for v in pair)
    return torch.nn.functional.pad(x, flat)

kernel_name = 'recurrent_scan__mha_block_cached__dense_activation_relu__prompt'

def setup_inputs():

    _w = (lambda a: (lambda t: t.to(torch.bfloat16) if t.is_floating_point() else t)(torch.as_tensor(a)))
    def _A_setup():
        B, Sq, D = 1, 2048, 576
        H, Sk, Dh = 32, 2048, 128
        return {
            "x": _w((lambda *s: torch.randn(*s, dtype=torch.bfloat16) * 0.05)(B, Sq, D)),
            "k_cache": _w((lambda *s: torch.randn(*s, dtype=torch.bfloat16) * 0.8)(B, H, Sk, Dh)),
            "v_cache": _w((lambda *s: torch.randn(*s, dtype=torch.bfloat16) * 0.2)(B, H, Sk, Dh)),
            "wq": _w((lambda *s: torch.randn(*s, dtype=torch.bfloat16) * 0.8)(D, H * Dh) * (D ** -0.5)),
            "wo": _w((lambda *s: torch.randn(*s, dtype=torch.bfloat16) * 0.2)(H * Dh, D) * ((H * Dh) ** -0.5)),
        }
    _A_in = _A_setup()

    def _B_setup():
        B, S, D, Dff = 1, 2048, 576, 4608
        return {
            "x": _w((lambda *s: torch.randn(*s, dtype=torch.bfloat16) * 0.05)(B, S, D)),
            "w1": _w((lambda *s: torch.randn(*s, dtype=torch.bfloat16) * 0.1)(D, Dff)),
            "w2": _w((lambda *s: torch.randn(*s, dtype=torch.bfloat16) * 0.1)(Dff, D)),
        }
    _B_in = _B_setup()

    return {
        "A_x": _A_in["x"],
        "A_k_cache": _A_in["k_cache"],
        "A_v_cache": _A_in["v_cache"],
        "A_wq": _A_in["wq"],
        "A_wo": _A_in["wo"],
        "B_x": _B_in["x"],
        "B_w1": _B_in["w1"],
        "B_w2": _B_in["w2"],
    }

def reference(A_x, A_k_cache, A_v_cache, A_wq, A_wo, B_x, B_w1, B_w2):
    def _A_ref(x, k_cache, v_cache, wq, wo):
        B, Sq, D = x.shape
        H, Dh = 32, 128
        q = (lambda x, *d: x.permute(*d))((x @ wq).reshape(B, Sq, H, Dh), 0, 2, 1, 3)
        scores = torch.einsum("bhqd,bhkd->bhqk", q, k_cache) * (Dh ** -0.5)
        p = _softmax(scores, axis=-1)
        o = torch.einsum("bhqk,bhkd->bhqd", p, v_cache)
        return (lambda x, *d: x.permute(*d))(o, 0, 2, 1, 3).reshape(B, Sq, H * Dh) @ wo

    def _B_ref(x, w1, w2):
        h = torch.einsum("bsd,df->bsf", x, w1)
        zero = torch.zeros_like(h)
        h = torch.maximum(h, zero)
        return torch.einsum("bsf,fd->bsd", h, w2)

    x = A_x
    xn = _layernorm(x, axis=-1)
    s = _A_ref(xn, A_k_cache, A_v_cache, A_wq, A_wo)
    g = _silu(_B_ref(xn, B_w1, B_w2))
    return x + s * g
```

```cpp
#include <hip/hip_runtime.h>
#include <hip/hip_bf16.h>
typedef __bf16 bf16x8 __attribute__((ext_vector_type(8)));
typedef float f32x4 __attribute__((ext_vector_type(4)));
typedef unsigned short u16;
__device__ __forceinline__ float b2f(u16 v){ return __uint_as_float(((unsigned)v)<<16); }
__device__ __forceinline__ u16 f2b(float f){ __bf16 b=(__bf16)f; return __builtin_bit_cast(u16,b); }

constexpr int S=2048, D=576, H=32, DH=128, FF=4608;

__global__ void ln_k(const u16* x, u16* xn){
  int row=blockIdx.x; int l=threadIdx.x;
  float v[9]; float s=0;
  for(int i=0;i<9;i++){ v[i]=b2f(x[row*D+l+64*i]); s+=v[i]; }
  for(int o=32;o;o>>=1) s+=__shfl_xor(s,o);
  float m=s/D; float q=0;
  for(int i=0;i<9;i++){ float d=v[i]-m; q+=d*d; }
  for(int o=32;o;o>>=1) q+=__shfl_xor(q,o);
  float r=1.0f/sqrtf(q/D+1e-5f);
  for(int i=0;i<9;i++) xn[row*D+l+64*i]=f2b((v[i]-m)*r);
}
template<int MODE> __global__ void gemm_k(const u16* A,const u16* B,void* C,int M,int N,int K){
  int l=threadIdx.x&63; int w=threadIdx.x>>6;
  int m0=(blockIdx.y*4+w)*16, n0=blockIdx.x*64;
  f32x4 acc[4]={};
  for(int k=0;k<K;k+=32){
    bf16x8 a=*(const bf16x8*)(A+(size_t)(m0+(l&15))*K+k+8*(l>>4));
    for(int t=0;t<4;t++){
      bf16x8 b;
      for(int j=0;j<8;j++) b[j]=__builtin_bit_cast(__bf16,B[(size_t)(k+8*(l>>4)+j)*N+n0+16*t+(l&15)]);
      acc[t]=__builtin_amdgcn_mfma_f32_16x16x32_bf16(a,b,acc[t],0,0,0);
    }
  }
  for(int t=0;t<4;t++) for(int r=0;r<4;r++){
    int row=m0+4*(l>>4)+r, col=n0+16*t+(l&15); float v=acc[t][r];
    if(MODE==1) v=fmaxf(v,0.f);
    if(MODE==2) ((float*)C)[(size_t)row*N+col]=v; else ((u16*)C)[(size_t)row*N+col]=f2b(v);
  }
}
__global__ void attn_k(const u16* q,const u16* kc,const u16* vc,u16* o){
  int l=threadIdx.x&63; int w=threadIdx.x>>6;
  int h=blockIdx.y; int q0=(blockIdx.x*4+w)*16; int g=l>>4, c=l&15;
  bf16x8 qf[4];
  for(int s=0;s<4;s++) qf[s]=*(const bf16x8*)(q+(size_t)(q0+c)*(H*DH)+h*DH+32*s+8*g);
  f32x4 O[8]={}; float m=-1e30f, lsum=0;
  const float sc=0.08838834764831845f*1.4426950408889634f;
  for(int k0=0;k0<S;k0+=32){
    f32x4 s2[2];
    for(int kb=0;kb<2;kb++){ f32x4 a={};
      for(int s=0;s<4;s++){ bf16x8 kf=*(const bf16x8*)(kc+((size_t)h*S+k0+16*kb+c)*DH+32*s+8*g);
        a=__builtin_amdgcn_mfma_f32_16x16x32_bf16(kf,qf[s],a,0,0,0);} s2[kb]=a; }
    float mx=-1e30f;
    for(int kb=0;kb<2;kb++) for(int r=0;r<4;r++){ s2[kb][r]*=sc; mx=fmaxf(mx,s2[kb][r]); }
    mx=fmaxf(mx,__shfl_xor(mx,16)); mx=fmaxf(mx,__shfl_xor(mx,32));
    float mn=fmaxf(m,mx); float al=exp2f(m-mn); m=mn; float rs=0; bf16x8 p;
    for(int kb=0;kb<2;kb++) for(int r=0;r<4;r++){ float e=exp2f(s2[kb][r]-m); rs+=e; p[4*kb+r]=(__bf16)e; }
    rs+=__shfl_xor(rs,16); rs+=__shfl_xor(rs,32); lsum=lsum*al+rs;
    for(int db=0;db<8;db++){ bf16x8 vf;
      for(int j=0;j<8;j++) vf[j]=__builtin_bit_cast(__bf16,vc[((size_t)h*S+k0+16*(j>>2)+4*g+(j&3))*DH+16*db+c]);
      for(int r=0;r<4;r++) O[db][r]*=al;
      O[db]=__builtin_amdgcn_mfma_f32_16x16x32_bf16(vf,p,O[db],0,0,0);
    }
  }
  float il=1.f/lsum;
  for(int db=0;db<8;db++) for(int r=0;r<4;r++) o[(size_t)(q0+c)*(H*DH)+h*DH+16*db+4*g+r]=f2b(O[db][r]*il);
}
__global__ void fin_k(const u16* x,const float* s,const float* g,u16* out,int n){
  int i=blockIdx.x*256+threadIdx.x; if(i>=n) return;
  float gv=g[i]; float si=gv/(1.f+__expf(-gv));
  out[i]=f2b(b2f(x[i])+s[i]*si);
}
extern "C" void kernel_launch(void* const* d_in, const int* in_sizes, int n_in, void* d_out, int out_size, void* d_ws, size_t ws_size, hipStream_t stream) {
  const u16* x=(const u16*)d_in[0]; const u16* kc=(const u16*)d_in[1]; const u16* vc=(const u16*)d_in[2];
  const u16* wq=(const u16*)d_in[3]; const u16* wo=(const u16*)d_in[4]; const u16* w1=(const u16*)d_in[6]; const u16* w2=(const u16*)d_in[7];
  char* ws=(char*)d_ws;
  u16* xn=(u16*)ws; u16* q=(u16*)(ws+(4<<20)); u16* hh=(u16*)(ws+(24<<20)); u16* o=(u16*)(ws+(48<<20));
  float* s=(float*)(ws+(72<<20)); float* g=(float*)(ws+(80<<20));
  ln_k<<<S,64,0,stream>>>(x,xn);
  gemm_k<0><<<dim3(H*DH/64,S/64),256,0,stream>>>(xn,wq,q,S,H*DH,D);
  gemm_k<1><<<dim3(FF/64,S/64),256,0,stream>>>(xn,w1,hh,S,FF,D);
  attn_k<<<dim3(S/64,H),256,0,stream>>>(q,kc,vc,o);
  gemm_k<2><<<dim3(D/64,S/64),256,0,stream>>>(o,wo,s,S,D,H*DH);
  gemm_k<2><<<dim3(D/64,S/64),256,0,stream>>>(hh,w2,g,S,D,FF);
  fin_k<<<(S*D+255)/256,256,0,stream>>>(x,s,g,(u16*)d_out,S*D);
}
```

```cpp
#include <hip/hip_runtime.h>
#include <hip/hip_bf16.h>
__global__ __launch_bounds__(256, 1) __attribute__((aligned(4096))) void k_fused(const void* x, const void* kc, const void* vc, const void* wq, const void* wo, const void* w1, const void* w2, void* out, void* ws) {
  __shared__ __attribute__((aligned(1024))) char lds[163840];
  asm volatile(
".LK_START:\n"
"s_load_dwordx16 s[64:79], %0, 0\n"
"s_load_dwordx2 s[80:81], %0, 64\n"
"v_mbcnt_lo_u32_b32 v0, -1, 0\n"
"v_mbcnt_hi_u32_b32 v0, -1, v0\n"
"v_lshlrev_b32 v1, 4, v0\n"
"s_mov_b32 s83, %2\n"
"s_lshr_b32 s54, %1, 3\n"
"s_getpc_b64 s[12:13]\n"
".LPF_PC0:\n"
"s_sub_u32 s12, s12, .LPF_PC0-.LK_START\n"
"s_subb_u32 s13, s13, 0\n"
"s_mov_b32 s14, .LK_END-.LK_START\n"
"s_mov_b32 s15, 0x20000\n"
"s_lshl_b32 s10, s54, 2\n"
"s_add_u32 s10, s10, s83\n"
"s_lshl_b32 s10, s10, 10\n"
"buffer_load_dword v31, v1, s[12:15], s10 offen\n"
"s_lshr_b32 s10, s10, 3\n"
"s_and_b32 s62, %1, 7\n"
"s_bfe_u32 s61, %1, 0x20003\n"
"s_lshr_b32 s59, %1, 5\n"
"s_lshl2_add_u32 s82, s59, %2\n"
"s_lshl_b32 s9, %2, 11\n"
"v_lshrrev_b32 v32, 3, v0\n"
"v_and_b32 v34, 6, v32\n"
"v_xor_b32 v33, v0, v34\n"
"v_and_b32 v33, 7, v33\n"
"v_lshlrev_b32 v33, 4, v33\n"
"v_mul_u32_u24 v34, 9216, v32\n"
"v_add_u32 v3, v34, v33\n"
"s_mul_i32 s52, s62, 1152\n"
"s_mul_i32 s53, s9, 36\n"
"s_add_u32 s52, s52, s53\n"
"s_lshl_b32 s53, s61, 8\n"
"s_add_u32 s52, s52, s53\n"
"s_sub_u32 s30, 5308416, s52\n"
"s_mov_b32 s31, 0x20000\n"
"s_mul_i32 s53, s82, 73728\n"
"s_mov_b32 s22, 73728\n"
"s_mov_b32 s23, s31\n"
"s_lshl2_add_u32 s63, s62, s61\n"
"s_lshl_b32 s54, s59, 5\n"
"s_add_u32 s55, s54, s63\n"
"v_add_u32 v40, s54, v0\n"
"v_lshlrev_b32 v41, 5, v0\n"
"s_sub_u32 s84, s63, 1024\n"
"v_add_u32 v41, s84, v41\n"
"v_cmp_gt_u32 vcc, 32, v0\n"
"s_add_u32 s84, s59, 32\n"
"v_mov_b32 v42, s84\n"
"v_cndmask_b32 v40, v41, v40, vcc\n"
"v_mov_b32 v41, s63\n"
"v_cndmask_b32 v41, v42, v41, vcc\n"
"v_lshl_add_u32 v40, v40, 6, v41\n"
"v_cmp_gt_u32 vcc, 40, v0\n"
"v_lshlrev_b32 v40, 3, v40\n"
"v_mov_b32 v42, 0x40000000\n"
"v_cndmask_b32 v18, v42, v40, vcc\n"
"s_lshl_b32 s54, s55, 6\n"
"v_add_lshl_u32 v40, v0, s54, 3\n"
"v_cndmask_b32 v19, v42, v40, vcc\n"
"v_and_b32 v40, 31, v0\n"
"v_lshrrev_b32 v41, 5, v0\n"
"v_mul_u32_u24 v56, 1152, v41\n"
"v_lshl_add_u32 v57, v40, 2, v56\n"
"v_lshl_add_u32 v56, v40, 4, v56\n"
"s_lshl2_add_u32 s54, s55, %2\n"
"s_mul_i32 s54, s54, 2304\n"
"s_mov_b32 s38, 2304\n"
"s_mov_b32 s39, s31\n"
"s_cmp_eq_u32 %2, 0\n"
"s_cselect_b32 s46, 720896, 0\n"
"s_mov_b32 s47, s31\n"
"s_mov_b32 s84, 196608\n"
"v_mov_b32 v21, 0\n"
"v_mov_b32 v20, 1\n"
"s_waitcnt lgkmcnt(0)\n"
"s_add_u32 s44, s80, 3145728\n"
"s_addc_u32 s45, s81, 0\n"
"s_add_u32 s36, s64, s54\n"
"s_addc_u32 s37, s65, 0\n"
"buffer_load_dwordx4 v[44:47], v56, s[36:39], 0 offen offset:0\n"
"buffer_load_dwordx4 v[48:51], v56, s[36:39], 0 offen offset:512\n"
"buffer_load_dword v52, v57, s[36:39], 0 offen offset:1024\n"
"buffer_store_dword v21, v19, s[44:47], s84 offen sc1\n"
"s_add_u32 s28, s74, s52\n"
"s_addc_u32 s29, s75, 0\n"
"s_add_u32 s20, s80, s53\n"
"s_addc_u32 s21, s81, 0\n"
"s_lshl_b32 s11, s55, 8\n"
"s_add_u32 m0, s9, 1024\n"
"s_mov_b32 s52, 128\n"
"buffer_load_dwordx4 v3, s[28:31], s52 offen lds\n"
"s_add_u32 m0, s9, 0\n"
"s_mov_b32 s53, 0\n"
"buffer_load_dwordx4 v3, s[28:31], s53 offen lds\n"
"s_add_u32 m0, s9, 9216\n"
"s_mov_b32 s52, 295040\n"
"buffer_load_dwordx4 v3, s[28:31], s52 offen lds\n"
"s_add_u32 m0, s9, 8192\n"
"s_mov_b32 s53, 294912\n"
"buffer_load_dwordx4 v3, s[28:31], s53 offen lds\n"
"s_lshr_b32 s58, s62, 1\n"
"s_lshl_b32 s48, %2, 12\n"
"s_lshl_b32 s49, %2, 10\n"
"s_mov_b32 s60, 0\n"
"v_lshl_add_u32 v2, v32, 13, v33\n"
"s_mul_i32 s54, s61, 224\n"
"s_mul_i32 s55, s9, 36\n"
"s_sub_u32 s54, s55, s54\n"
"s_add_u32 s54, s54, 1024\n"
"v_lshrrev_b32 v32, 1, v0\n"
"v_mul_u32_u24 v32, 9216, v32\n"
"v_and_b32 v33, 1, v0\n"
"v_lshl_add_u32 v32, v33, 4, v32\n"
"v_add_u32 v32, s54, v32\n"
"v_lshrrev_b32 v33, 5, v0\n"
"v_lshlrev_b32 v33, 31, v33\n"
"v_or_b32 v4, v32, v33\n"
"s_lshl_b32 s54, s63, 8\n"
"s_lshl_b32 s55, s9, 5\n"
"s_add_u32 s54, s54, s55\n"
"s_add_u32 s24, s70, s54\n"
"s_addc_u32 s25, s71, 0\n"
"s_sub_u32 s26, 4718592, s54\n"
"s_mov_b32 s27, s31\n"
"s_add_u32 m0, s49, 16384\n"
"s_mov_b32 s52, 0\n"
"buffer_load_dwordx4 v4, s[28:31], s52 offen lds\n"
"v_lshrrev_b32 v36, 5, v0\n"
"v_bfe_u32 v33, v0, 2, 3\n"
"v_lshlrev_b32 v32, 11, v36\n"
"v_lshl_add_u32 v32, v33, 7, v32\n"
"v_add_u32 v32, 0, v32\n"
"v_lshrrev_b32 v33, 1, v33\n"
"v_and_b32 v34, 3, v0\n"
"v_xor_b32 v35, 0, v33\n"
"v_lshl_add_u32 v35, v35, 5, v32\n"
"v_lshl_add_u32 v5, v34, 3, v35\n"
"v_xor_b32 v35, 1, v33\n"
"v_lshl_add_u32 v35, v35, 5, v32\n"
"v_lshl_add_u32 v6, v34, 3, v35\n"
"v_xor_b32 v35, 2, v33\n"
"v_lshl_add_u32 v35, v35, 5, v32\n"
"v_lshl_add_u32 v7, v34, 3, v35\n"
"v_xor_b32 v35, 3, v33\n"
"v_lshl_add_u32 v35, v35, 5, v32\n"
"v_lshl_add_u32 v8, v34, 3, v35\n"
"v_lshrrev_b32 v36, 1, v34\n"
"v_and_b32 v34, 1, v34\n"
"v_add_u32 v32, 32768, v32\n"
"v_or_b32 v35, 0, v36\n"
"v_xor_b32 v35, v35, v33\n"
"v_lshl_add_u32 v35, v35, 5, v32\n"
"v_lshl_add_u32 v9, v34, 4, v35\n"
"v_or_b32 v35, 2, v36\n"
"v_xor_b32 v35, v35, v33\n"
"v_lshl_add_u32 v35, v35, 5, v32\n"
"v_lshl_add_u32 v10, v34, 4, v35\n"
"v_lshlrev_b32 v11, 3, v0\n"
"v_add_u32 v11, 0, v11\n"
"v_lshl_add_u32 v58, %2, 1, v41\n"
"v_lshrrev_b32 v42, 2, v40\n"
"v_and_b32 v56, 1, v40\n"
"v_lshl_add_u32 v42, v42, 1, v56\n"
"v_and_b32 v59, 7, v42\n"
"v_xor_b32 v59, v59, v58\n"
"v_lshlrev_b32 v42, 8, v42\n"
"v_lshl_add_u32 v42, v59, 4, v42\n"
"v_and_b32 v57, 2, v40\n"
"v_lshl_add_u32 v42, v57, 2, v42\n"
"v_add_u32 v42, 20480, v42\n"
"v_lshrrev_b32 v43, 4, v40\n"
"v_bfe_u32 v57, v40, 1, 2\n"
"v_lshl_add_u32 v43, v43, 2, v57\n"
"v_lshrrev_b32 v59, 1, v43\n"
"v_xor_b32 v59, v59, v58\n"
"v_lshlrev_b32 v43, 7, v43\n"
"v_lshl_add_u32 v43, v59, 4, v43\n"
"v_lshl_add_u32 v43, v56, 2, v43\n"
"v_and_b32 v56, 8, v40\n"
"v_add_u32 v43, v43, v56\n"
"v_add_u32 v43, 20480, v43\n"
"v_lshlrev_b32 v40, 12, v58\n"
"v_bfe_u32 v56, v0, 3, 2\n"
"v_lshl_add_u32 v40, v56, 8, v40\n"
"v_and_b32 v56, 7, v0\n"
"v_lshl_add_u32 v40, v56, 4, v40\n"
"v_lshl_add_u32 v41, %2, 6, v0\n"
"v_bfe_u32 v56, v41, 4, 3\n"
"v_xor_b32 v41, v41, v56\n"
"v_lshlrev_b32 v41, 4, v41\n"
"v_add_u32 v41, 20480, v41\n"
"v_lshlrev_b32 v56, 16, v0\n"
"v_mov_b32 v57, 0\n"
"s_mov_b64 exec, 7\n"
"buffer_store_dword v57, v56, s[44:47], s11 offen sc1\n"
"s_mov_b64 exec, -1\n"
"s_lshr_b32 s54, s63, 3\n"
"s_lshl2_add_u32 s54, s59, s54\n"
"s_mul_i32 s54, s54, 72\n"
"s_bfe_u32 s55, s63, 0x20001\n"
"s_add_u32 s54, s54, s55\n"
"s_lshl_b32 s54, s54, 3\n"
"s_and_b32 s55, s63, 1\n"
"s_add_u32 s54, s54, s55\n"
"s_lshl_b32 s54, s54, 7\n"
"s_add_u32 s32, s80, s54\n"
"s_addc_u32 s33, s81, 0\n"
"s_mov_b32 s34, 70528\n"
"s_mov_b32 s35, 0x20000\n"
"v_mov_b32 v58, 0x3f803f80\n"
"s_waitcnt vmcnt(7)\n"
"v_dot2_f32_bf16 v56, v44, v58, 0\n"
"v_dot2_f32_bf16 v57, v45, v58, 0\n"
"v_dot2_f32_bf16 v56, v46, v58, v56\n"
"v_dot2_f32_bf16 v57, v47, v58, v57\n"
"v_dot2_f32_bf16 v56, v48, v58, v56\n"
"v_dot2_f32_bf16 v57, v49, v58, v57\n"
"v_dot2_f32_bf16 v56, v50, v58, v56\n"
"v_dot2_f32_bf16 v57, v51, v58, v57\n"
"v_dot2_f32_bf16 v56, v52, v58, v56\n"
"v_lshlrev_b32 v60, 16, v44\n"
"v_and_b32 v61, 0xffff0000, v44\n"
"v_lshlrev_b32 v62, 16, v45\n"
"v_add_f32 v56, v56, v57\n"
"v_and_b32 v63, 0xffff0000, v45\n"
"v_lshlrev_b32 v64, 16, v46\n"
"v_and_b32 v65, 0xffff0000, v46\n"
"v_add_f32_dpp v56, v56, v56 row_ror:8 row_mask:0xf bank_mask:0xf\n"
"v_lshlrev_b32 v66, 16, v47\n"
"v_and_b32 v67, 0xffff0000, v47\n"
"v_lshlrev_b32 v68, 16, v48\n"
"v_add_f32_dpp v56, v56, v56 row_ror:4 row_mask:0xf bank_mask:0xf\n"
"v_and_b32 v69, 0xffff0000, v48\n"
"v_lshlrev_b32 v70, 16, v49\n"
"v_add_f32_dpp v56, v56, v56 row_ror:2 row_mask:0xf bank_mask:0xf\n"
"v_and_b32 v71, 0xffff0000, v49\n"
"v_lshlrev_b32 v72, 16, v50\n"
"v_add_f32_dpp v56, v56, v56 row_ror:1 row_mask:0xf bank_mask:0xf\n"
"v_mov_b32 v57, v56\n"
"v_and_b32 v73, 0xffff0000, v50\n"
"v_lshlrev_b32 v74, 16, v51\n"
"v_permlane16_swap_b32 v56, v57\n"
"v_add_f32 v56, v56, v57\n"
"v_and_b32 v75, 0xffff0000, v51\n"
"v_lshlrev_b32 v76, 16, v52\n"
"v_and_b32 v77, 0xffff0000, v52\n"
"v_mul_f32 v56, 0xbae38e39, v56\n"
"v_pk_add_f32 v[60:61], v[60:61], v[56:57] op_sel_hi:[1,0]\n"
"v_pk_add_f32 v[62:63], v[62:63], v[56:57] op_sel_hi:[1,0]\n"
"v_pk_add_f32 v[64:65], v[64:65], v[56:57] op_sel_hi:[1,0]\n"
"v_pk_add_f32 v[66:67], v[66:67], v[56:57] op_sel_hi:[1,0]\n"
"v_pk_add_f32 v[68:69], v[68:69], v[56:57] op_sel_hi:[1,0]\n"
"v_pk_add_f32 v[70:71], v[70:71], v[56:57] op_sel_hi:[1,0]\n"
"v_pk_add_f32 v[72:73], v[72:73], v[56:57] op_sel_hi:[1,0]\n"
"v_pk_add_f32 v[74:75], v[74:75], v[56:57] op_sel_hi:[1,0]\n"
"v_pk_add_f32 v[76:77], v[76:77], v[56:57] op_sel_hi:[1,0]\n"
"v_pk_mul_f32 v[56:57], v[60:61], v[60:61]\n"
"v_pk_mul_f32 v[58:59], v[62:63], v[62:63]\n"
"v_pk_fma_f32 v[56:57], v[64:65], v[64:65], v[56:57]\n"
"v_pk_fma_f32 v[58:59], v[66:67], v[66:67], v[58:59]\n"
"v_pk_fma_f32 v[56:57], v[68:69], v[68:69], v[56:57]\n"
"v_pk_fma_f32 v[58:59], v[70:71], v[70:71], v[58:59]\n"
"v_pk_fma_f32 v[56:57], v[72:73], v[72:73], v[56:57]\n"
"v_pk_fma_f32 v[58:59], v[74:75], v[74:75], v[58:59]\n"
"v_pk_fma_f32 v[56:57], v[76:77], v[76:77], v[56:57]\n"
"v_pk_add_f32 v[56:57], v[56:57], v[58:59]\n"
"v_add_f32 v56, v56, v57\n"
"v_mov_b32 v58, 0x3727c5ac\n"
"s_mov_b32 s54, 0x8000\n"
"v_add_f32_dpp v56, v56, v56 row_ror:8 row_mask:0xf bank_mask:0xf\n"
"s_mov_b32 s55, 0x10000\n"
"s_nop 0\n"
"v_add_f32_dpp v56, v56, v56 row_ror:4 row_mask:0xf bank_mask:0xf\n"
"s_nop 1\n"
"v_add_f32_dpp v56, v56, v56 row_ror:2 row_mask:0xf bank_mask:0xf\n"
"s_nop 1\n"
"v_add_f32_dpp v56, v56, v56 row_ror:1 row_mask:0xf bank_mask:0xf\n"
"v_mov_b32 v57, v56\n"
"s_nop 1\n"
"v_permlane16_swap_b32 v56, v57\n"
"v_add_f32 v56, v56, v57\n"
"v_fmac_f32 v58, 0x3ae38e39, v56\n"
"v_rsq_f32 v56, v58\n"
"s_nop 0\n"
"v_mul_f32 v56, 0x3e0293ee, v56\n"
"v_pk_mul_f32 v[60:61], v[60:61], v[56:57] op_sel_hi:[1,0]\n"
"v_pk_mul_f32 v[62:63], v[62:63], v[56:57] op_sel_hi:[1,0]\n"
"v_pk_mul_f32 v[64:65], v[64:65], v[56:57] op_sel_hi:[1,0]\n"
"v_pk_mul_f32 v[66:67], v[66:67], v[56:57] op_sel_hi:[1,0]\n"
"v_cvt_pk_bf16_f32 v44, v60, v61\n"
"v_cvt_pk_bf16_f32 v45, v62, v63\n"
"v_cvt_pk_bf16_f32 v46, v64, v65\n"
"v_cvt_pk_bf16_f32 v47, v66, v67\n"
"ds_write_b64 v42, v[44:45] offset:0\n"
"ds_write_b64 v42, v[46:47] offset:128\n"
"v_pk_mul_f32 v[68:69], v[68:69], v[56:57] op_sel_hi:[1,0]\n"
"v_pk_mul_f32 v[70:71], v[70:71], v[56:57] op_sel_hi:[1,0]\n"
"v_pk_mul_f32 v[72:73], v[72:73], v[56:57] op_sel_hi:[1,0]\n"
"v_pk_mul_f32 v[74:75], v[74:75], v[56:57] op_sel_hi:[1,0]\n"
"v_cvt_pk_bf16_f32 v48, v68, v69\n"
"v_cvt_pk_bf16_f32 v49, v70, v71\n"
"v_cvt_pk_bf16_f32 v50, v72, v73\n"
"v_cvt_pk_bf16_f32 v51, v74, v75\n"
"ds_write_b64 v42, v[48:49] offset:4096\n"
"ds_write_b64 v42, v[50:51] offset:4224\n"
"v_pk_mul_f32 v[76:77], v[76:77], v[56:57] op_sel_hi:[1,0]\n"
"v_cvt_pk_bf16_f32 v52, v76, v77\n"
"ds_write_b32 v43, v52 offset:8192\n"
"s_waitcnt lgkmcnt(0)\n"
"s_barrier\n"
"ds_read_b128 v[44:47], v41 offset:0\n"
"ds_read_b128 v[48:51], v41 offset:4096\n"
"ds_read_b128 v[52:55], v41 offset:8192\n"
"s_waitcnt lgkmcnt(2)\n"
"buffer_store_dwordx4 v[44:47], v40, s[32:35], 0 offen sc1\n"
"s_waitcnt lgkmcnt(1)\n"
"buffer_store_dwordx4 v[48:51], v40, s[32:35], s54 offen sc1\n"
"s_waitcnt lgkmcnt(0)\n"
"buffer_store_dwordx4 v[52:55], v40, s[32:35], s55 offen sc1\n"
"s_add_u32 m0, s9, 82944\n"
"s_mov_b32 s52, 128\n"
"buffer_load_dwordx4 v2, s[24:27], s52 offen lds\n"
"s_add_u32 m0, s9, 81920\n"
"s_mov_b32 s53, 0\n"
"buffer_load_dwordx4 v2, s[24:27], s53 offen lds\n"
"s_add_u32 m0, s9, 91136\n"
"s_mov_b32 s52, 262272\n"
"buffer_load_dwordx4 v2, s[24:27], s52 offen lds\n"
"s_add_u32 m0, s9, 90112\n"
"s_mov_b32 s53, 262144\n"
"buffer_load_dwordx4 v2, s[24:27], s53 offen lds\n"
"s_waitcnt vmcnt(4)\n"
"s_barrier\n"
"s_cmp_lg_u32 %2, 0\n"
"s_cbranch_scc1 .LDRY\n"
"s_mov_b32 s11, 0x40000\n"
"s_mov_b64 exec, 0xffffffff\n"
"s_mov_b32 exec_hi, 0xff\n"
"buffer_store_dword v20, v18, s[44:47], s84 offen sc1\n"
"s_sleep 12\n"
".LHK_L:\n"
"buffer_load_dword v22, v19, s[44:47], s84 offen sc1\n"
"s_waitcnt vmcnt(0)\n"
"v_cmp_ne_u32 vcc, v22, v20\n"
"s_sub_u32 s11, s11, 1\n"
"s_cbranch_scc1 .LHK_Y\n"
"s_cbranch_vccz .LHK_Y\n"
"s_and_b32 s54, s11, 3\n"
"s_cbranch_scc1 .LHK_S\n"
"buffer_store_dword v20, v18, s[44:47], s84 offen sc1\n"
".LHK_S:\n"
"s_sleep 2\n"
"s_branch .LHK_L\n"
".LDRY:\n"
"s_mov_b64 exec, 0\n"
"s_cmp_eq_u32 %2, 1\n"
"s_cbranch_scc1 .LPAD0\n"
"s_cmp_eq_u32 %2, 2\n"
"s_cbranch_scc1 .LPAD1\n"
"s_branch .LPAD2\n"
".LPAD17:\n"
".LPAD18:\n"
".LPAD19:\n"
".LHK_D:\n"
".LHK_X:\n"
"s_waitcnt vmcnt(0)\n"
".LHK_Y:\n"
"s_mov_b64 exec, -1\n"
"s_barrier\n"
"s_mov_b32 s52, 0\n"
"buffer_load_dwordx4 a[0:3], v1, s[20:23], s52 offen offset:0 sc1\n"
"buffer_load_dwordx4 a[4:7], v1, s[20:23], s52 offen offset:1024 sc1\n"
"buffer_load_dwordx4 a[8:11], v1, s[20:23], s52 offen offset:2048 sc1\n"
"buffer_load_dwordx4 a[12:15], v1, s[20:23], s52 offen offset:3072 sc1\n"
"s_mov_b32 s53, 4096\n"
"buffer_load_dwordx4 a[16:19], v1, s[20:23], s53 offen offset:0 sc1\n"
"buffer_load_dwordx4 a[20:23], v1, s[20:23], s53 offen offset:1024 sc1\n"
"buffer_load_dwordx4 a[24:27], v1, s[20:23], s53 offen offset:2048 sc1\n"
"buffer_load_dwordx4 a[28:31], v1, s[20:23], s53 offen offset:3072 sc1\n"
"ds_read_b64_tr_b16 a[160:161], v7 offset:1024\n"
"ds_read_b64_tr_b16 a[162:163], v7 offset:5120\n"
"ds_read_b64_tr_b16 a[164:165], v8 offset:1024\n"
"ds_read_b64_tr_b16 a[166:167], v8 offset:5120\n"
"ds_read_b64_tr_b16 a[168:169], v5 offset:0\n"
"ds_read_b64_tr_b16 a[170:171], v5 offset:4096\n"
"ds_read_b64_tr_b16 a[172:173], v6 offset:0\n"
"ds_read_b64_tr_b16 a[174:175], v6 offset:4096\n"
"s_waitcnt vmcnt(4) lgkmcnt(4)\n"
"v_mfma_f32_16x16x32_bf16 v[224:227], a[160:163], a[0:3], 0\n"
"v_mfma_f32_16x16x32_bf16 v[240:243], a[164:167], a[0:3], 0\n"
"s_add_u32 m0, s9, 99328\n"
"ds_read_b64_tr_b16 a[176:177], v7 offset:0\n"
"v_mfma_f32_16x16x32_bf16 v[244:247], a[164:167], a[4:7], 0\n"
"s_mov_b32 s52, 589952\n"
"buffer_load_dwordx4 v3, s[28:31], s52 offen lds\n"
"v_mfma_f32_16x16x32_bf16 v[228:231], a[160:163], a[4:7], 0\n"
"ds_read_b64_tr_b16 a[178:179], v7 offset:4096\n"
"v_mfma_f32_16x16x32_bf16 v[232:235], a[160:163], a[8:11], 0\n"
"v_mfma_f32_16x16x32_bf16 v[248:251], a[164:167], a[8:11], 0\n"
"ds_read_b64_tr_b16 a[180:181], v8 offset:0\n"
"v_mfma_f32_16x16x32_bf16 v[252:255], a[164:167], a[12:15], 0\n"
"v_mfma_f32_16x16x32_bf16 v[236:239], a[160:163], a[12:15], 0\n"
"s_waitcnt lgkmcnt(2)\n"
"ds_read_b64_tr_b16 a[182:183], v8 offset:4096\n"
"v_mfma_f32_16x16x32_bf16 a[252:255], a[168:171], a[12:15], 0\n"
"v_mfma_f32_16x16x32_bf16 a[236:239], a[172:175], a[12:15], 0\n"
"s_add_u32 m0, s9, 98304\n"
"ds_read_b64_tr_b16 a[184:185], v5 offset:1024\n"
"v_mfma_f32_16x16x32_bf16 a[232:235], a[172:175], a[8:11], 0\n"
"s_mov_b32 s53, 589824\n"
"buffer_load_dwordx4 v3, s[28:31], s53 offen lds\n"
"v_mfma_f32_16x16x32_bf16 a[248:251], a[168:171], a[8:11], 0\n"
"ds_read_b64_tr_b16 a[186:187], v5 offset:5120\n"
"v_mfma_f32_16x16x32_bf16 a[244:247], a[168:171], a[4:7], 0\n"
"v_mfma_f32_16x16x32_bf16 a[228:231], a[172:175], a[4:7], 0\n"
"s_add_u32 m0, s9, 107520\n"
"ds_read_b64_tr_b16 a[188:189], v6 offset:1024\n"
"v_mfma_f32_16x16x32_bf16 a[224:227], a[172:175], a[0:3], 0\n"
"s_mov_b32 s52, 884864\n"
"buffer_load_dwordx4 v3, s[28:31], s52 offen lds\n"
"v_mfma_f32_16x16x32_bf16 a[240:243], a[168:171], a[0:3], 0\n"
"s_waitcnt lgkmcnt(2)\n"
"ds_read_b64_tr_b16 a[190:191], v6 offset:5120\n"
"v_mfma_f32_16x16x32_bf16 a[144:147], a[176:179], a[0:3], 0\n"
"v_mfma_f32_16x16x32_bf16 v[160:163], a[180:183], a[0:3], 0\n"
"ds_read_b64_tr_b16 a[160:161], v7 offset:9216\n"
"v_mfma_f32_16x16x32_bf16 v[164:167], a[180:183], a[4:7], 0\n"
"v_mfma_f32_16x16x32_bf16 a[148:151], a[176:179], a[4:7], 0\n"
"s_add_u32 m0, s9, 106496\n"
"ds_read_b64_tr_b16 a[162:163], v7 offset:13312\n"
"v_mfma_f32_16x16x32_bf16 a[152:155], a[176:179], a[8:11], 0\n"
"s_mov_b32 s53, 884736\n"
"buffer_load_dwordx4 v3, s[28:31], s53 offen lds\n"
"v_mfma_f32_16x16x32_bf16 v[168:171], a[180:183], a[8:11], 0\n"
"ds_read_b64_tr_b16 a[164:165], v8 offset:9216\n"
"v_mfma_f32_16x16x32_bf16 v[172:175], a[180:183], a[12:15], 0\n"
"v_mfma_f32_16x16x32_bf16 a[156:159], a[176:179], a[12:15], 0\n"
"s_waitcnt lgkmcnt(2)\n"
"ds_read_b64_tr_b16 a[166:167], v8 offset:13312\n"
"v_mfma_f32_16x16x32_bf16 v[188:191], a[184:187], a[12:15], 0\n"
"v_mfma_f32_16x16x32_bf16 v[204:207], a[188:191], a[12:15], 0\n"
"s_add_u32 m0, s49, 114688\n"
"ds_read_b64_tr_b16 a[168:169], v5 offset:8192\n"
"v_mfma_f32_16x16x32_bf16 v[200:203], a[188:191], a[8:11], 0\n"
"s_mov_b32 s52, 589824\n"
"buffer_load_dwordx4 v4, s[28:31], s52 offen lds\n"
"v_mfma_f32_16x16x32_bf16 v[184:187], a[184:187], a[8:11], 0\n"
"ds_read_b64_tr_b16 a[170:171], v5 offset:12288\n"
"v_mfma_f32_16x16x32_bf16 v[180:183], a[184:187], a[4:7], 0\n"
"v_mfma_f32_16x16x32_bf16 v[196:199], a[188:191], a[4:7], 0\n"
"ds_read_b64_tr_b16 a[172:173], v6 offset:8192\n"
"v_mfma_f32_16x16x32_bf16 v[192:195], a[188:191], a[0:3], 0\n"
"v_mfma_f32_16x16x32_bf16 v[176:179], a[184:187], a[0:3], 0\n"
"s_waitcnt vmcnt(5) lgkmcnt(2)\n"
"ds_read_b64_tr_b16 a[174:175], v6 offset:12288\n"
"v_mfma_f32_16x16x32_bf16 v[224:227], a[160:163], a[16:19], v[224:227]\n"
"v_mfma_f32_16x16x32_bf16 v[240:243], a[164:167], a[16:19], v[240:243]\n"
"ds_read_b64_tr_b16 a[176:177], v7 offset:8192\n"
"v_mfma_f32_16x16x32_bf16 v[244:247], a[164:167], a[20:23], v[244:247]\n"
"s_mov_b32 s52, 8192\n"
"buffer_load_dwordx4 a[32:35], v1, s[20:23], s52 offen offset:0 sc1\n"
"v_mfma_f32_16x16x32_bf16 v[228:231], a[160:163], a[20:23], v[228:231]\n"
"ds_read_b64_tr_b16 a[178:179], v7 offset:12288\n"
"v_mfma_f32_16x16x32_bf16 v[232:235], a[160:163], a[24:27], v[232:235]\n"
"v_mfma_f32_16x16x32_bf16 v[248:251], a[164:167], a[24:27], v[248:251]\n"
"ds_read_b64_tr_b16 a[180:181], v8 offset:8192\n"
"v_mfma_f32_16x16x32_bf16 v[252:255], a[164:167], a[28:31], v[252:255]\n"
"buffer_load_dwordx4 a[36:39], v1, s[20:23], s52 offen offset:1024 sc1\n"
"v_mfma_f32_16x16x32_bf16 v[236:239], a[160:163], a[28:31], v[236:239]\n"
"s_waitcnt lgkmcnt(2)\n"
"ds_read_b64_tr_b16 a[182:183], v8 offset:12288\n"
"v_add_u32 v7, 0x18000, v7\n"
"v_mfma_f32_16x16x32_bf16 a[252:255], a[168:171], a[28:31], a[252:255]\n"
"v_mfma_f32_16x16x32_bf16 a[236:239], a[172:175], a[28:31], a[236:239]\n"
"ds_read_b64_tr_b16 a[184:185], v5 offset:9216\n"
"v_add_u32 v8, 0x18000, v8\n"
"v_mfma_f32_16x16x32_bf16 a[232:235], a[172:175], a[24:27], a[232:235]\n"
"v_mfma_f32_16x16x32_bf16 a[248:251], a[168:171], a[24:27], a[248:251]\n"
"ds_read_b64_tr_b16 a[186:187], v5 offset:13312\n"
"v_mfma_f32_16x16x32_bf16 a[244:247], a[168:171], a[20:23], a[244:247]\n"
"buffer_load_dwordx4 a[40:43], v1, s[20:23], s52 offen offset:2048 sc1\n"
"v_mfma_f32_16x16x32_bf16 a[228:231], a[172:175], a[20:23], a[228:231]\n"
"ds_read_b64_tr_b16 a[188:189], v6 offset:9216\n"
"v_mfma_f32_16x16x32_bf16 a[224:227], a[172:175], a[16:19], a[224:227]\n"
"v_mfma_f32_16x16x32_bf16 a[240:243], a[168:171], a[16:19], a[240:243]\n"
"s_waitcnt lgkmcnt(2)\n"
"ds_read_b64_tr_b16 a[190:191], v6 offset:13312\n"
"v_add_u32 v5, 0x18000, v5\n"
"v_mfma_f32_16x16x32_bf16 a[144:147], a[176:179], a[16:19], a[144:147]\n"
"v_mfma_f32_16x16x32_bf16 v[160:163], a[180:183], a[16:19], v[160:163]\n"
"ds_read_b64_tr_b16 a[160:161], v11 offset:16384\n"
"v_add_u32 v6, 0x18000, v6\n"
"v_mfma_f32_16x16x32_bf16 v[164:167], a[180:183], a[20:23], v[164:167]\n"
"buffer_load_dwordx4 a[44:47], v1, s[20:23], s52 offen offset:3072 sc1\n"
"v_mfma_f32_16x16x32_bf16 a[148:151], a[176:179], a[20:23], a[148:151]\n"
"ds_read_b64_tr_b16 a[162:163], v11 offset:17408\n"
"v_mfma_f32_16x16x32_bf16 a[152:155], a[176:179], a[24:27], a[152:155]\n"
"v_mfma_f32_16x16x32_bf16 v[168:171], a[180:183], a[24:27], v[168:171]\n"
"ds_read_b64_tr_b16 a[164:165], v11 offset:18432\n"
"v_mfma_f32_16x16x32_bf16 v[172:175], a[180:183], a[28:31], v[172:175]\n"
"v_mfma_f32_16x16x32_bf16 a[156:159], a[176:179], a[28:31], a[156:159]\n"
"s_waitcnt lgkmcnt(2)\n"
"ds_read_b64_tr_b16 a[166:167], v11 offset:19456\n"
"v_add_u32 v11, 0x18000, v11\n"
"v_mfma_f32_16x16x32_bf16 v[188:191], a[184:187], a[28:31], v[188:191]\n"
"v_mfma_f32_16x16x32_bf16 v[204:207], a[188:191], a[28:31], v[204:207]\n"
"ds_read_b64_tr_b16 a[168:169], v9 offset:49152\n"
"v_mfma_f32_16x16x32_bf16 v[200:203], a[188:191], a[24:27], v[200:203]\n"
"s_mov_b32 s53, 12288\n"
"buffer_load_dwordx4 a[48:51], v1, s[20:23], s53 offen offset:0 sc1\n"
"v_mfma_f32_16x16x32_bf16 v[184:187], a[184:187], a[24:27], v[184:187]\n"
"ds_read_b64_tr_b16 a[170:171], v9 offset:53248\n"
"v_mfma_f32_16x16x32_bf16 v[180:183], a[184:187], a[20:23], v[180:183]\n"
"v_mfma_f32_16x16x32_bf16 v[196:199], a[188:191], a[20:23], v[196:199]\n"
"ds_read_b64_tr_b16 a[172:173], v9 offset:57344\n"
"v_mfma_f32_16x16x32_bf16 v[192:195], a[188:191], a[16:19], v[192:195]\n"
"buffer_load_dwordx4 a[52:55], v1, s[20:23], s53 offen offset:1024 sc1\n"
"v_mfma_f32_16x16x32_bf16 v[176:179], a[184:187], a[16:19], v[176:179]\n"
"s_waitcnt lgkmcnt(2)\n"
"ds_read_b64_tr_b16 a[174:175], v9 offset:61440\n"
"v_mfma_f32_16x16x32_bf16 v[208:211], a[160:163], a[0:3], 0\n"
"v_mfma_f32_16x16x32_bf16 v[208:211], a[164:167], a[16:19], v[208:211]\n"
"ds_read_b64_tr_b16 a[176:177], v9 offset:49160\n"
"v_mfma_f32_16x16x32_bf16 v[212:215], a[164:167], a[20:23], 0\n"
"v_mfma_f32_16x16x32_bf16 v[212:215], a[160:163], a[4:7], v[212:215]\n"
"ds_read_b64_tr_b16 a[178:179], v9 offset:53256\n"
"v_mfma_f32_16x16x32_bf16 v[216:219], a[160:163], a[8:11], 0\n"
"buffer_load_dwordx4 a[56:59], v1, s[20:23], s53 offen offset:2048 sc1\n"
"v_mfma_f32_16x16x32_bf16 v[216:219], a[164:167], a[24:27], v[216:219]\n"
"ds_read_b64_tr_b16 a[180:181], v9 offset:57352\n"
"v_mfma_f32_16x16x32_bf16 v[220:223], a[164:167], a[28:31], 0\n"
"v_mfma_f32_16x16x32_bf16 v[220:223], a[160:163], a[12:15], v[220:223]\n"
"s_waitcnt lgkmcnt(2)\n"
"ds_read_b64_tr_b16 a[182:183], v9 offset:61448\n"
"v_mfma_f32_16x16x32_bf16 v[44:47], a[168:171], a[12:15], 0\n"
"v_mfma_f32_16x16x32_bf16 v[44:47], a[172:175], a[28:31], v[44:47]\n"
"ds_read_b64_tr_b16 a[184:185], v10 offset:49152\n"
"v_mfma_f32_16x16x32_bf16 v[40:43], a[172:175], a[24:27], 0\n"
"buffer_load_dwordx4 a[60:63], v1, s[20:23], s53 offen offset:3072 sc1\n"
"v_mfma_f32_16x16x32_bf16 v[40:43], a[168:171], a[8:11], v[40:43]\n"
"ds_read_b64_tr_b16 a[186:187], v10 offset:53248\n"
"v_mfma_f32_16x16x32_bf16 v[36:39], a[168:171], a[4:7], 0\n"
"v_mfma_f32_16x16x32_bf16 v[36:39], a[172:175], a[20:23], v[36:39]\n"
"ds_read_b64_tr_b16 a[188:189], v10 offset:57344\n"
"v_mfma_f32_16x16x32_bf16 v[32:35], a[172:175], a[16:19], 0\n"
"v_mfma_f32_16x16x32_bf16 v[32:35], a[168:171], a[0:3], v[32:35]\n"
"s_waitcnt lgkmcnt(2)\n"
"ds_read_b64_tr_b16 a[190:191], v10 offset:61440\n"
"v_mfma_f32_16x16x32_bf16 v[48:51], a[176:179], a[0:3], 0\n"
"v_mfma_f32_16x16x32_bf16 v[48:51], a[180:183], a[16:19], v[48:51]\n"
"s_add_u32 m0, s9, 33792\n"
"ds_read_b64_tr_b16 a[160:161], v10 offset:49160\n"
"v_mfma_f32_16x16x32_bf16 v[52:55], a[180:183], a[20:23], 0\n"
"s_mov_b32 s52, 524416\n"
"buffer_load_dwordx4 v2, s[24:27], s52 offen lds\n"
"v_mfma_f32_16x16x32_bf16 v[52:55], a[176:179], a[4:7], v[52:55]\n"
"ds_read_b64_tr_b16 a[162:163], v10 offset:53256\n"
"v_mfma_f32_16x16x32_bf16 v[56:59], a[176:179], a[8:11], 0\n"
"v_mfma_f32_16x16x32_bf16 v[56:59], a[180:183], a[24:27], v[56:59]\n"
"s_add_u32 m0, s9, 32768\n"
"ds_read_b64_tr_b16 a[164:165], v10 offset:57352\n"
"v_mfma_f32_16x16x32_bf16 v[60:63], a[180:183], a[28:31], 0\n"
"s_mov_b32 s53, 524288\n"
"buffer_load_dwordx4 v2, s[24:27], s53 offen lds\n"
"v_mfma_f32_16x16x32_bf16 v[60:63], a[176:179], a[12:15], v[60:63]\n"
"s_waitcnt lgkmcnt(2)\n"
"ds_read_b64_tr_b16 a[166:167], v10 offset:61448\n"
"v_mfma_f32_16x16x32_bf16 v[76:79], a[184:187], a[12:15], 0\n"
"v_mfma_f32_16x16x32_bf16 v[76:79], a[188:191], a[28:31], v[76:79]\n"
"ds_read_b64_tr_b16 a[168:169], v9 offset:50176\n"
"v_mfma_f32_16x16x32_bf16 v[72:75], a[188:191], a[24:27], 0\n"
"v_mfma_f32_16x16x32_bf16 v[72:75], a[184:187], a[8:11], v[72:75]\n"
"s_add_u32 m0, s9, 41984\n"
"ds_read_b64_tr_b16 a[170:171], v9 offset:54272\n"
"v_mfma_f32_16x16x32_bf16 v[68:71], a[184:187], a[4:7], 0\n"
"s_mov_b32 s52, 786560\n"
"buffer_load_dwordx4 v2, s[24:27], s52 offen lds\n"
"v_mfma_f32_16x16x32_bf16 v[68:71], a[188:191], a[20:23], v[68:71]\n"
"ds_read_b64_tr_b16 a[172:173], v9 offset:58368\n"
"v_mfma_f32_16x16x32_bf16 v[64:67], a[188:191], a[16:19], 0\n"
"v_mfma_f32_16x16x32_bf16 v[64:67], a[184:187], a[0:3], v[64:67]\n"
"s_waitcnt lgkmcnt(2)\n"
"ds_read_b64_tr_b16 a[174:175], v9 offset:62464\n"
"v_mfma_f32_16x16x32_bf16 v[80:83], a[160:163], a[0:3], 0\n"
"v_mfma_f32_16x16x32_bf16 v[80:83], a[164:167], a[16:19], v[80:83]\n"
"s_add_u32 m0, s9, 40960\n"
"ds_read_b64_tr_b16 a[176:177], v9 offset:50184\n"
"v_mfma_f32_16x16x32_bf16 v[84:87], a[164:167], a[20:23], 0\n"
"s_mov_b32 s53, 786432\n"
"buffer_load_dwordx4 v2, s[24:27], s53 offen lds\n"
"v_mfma_f32_16x16x32_bf16 v[84:87], a[160:163], a[4:7], v[84:87]\n"
"ds_read_b64_tr_b16 a[178:179], v9 offset:54280\n"
"v_mfma_f32_16x16x32_bf16 v[88:91], a[160:163], a[8:11], 0\n"
"v_mfma_f32_16x16x32_bf16 v[88:91], a[164:167], a[24:27], v[88:91]\n"
"ds_read_b64_tr_b16 a[180:181], v9 offset:58376\n"
"v_mfma_f32_16x16x32_bf16 v[92:95], a[164:167], a[28:31], 0\n"
"v_mfma_f32_16x16x32_bf16 v[92:95], a[160:163], a[12:15], v[92:95]\n"
"s_waitcnt lgkmcnt(2)\n"
"ds_read_b64_tr_b16 a[182:183], v9 offset:62472\n"
"v_mfma_f32_16x16x32_bf16 v[108:111], a[168:171], a[12:15], 0\n"
"v_mfma_f32_16x16x32_bf16 v[108:111], a[172:175], a[28:31], v[108:111]\n"
"s_add_u32 m0, s9, 119808\n"
"ds_read_b64_tr_b16 a[184:185], v10 offset:50176\n"
"v_mfma_f32_16x16x32_bf16 v[104:107], a[172:175], a[24:27], 0\n"
"s_mov_b32 s52, 1179776\n"
"buffer_load_dwordx4 v3, s[28:31], s52 offen lds\n"
"v_mfma_f32_16x16x32_bf16 v[104:107], a[168:171], a[8:11], v[104:107]\n"
"ds_read_b64_tr_b16 a[186:187], v10 offset:54272\n"
"v_mfma_f32_16x16x32_bf16 v[100:103], a[168:171], a[4:7], 0\n"
"v_mfma_f32_16x16x32_bf16 v[100:103], a[172:175], a[20:23], v[100:103]\n"
"ds_read_b64_tr_b16 a[188:189], v10 offset:58368\n"
"v_mfma_f32_16x16x32_bf16 v[96:99], a[172:175], a[16:19], 0\n"
"v_mfma_f32_16x16x32_bf16 v[96:99], a[168:171], a[0:3], v[96:99]\n"
"s_waitcnt lgkmcnt(2)\n"
"ds_read_b64_tr_b16 a[190:191], v10 offset:62464\n"
"v_mfma_f32_16x16x32_bf16 v[112:115], a[176:179], a[0:3], 0\n"
".LPAD0:\n"
"s_cbranch_execz .LPAD3\n"
"v_mfma_f32_16x16x32_bf16 v[112:115], a[180:183], a[16:19], v[112:115]\n"
"s_add_u32 m0, s9, 118784\n"
"ds_read_b64_tr_b16 a[160:161], v10 offset:50184\n"
"v_mfma_f32_16x16x32_bf16 v[116:119], a[180:183], a[20:23], 0\n"
"s_mov_b32 s53, 1179648\n"
"buffer_load_dwordx4 v3, s[28:31], s53 offen lds\n"
"v_mfma_f32_16x16x32_bf16 v[116:119], a[176:179], a[4:7], v[116:119]\n"
"ds_read_b64_tr_b16 a[162:163], v10 offset:54280\n"
"v_mfma_f32_16x16x32_bf16 v[120:123], a[176:179], a[8:11], 0\n"
"v_mfma_f32_16x16x32_bf16 v[120:123], a[180:183], a[24:27], v[120:123]\n"
"s_add_u32 m0, s9, 128000\n"
"ds_read_b64_tr_b16 a[164:165], v10 offset:58376\n"
"v_mfma_f32_16x16x32_bf16 v[124:127], a[180:183], a[28:31], 0\n"
"s_mov_b32 s52, 1474688\n"
"buffer_load_dwordx4 v3, s[28:31], s52 offen lds\n"
"v_mfma_f32_16x16x32_bf16 v[124:127], a[176:179], a[12:15], v[124:127]\n"
"s_waitcnt lgkmcnt(2)\n"
"ds_read_b64_tr_b16 a[166:167], v10 offset:62472\n"
"v_mfma_f32_16x16x32_bf16 v[140:143], a[184:187], a[12:15], 0\n"
"s_waitcnt vmcnt(15)\n"
"s_barrier\n"
"v_mfma_f32_16x16x32_bf16 v[140:143], a[188:191], a[28:31], v[140:143]\n"
"ds_read_b64_tr_b16 a[168:169], v7 offset:1024\n"
"v_mfma_f32_16x16x32_bf16 v[136:139], a[188:191], a[24:27], 0\n"
"v_mfma_f32_16x16x32_bf16 v[136:139], a[184:187], a[8:11], v[136:139]\n"
"s_add_u32 m0, s9, 126976\n"
"ds_read_b64_tr_b16 a[170:171], v7 offset:5120\n"
"v_mfma_f32_16x16x32_bf16 v[132:135], a[184:187], a[4:7], 0\n"
"s_mov_b32 s53, 1474560\n"
"buffer_load_dwordx4 v3, s[28:31], s53 offen lds\n"
"v_mfma_f32_16x16x32_bf16 v[132:135], a[188:191], a[20:23], v[132:135]\n"
"ds_read_b64_tr_b16 a[172:173], v7 offset:9216\n"
"v_mfma_f32_16x16x32_bf16 v[128:131], a[188:191], a[16:19], 0\n"
"v_mfma_f32_16x16x32_bf16 v[128:131], a[184:187], a[0:3], v[128:131]\n"
"s_waitcnt lgkmcnt(2)\n"
"ds_read_b64_tr_b16 a[174:175], v7 offset:13312\n"
"v_mfma_f32_16x16x32_bf16 v[144:147], a[160:163], a[0:3], 0\n"
"v_mfma_f32_16x16x32_bf16 v[144:147], a[164:167], a[16:19], v[144:147]\n"
"s_add_u32 m0, s49, 135168\n"
"ds_read_b64_tr_b16 a[176:177], v8 offset:1024\n"
"v_mfma_f32_16x16x32_bf16 v[148:151], a[164:167], a[20:23], 0\n"
"s_mov_b32 s52, 1179648\n"
"buffer_load_dwordx4 v4, s[28:31], s52 offen lds\n"
"v_mfma_f32_16x16x32_bf16 v[148:151], a[160:163], a[4:7], v[148:151]\n"
"ds_read_b64_tr_b16 a[178:179], v8 offset:5120\n"
"v_mfma_f32_16x16x32_bf16 v[152:155], a[160:163], a[8:11], 0\n"
"v_mfma_f32_16x16x32_bf16 v[152:155], a[164:167], a[24:27], v[152:155]\n"
"ds_read_b64_tr_b16 a[180:181], v8 offset:9216\n"
"v_mfma_f32_16x16x32_bf16 v[156:159], a[164:167], a[28:31], 0\n"
"v_mfma_f32_16x16x32_bf16 v[156:159], a[160:163], a[12:15], v[156:159]\n"
"s_waitcnt vmcnt(9) lgkmcnt(2)\n"
"ds_read_b64_tr_b16 a[182:183], v8 offset:13312\n"
"v_mfma_f32_16x16x32_bf16 v[236:239], a[168:171], a[44:47], v[236:239]\n"
"v_mfma_f32_16x16x32_bf16 v[236:239], a[172:175], a[60:63], v[236:239]\n"
"ds_read_b64_tr_b16 a[184:185], v5 offset:0\n"
"v_mfma_f32_16x16x32_bf16 v[232:235], a[172:175], a[56:59], v[232:235]\n"
"s_mov_b32 s52, 16384\n"
"buffer_load_dwordx4 a[64:67], v1, s[20:23], s52 offen offset:0 sc1\n"
"v_mfma_f32_16x16x32_bf16 v[232:235], a[168:171], a[40:43], v[232:235]\n"
"ds_read_b64_tr_b16 a[186:187], v5 offset:4096\n"
"v_mfma_f32_16x16x32_bf16 v[228:231], a[168:171], a[36:39], v[228:231]\n"
"v_mfma_f32_16x16x32_bf16 v[228:231], a[172:175], a[52:55], v[228:231]\n"
"ds_read_b64_tr_b16 a[188:189], v5 offset:8192\n"
"v_mfma_f32_16x16x32_bf16 v[224:227], a[172:175], a[48:51], v[224:227]\n"
"buffer_load_dwordx4 a[68:71], v1, s[20:23], s52 offen offset:1024 sc1\n"
"v_mfma_f32_16x16x32_bf16 v[224:227], a[168:171], a[32:35], v[224:227]\n"
"s_waitcnt lgkmcnt(2)\n"
"ds_read_b64_tr_b16 a[190:191], v5 offset:12288\n"
"v_mfma_f32_16x16x32_bf16 v[240:243], a[176:179], a[32:35], v[240:243]\n"
"v_mfma_f32_16x16x32_bf16 v[240:243], a[180:183], a[48:51], v[240:243]\n"
"ds_read_b64_tr_b16 a[160:161], v6 offset:0\n"
"v_mfma_f32_16x16x32_bf16 v[244:247], a[180:183], a[52:55], v[244:247]\n"
"v_mfma_f32_16x16x32_bf16 v[244:247], a[176:179], a[36:39], v[244:247]\n"
"ds_read_b64_tr_b16 a[162:163], v6 offset:4096\n"
"v_mfma_f32_16x16x32_bf16 v[248:251], a[176:179], a[40:43], v[248:251]\n"
"buffer_load_dwordx4 a[72:75], v1, s[20:23], s52 offen offset:2048 sc1\n"
"v_mfma_f32_16x16x32_bf16 v[248:251], a[180:183], a[56:59], v[248:251]\n"
"ds_read_b64_tr_b16 a[164:165], v6 offset:8192\n"
"v_mfma_f32_16x16x32_bf16 v[252:255], a[180:183], a[60:63], v[252:255]\n"
"v_mfma_f32_16x16x32_bf16 v[252:255], a[176:179], a[44:47], v[252:255]\n"
"s_waitcnt lgkmcnt(2)\n"
"ds_read_b64_tr_b16 a[166:167], v6 offset:12288\n"
"v_mfma_f32_16x16x32_bf16 a[252:255], a[184:187], a[44:47], a[252:255]\n"
"v_mfma_f32_16x16x32_bf16 a[252:255], a[188:191], a[60:63], a[252:255]\n"
"ds_read_b64_tr_b16 a[168:169], v7 offset:0\n"
"v_mfma_f32_16x16x32_bf16 a[248:251], a[188:191], a[56:59], a[248:251]\n"
"buffer_load_dwordx4 a[76:79], v1, s[20:23], s52 offen offset:3072 sc1\n"
"v_mfma_f32_16x16x32_bf16 a[248:251], a[184:187], a[40:43], a[248:251]\n"
"ds_read_b64_tr_b16 a[170:171], v7 offset:4096\n"
"v_mfma_f32_16x16x32_bf16 a[244:247], a[184:187], a[36:39], a[244:247]\n"
"v_mfma_f32_16x16x32_bf16 a[244:247], a[188:191], a[52:55], a[244:247]\n"
"ds_read_b64_tr_b16 a[172:173], v7 offset:8192\n"
"v_mfma_f32_16x16x32_bf16 a[240:243], a[188:191], a[48:51], a[240:243]\n"
"v_mfma_f32_16x16x32_bf16 a[240:243], a[184:187], a[32:35], a[240:243]\n"
"s_waitcnt lgkmcnt(2)\n"
"ds_read_b64_tr_b16 a[174:175], v7 offset:12288\n"
"v_add_u32 v7, 0x5000, v7\n"
"v_mfma_f32_16x16x32_bf16 a[224:227], a[160:163], a[32:35], a[224:227]\n"
"v_mfma_f32_16x16x32_bf16 a[224:227], a[164:167], a[48:51], a[224:227]\n"
"ds_read_b64_tr_b16 a[176:177], v8 offset:0\n"
"v_mfma_f32_16x16x32_bf16 a[228:231], a[164:167], a[52:55], a[228:231]\n"
"s_mov_b32 s53, 20480\n"
"buffer_load_dwordx4 a[80:83], v1, s[20:23], s53 offen offset:0 sc1\n"
"v_mfma_f32_16x16x32_bf16 a[228:231], a[160:163], a[36:39], a[228:231]\n"
"ds_read_b64_tr_b16 a[178:179], v8 offset:4096\n"
"v_mfma_f32_16x16x32_bf16 a[232:235], a[160:163], a[40:43], a[232:235]\n"
"v_mfma_f32_16x16x32_bf16 a[232:235], a[164:167], a[56:59], a[232:235]\n"
"ds_read_b64_tr_b16 a[180:181], v8 offset:8192\n"
"v_mfma_f32_16x16x32_bf16 a[236:239], a[164:167], a[60:63], a[236:239]\n"
"buffer_load_dwordx4 a[84:87], v1, s[20:23], s53 offen offset:1024 sc1\n"
"v_mfma_f32_16x16x32_bf16 a[236:239], a[160:163], a[44:47], a[236:239]\n"
"s_waitcnt lgkmcnt(2)\n"
"ds_read_b64_tr_b16 a[182:183], v8 offset:12288\n"
"v_add_u32 v8, 0x5000, v8\n"
"v_mfma_f32_16x16x32_bf16 a[156:159], a[168:171], a[44:47], a[156:159]\n"
"v_mfma_f32_16x16x32_bf16 a[156:159], a[172:175], a[60:63], a[156:159]\n"
"ds_read_b64_tr_b16 a[184:185], v5 offset:1024\n"
"v_mfma_f32_16x16x32_bf16 a[152:155], a[172:175], a[56:59], a[152:155]\n"
"v_mfma_f32_16x16x32_bf16 a[152:155], a[168:171], a[40:43], a[152:155]\n"
"ds_read_b64_tr_b16 a[186:187], v5 offset:5120\n"
"v_mfma_f32_16x16x32_bf16 a[148:151], a[168:171], a[36:39], a[148:151]\n"
"buffer_load_dwordx4 a[88:91], v1, s[20:23], s53 offen offset:2048 sc1\n"
"v_mfma_f32_16x16x32_bf16 a[148:151], a[172:175], a[52:55], a[148:151]\n"
"ds_read_b64_tr_b16 a[188:189], v5 offset:9216\n"
"v_mfma_f32_16x16x32_bf16 a[144:147], a[172:175], a[48:51], a[144:147]\n"
"v_mfma_f32_16x16x32_bf16 a[144:147], a[168:171], a[32:35], a[144:147]\n"
"s_waitcnt lgkmcnt(2)\n"
"ds_read_b64_tr_b16 a[190:191], v5 offset:13312\n"
"v_add_u32 v5, 0x5000, v5\n"
"v_mfma_f32_16x16x32_bf16 v[160:163], a[176:179], a[32:35], v[160:163]\n"
"v_mfma_f32_16x16x32_bf16 v[160:163], a[180:183], a[48:51], v[160:163]\n"
"ds_read_b64_tr_b16 a[160:161], v6 offset:1024\n"
"v_mfma_f32_16x16x32_bf16 v[164:167], a[180:183], a[52:55], v[164:167]\n"
"buffer_load_dwordx4 a[92:95], v1, s[20:23], s53 offen offset:3072 sc1\n"
"v_mfma_f32_16x16x32_bf16 v[164:167], a[176:179], a[36:39], v[164:167]\n"
"ds_read_b64_tr_b16 a[162:163], v6 offset:5120\n"
"v_mfma_f32_16x16x32_bf16 v[168:171], a[176:179], a[40:43], v[168:171]\n"
"v_mfma_f32_16x16x32_bf16 v[168:171], a[180:183], a[56:59], v[168:171]\n"
"ds_read_b64_tr_b16 a[164:165], v6 offset:9216\n"
"v_mfma_f32_16x16x32_bf16 v[172:175], a[180:183], a[60:63], v[172:175]\n"
"v_mfma_f32_16x16x32_bf16 v[172:175], a[176:179], a[44:47], v[172:175]\n"
"s_waitcnt lgkmcnt(2)\n"
"ds_read_b64_tr_b16 a[166:167], v6 offset:13312\n"
"v_add_u32 v6, 0x5000, v6\n"
"v_mfma_f32_16x16x32_bf16 v[188:191], a[184:187], a[44:47], v[188:191]\n"
"v_mfma_f32_16x16x32_bf16 v[188:191], a[188:191], a[60:63], v[188:191]\n"
"ds_read_b64_tr_b16 a[168:169], v11 offset:16384\n"
"v_mfma_f32_16x16x32_bf16 v[184:187], a[188:191], a[56:59], v[184:187]\n"
"buffer_store_dword v20, v18, s[44:47], s84 offen sc1\n"
"v_mfma_f32_16x16x32_bf16 v[184:187], a[184:187], a[40:43], v[184:187]\n"
"ds_read_b64_tr_b16 a[170:171], v11 offset:17408\n"
"v_mfma_f32_16x16x32_bf16 v[180:183], a[184:187], a[36:39], v[180:183]\n"
"v_mfma_f32_16x16x32_bf16 v[180:183], a[188:191], a[52:55], v[180:183]\n"
"s_add_u32 m0, s9, 50176\n"
"ds_read_b64_tr_b16 a[172:173], v11 offset:18432\n"
"v_mfma_f32_16x16x32_bf16 v[176:179], a[188:191], a[48:51], v[176:179]\n"
"s_mov_b32 s52, 1048704\n"
"buffer_load_dwordx4 v2, s[24:27], s52 offen lds\n"
"v_mfma_f32_16x16x32_bf16 v[176:179], a[184:187], a[32:35], v[176:179]\n"
"s_waitcnt lgkmcnt(2)\n"
"ds_read_b64_tr_b16 a[174:175], v11 offset:19456\n"
"v_add_u32 v11, 0x5000, v11\n"
"v_mfma_f32_16x16x32_bf16 v[192:195], a[160:163], a[32:35], v[192:195]\n"
"s_waitcnt vmcnt(15)\n"
"s_barrier\n"
"v_mfma_f32_16x16x32_bf16 v[192:195], a[164:167], a[48:51], v[192:195]\n"
"ds_read_b64_tr_b16 a[176:177], v9 offset:0\n"
"v_mfma_f32_16x16x32_bf16 v[196:199], a[164:167], a[52:55], v[196:199]\n"
"v_mfma_f32_16x16x32_bf16 v[196:199], a[160:163], a[36:39], v[196:199]\n"
"s_add_u32 m0, s9, 49152\n"
"ds_read_b64_tr_b16 a[178:179], v9 offset:4096\n"
"v_mfma_f32_16x16x32_bf16 v[200:203], a[160:163], a[40:43], v[200:203]\n"
"s_mov_b32 s53, 1048576\n"
"buffer_load_dwordx4 v2, s[24:27], s53 offen lds\n"
"v_mfma_f32_16x16x32_bf16 v[200:203], a[164:167], a[56:59], v[200:203]\n"
"ds_read_b64_tr_b16 a[180:181], v9 offset:8192\n"
"v_mfma_f32_16x16x32_bf16 v[204:207], a[164:167], a[60:63], v[204:207]\n"
"v_mfma_f32_16x16x32_bf16 v[204:207], a[160:163], a[44:47], v[204:207]\n"
"s_waitcnt lgkmcnt(2)\n"
"ds_read_b64_tr_b16 a[182:183], v9 offset:12288\n"
"v_mfma_f32_16x16x32_bf16 v[220:223], a[168:171], a[44:47], v[220:223]\n"
"v_mfma_f32_16x16x32_bf16 v[220:223], a[172:175], a[60:63], v[220:223]\n"
"s_add_u32 m0, s9, 58368\n"
"ds_read_b64_tr_b16 a[184:185], v9 offset:8\n"
"v_mfma_f32_16x16x32_bf16 v[216:219], a[172:175], a[56:59], v[216:219]\n"
"s_mov_b32 s52, 1310848\n"
"buffer_load_dwordx4 v2, s[24:27], s52 offen lds\n"
"v_mfma_f32_16x16x32_bf16 v[216:219], a[168:171], a[40:43], v[216:219]\n"
"ds_read_b64_tr_b16 a[186:187], v9 offset:4104\n"
"v_mfma_f32_16x16x32_bf16 v[212:215], a[168:171], a[36:39], v[212:215]\n"
"v_mfma_f32_16x16x32_bf16 v[212:215], a[172:175], a[52:55], v[212:215]\n"
"ds_read_b64_tr_b16 a[188:189], v9 offset:8200\n"
"v_mfma_f32_16x16x32_bf16 v[208:211], a[172:175], a[48:51], v[208:211]\n"
"v_mfma_f32_16x16x32_bf16 v[208:211], a[168:171], a[32:35], v[208:211]\n"
"s_waitcnt lgkmcnt(2)\n"
"ds_read_b64_tr_b16 a[190:191], v9 offset:12296\n"
"v_mfma_f32_16x16x32_bf16 v[32:35], a[176:179], a[32:35], v[32:35]\n"
"v_mfma_f32_16x16x32_bf16 v[32:35], a[180:183], a[48:51], v[32:35]\n"
"s_add_u32 m0, s9, 57344\n"
"ds_read_b64_tr_b16 a[160:161], v10 offset:0\n"
"v_mfma_f32_16x16x32_bf16 v[36:39], a[180:183], a[52:55], v[36:39]\n"
"s_mov_b32 s53, 1310720\n"
"buffer_load_dwordx4 v2, s[24:27], s53 offen lds\n"
"v_mfma_f32_16x16x32_bf16 v[36:39], a[176:179], a[36:39], v[36:39]\n"
"ds_read_b64_tr_b16 a[162:163], v10 offset:4096\n"
"v_mfma_f32_16x16x32_bf16 v[40:43], a[176:179], a[40:43], v[40:43]\n"
"v_mfma_f32_16x16x32_bf16 v[40:43], a[180:183], a[56:59], v[40:43]\n"
"s_add_u32 m0, s9, 140288\n"
"ds_read_b64_tr_b16 a[164:165], v10 offset:8192\n"
"v_mfma_f32_16x16x32_bf16 v[44:47], a[180:183], a[60:63], v[44:47]\n"
"s_mov_b32 s52, 1769600\n"
"buffer_load_dwordx4 v3, s[28:31], s52 offen lds\n"
"v_mfma_f32_16x16x32_bf16 v[44:47], a[176:179], a[44:47], v[44:47]\n"
"s_waitcnt lgkmcnt(2)\n"
"ds_read_b64_tr_b16 a[166:167], v10 offset:12288\n"
"v_mfma_f32_16x16x32_bf16 v[60:63], a[184:187], a[44:47], v[60:63]\n"
"v_mfma_f32_16x16x32_bf16 v[60:63], a[188:191], a[60:63], v[60:63]\n"
"ds_read_b64_tr_b16 a[168:169], v10 offset:8\n"
"v_mfma_f32_16x16x32_bf16 v[56:59], a[188:191], a[56:59], v[56:59]\n"
"v_mfma_f32_16x16x32_bf16 v[56:59], a[184:187], a[40:43], v[56:59]\n"
"ds_read_b64_tr_b16 a[170:171], v10 offset:4104\n"
"v_mfma_f32_16x16x32_bf16 v[52:55], a[184:187], a[36:39], v[52:55]\n"
"v_mfma_f32_16x16x32_bf16 v[52:55], a[188:191], a[52:55], v[52:55]\n"
"s_add_u32 m0, s9, 139264\n"
"ds_read_b64_tr_b16 a[172:173], v10 offset:8200\n"
"v_mfma_f32_16x16x32_bf16 v[48:51], a[188:191], a[48:51], v[48:51]\n"
"s_mov_b32 s53, 1769472\n"
"buffer_load_dwordx4 v3, s[28:31], s53 offen lds\n"
"v_mfma_f32_16x16x32_bf16 v[48:51], a[184:187], a[32:35], v[48:51]\n"
"s_waitcnt lgkmcnt(2)\n"
"ds_read_b64_tr_b16 a[174:175], v10 offset:12296\n"
"v_mfma_f32_16x16x32_bf16 v[64:67], a[160:163], a[32:35], v[64:67]\n"
"v_mfma_f32_16x16x32_bf16 v[64:67], a[164:167], a[48:51], v[64:67]\n"
"ds_read_b64_tr_b16 a[176:177], v9 offset:1024\n"
"v_mfma_f32_16x16x32_bf16 v[68:71], a[164:167], a[52:55], v[68:71]\n"
"v_mfma_f32_16x16x32_bf16 v[68:71], a[160:163], a[36:39], v[68:71]\n"
"ds_read_b64_tr_b16 a[178:179], v9 offset:5120\n"
"v_mfma_f32_16x16x32_bf16 v[72:75], a[160:163], a[40:43], v[72:75]\n"
"v_mfma_f32_16x16x32_bf16 v[72:75], a[164:167], a[56:59], v[72:75]\n"
"s_add_u32 m0, s9, 148480\n"
"ds_read_b64_tr_b16 a[180:181], v9 offset:9216\n"
"v_mfma_f32_16x16x32_bf16 v[76:79], a[164:167], a[60:63], v[76:79]\n"
"s_mov_b32 s52, 2064512\n"
"buffer_load_dwordx4 v3, s[28:31], s52 offen lds\n"
"v_mfma_f32_16x16x32_bf16 v[76:79], a[160:163], a[44:47], v[76:79]\n"
"s_waitcnt lgkmcnt(2)\n"
"ds_read_b64_tr_b16 a[182:183], v9 offset:13312\n"
"v_mfma_f32_16x16x32_bf16 v[92:95], a[168:171], a[44:47], v[92:95]\n"
"v_mfma_f32_16x16x32_bf16 v[92:95], a[172:175], a[60:63], v[92:95]\n"
"ds_read_b64_tr_b16 a[184:185], v9 offset:1032\n"
"v_mfma_f32_16x16x32_bf16 v[88:91], a[172:175], a[56:59], v[88:91]\n"
"v_mfma_f32_16x16x32_bf16 v[88:91], a[168:171], a[40:43], v[88:91]\n"
"ds_read_b64_tr_b16 a[186:187], v9 offset:5128\n"
"v_mfma_f32_16x16x32_bf16 v[84:87], a[168:171], a[36:39], v[84:87]\n"
"v_mfma_f32_16x16x32_bf16 v[84:87], a[172:175], a[52:55], v[84:87]\n"
"s_add_u32 m0, s9, 147456\n"
"ds_read_b64_tr_b16 a[188:189], v9 offset:9224\n"
"v_mfma_f32_16x16x32_bf16 v[80:83], a[172:175], a[48:51], v[80:83]\n"
"s_mov_b32 s53, 2064384\n"
"buffer_load_dwordx4 v3, s[28:31], s53 offen lds\n"
"v_mfma_f32_16x16x32_bf16 v[80:83], a[168:171], a[32:35], v[80:83]\n"
"s_waitcnt lgkmcnt(2)\n"
"ds_read_b64_tr_b16 a[190:191], v9 offset:13320\n"
"v_mfma_f32_16x16x32_bf16 v[96:99], a[176:179], a[32:35], v[96:99]\n"
"v_mfma_f32_16x16x32_bf16 v[96:99], a[180:183], a[48:51], v[96:99]\n"
"ds_read_b64_tr_b16 a[160:161], v10 offset:1024\n"
"v_mfma_f32_16x16x32_bf16 v[100:103], a[180:183], a[52:55], v[100:103]\n"
"v_mfma_f32_16x16x32_bf16 v[100:103], a[176:179], a[36:39], v[100:103]\n"
"ds_read_b64_tr_b16 a[162:163], v10 offset:5120\n"
"v_mfma_f32_16x16x32_bf16 v[104:107], a[176:179], a[40:43], v[104:107]\n"
"v_mfma_f32_16x16x32_bf16 v[104:107], a[180:183], a[56:59], v[104:107]\n"
"s_add_u32 m0, s49, 155648\n"
"ds_read_b64_tr_b16 a[164:165], v10 offset:9216\n"
"v_mfma_f32_16x16x32_bf16 v[108:111], a[180:183], a[60:63], v[108:111]\n"
"s_mov_b32 s52, 1769472\n"
"buffer_load_dwordx4 v4, s[28:31], s52 offen lds\n"
"v_mfma_f32_16x16x32_bf16 v[108:111], a[176:179], a[44:47], v[108:111]\n"
"s_waitcnt lgkmcnt(2)\n"
"ds_read_b64_tr_b16 a[166:167], v10 offset:13312\n"
"v_mfma_f32_16x16x32_bf16 v[124:127], a[184:187], a[44:47], v[124:127]\n"
"v_mfma_f32_16x16x32_bf16 v[124:127], a[188:191], a[60:63], v[124:127]\n"
"ds_read_b64_tr_b16 a[168:169], v10 offset:1032\n"
"v_mfma_f32_16x16x32_bf16 v[120:123], a[188:191], a[56:59], v[120:123]\n"
"v_mfma_f32_16x16x32_bf16 v[120:123], a[184:187], a[40:43], v[120:123]\n"
"ds_read_b64_tr_b16 a[170:171], v10 offset:5128\n"
"v_mfma_f32_16x16x32_bf16 v[116:119], a[184:187], a[36:39], v[116:119]\n"
"s_mov_b32 s52, 24576\n"
"buffer_load_dwordx4 a[192:195], v1, s[20:23], s52 offen offset:0 sc1\n"
"v_mfma_f32_16x16x32_bf16 v[116:119], a[188:191], a[52:55], v[116:119]\n"
"ds_read_b64_tr_b16 a[172:173], v10 offset:9224\n"
"v_mfma_f32_16x16x32_bf16 v[112:115], a[188:191], a[48:51], v[112:115]\n"
"v_mfma_f32_16x16x32_bf16 v[112:115], a[184:187], a[32:35], v[112:115]\n"
"s_waitcnt lgkmcnt(2)\n"
"ds_read_b64_tr_b16 a[174:175], v10 offset:13320\n"
"v_mfma_f32_16x16x32_bf16 v[128:131], a[160:163], a[32:35], v[128:131]\n"
"s_waitcnt vmcnt(19)\n"
"s_barrier\n"
"v_mfma_f32_16x16x32_bf16 v[128:131], a[164:167], a[48:51], v[128:131]\n"
"ds_read_b64_tr_b16 a[176:177], v7 offset:1024\n"
"v_mfma_f32_16x16x32_bf16 v[132:135], a[164:167], a[52:55], v[132:135]\n"
"v_mfma_f32_16x16x32_bf16 v[132:135], a[160:163], a[36:39], v[132:135]\n"
"ds_read_b64_tr_b16 a[178:179], v7 offset:5120\n"
"v_mfma_f32_16x16x32_bf16 v[136:139], a[160:163], a[40:43], v[136:139]\n"
"buffer_load_dwordx4 a[196:199], v1, s[20:23], s52 offen offset:1024 sc1\n"
"v_mfma_f32_16x16x32_bf16 v[136:139], a[164:167], a[56:59], v[136:139]\n"
"ds_read_b64_tr_b16 a[180:181], v7 offset:9216\n"
"v_mfma_f32_16x16x32_bf16 v[140:143], a[164:167], a[60:63], v[140:143]\n"
"v_mfma_f32_16x16x32_bf16 v[140:143], a[160:163], a[44:47], v[140:143]\n"
"s_waitcnt lgkmcnt(2)\n"
"ds_read_b64_tr_b16 a[182:183], v7 offset:13312\n"
"v_mfma_f32_16x16x32_bf16 v[156:159], a[168:171], a[44:47], v[156:159]\n"
"v_mfma_f32_16x16x32_bf16 v[156:159], a[172:175], a[60:63], v[156:159]\n"
"ds_read_b64_tr_b16 a[184:185], v8 offset:1024\n"
"v_mfma_f32_16x16x32_bf16 v[152:155], a[172:175], a[56:59], v[152:155]\n"
"buffer_load_dwordx4 a[200:203], v1, s[20:23], s52 offen offset:2048 sc1\n"
"v_mfma_f32_16x16x32_bf16 v[152:155], a[168:171], a[40:43], v[152:155]\n"
"ds_read_b64_tr_b16 a[186:187], v8 offset:5120\n"
"v_mfma_f32_16x16x32_bf16 v[148:151], a[168:171], a[36:39], v[148:151]\n"
"v_mfma_f32_16x16x32_bf16 v[148:151], a[172:175], a[52:55], v[148:151]\n"
"ds_read_b64_tr_b16 a[188:189], v8 offset:9216\n"
"v_mfma_f32_16x16x32_bf16 v[144:147], a[172:175], a[48:51], v[144:147]\n"
"v_mfma_f32_16x16x32_bf16 v[144:147], a[168:171], a[32:35], v[144:147]\n"
"s_waitcnt vmcnt(13) lgkmcnt(2)\n"
"ds_read_b64_tr_b16 a[190:191], v8 offset:13312\n"
"v_mfma_f32_16x16x32_bf16 v[224:227], a[176:179], a[64:67], v[224:227]\n"
"v_lshrrev_b32 v18, 4, v0\n"
"v_and_b32 v19, 15, v0\n"
"v_mfma_f32_16x16x32_bf16 v[224:227], a[180:183], a[80:83], v[224:227]\n"
"s_lshl_b32 s54, s63, 19\n"
"ds_read_b64_tr_b16 a[160:161], v5 offset:0\n"
"v_lshlrev_b32 v20, 8, v18\n"
"v_mfma_f32_16x16x32_bf16 v[228:231], a[180:183], a[84:87], v[228:231]\n"
"buffer_load_dwordx4 a[204:207], v1, s[20:23], s52 offen offset:3072 sc1\n"
"v_mfma_f32_16x16x32_bf16 v[228:231], a[176:179], a[68:71], v[228:231]\n"
"ds_read_b64_tr_b16 a[162:163], v5 offset:4096\n"
"v_add_u32 v20, s48, v20\n"
"v_mfma_f32_16x16x32_bf16 v[232:235], a[176:179], a[72:75], v[232:235]\n"
"s_add_u32 s32, s66, s54\n"
"s_addc_u32 s33, s67, 0\n"
"v_mfma_f32_16x16x32_bf16 v[232:235], a[180:183], a[88:91], v[232:235]\n"
"s_mov_b32 s34, 524288\n"
"ds_read_b64_tr_b16 a[164:165], v5 offset:8192\n"
"v_or_b32 v31, 0, v18\n"
"v_mfma_f32_16x16x32_bf16 v[236:239], a[180:183], a[92:95], v[236:239]\n"
"s_mov_b32 s53, 28672\n"
"buffer_load_dwordx4 a[208:211], v1, s[20:23], s53 offen offset:0 sc1\n"
"v_mfma_f32_16x16x32_bf16 v[236:239], a[176:179], a[76:79], v[236:239]\n"
"s_waitcnt lgkmcnt(2)\n"
"ds_read_b64_tr_b16 a[166:167], v5 offset:12288\n"
"v_xor_b32 v31, v31, v19\n"
"v_mfma_f32_16x16x32_bf16 v[252:255], a[184:187], a[76:79], v[252:255]\n"
"s_mov_b32 s35, 0x20000\n"
"v_lshl_add_u32 v12, v31, 4, v20\n"
"v_or_b32 v31, 4, v18\n"
"v_mfma_f32_16x16x32_bf16 v[252:255], a[188:191], a[92:95], v[252:255]\n"
"ds_read_b64_tr_b16 a[168:169], v6 offset:0\n"
"v_xor_b32 v31, v31, v19\n"
"v_mfma_f32_16x16x32_bf16 v[248:251], a[188:191], a[88:91], v[248:251]\n"
"s_add_u32 s36, s68, s54\n"
"s_addc_u32 s37, s69, 0\n"
"v_mfma_f32_16x16x32_bf16 v[248:251], a[184:187], a[72:75], v[248:251]\n"
"s_mov_b32 s38, 524288\n"
"ds_read_b64_tr_b16 a[170:171], v6 offset:4096\n"
"v_lshl_add_u32 v13, v31, 4, v20\n"
"v_mfma_f32_16x16x32_bf16 v[244:247], a[184:187], a[68:71], v[244:247]\n"
"s_mov_b32 s39, 0x20000\n"
"buffer_load_dwordx4 a[212:215], v1, s[20:23], s53 offen offset:1024 sc1\n"
"v_mfma_f32_16x16x32_bf16 v[244:247], a[188:191], a[84:87], v[244:247]\n"
"s_lshl_b32 s54, s82, 3\n"
"ds_read_b64_tr_b16 a[172:173], v6 offset:8192\n"
"v_or_b32 v31, 8, v18\n"
"v_mfma_f32_16x16x32_bf16 v[240:243], a[188:191], a[80:83], v[240:243]\n"
"s_add_u32 s54, s54, s58\n"
"v_xor_b32 v31, v31, v19\n"
"v_lshl_add_u32 v14, v31, 4, v20\n"
"v_mfma_f32_16x16x32_bf16 v[240:243], a[184:187], a[64:67], v[240:243]\n"
"s_waitcnt lgkmcnt(2)\n"
"ds_read_b64_tr_b16 a[174:175], v6 offset:12288\n"
"v_or_b32 v31, 12, v18\n"
"v_mfma_f32_16x16x32_bf16 a[240:243], a[160:163], a[64:67], a[240:243]\n"
"s_mul_i32 s54, s54, 139264\n"
"v_xor_b32 v31, v31, v19\n"
"v_lshl_add_u32 v15, v31, 4, v20\n"
"v_mfma_f32_16x16x32_bf16 a[240:243], a[164:167], a[80:83], a[240:243]\n"
"s_add_u32 s54, s54, 4194304\n"
"ds_read_b64_tr_b16 a[176:177], v7 offset:0\n"
"v_or_b32 v31, 0, v18\n"
"v_mfma_f32_16x16x32_bf16 a[244:247], a[164:167], a[84:87], a[244:247]\n"
"buffer_load_dwordx4 a[216:219], v1, s[20:23], s53 offen offset:2048 sc1\n"
"v_mfma_f32_16x16x32_bf16 a[244:247], a[160:163], a[68:71], a[244:247]\n"
"ds_read_b64_tr_b16 a[178:179], v7 offset:4096\n"
"v_lshlrev_b32 v31, 1, v31\n"
"v_mfma_f32_16x16x32_bf16 a[248:251], a[160:163], a[72:75], a[248:251]\n"
"s_add_u32 s40, s80, s54\n"
"s_addc_u32 s41, s81, 0\n"
"v_mfma_f32_16x16x32_bf16 a[248:251], a[164:167], a[88:91], a[248:251]\n"
"s_mov_b32 s42, 696320\n"
"ds_read_b64_tr_b16 a[180:181], v7 offset:8192\n"
"v_xor_b32 v31, v31, v19\n"
"v_mfma_f32_16x16x32_bf16 a[252:255], a[164:167], a[92:95], a[252:255]\n"
"s_mov_b32 s43, 0x20000\n"
"v_lshl_add_u32 v16, v31, 4, v20\n"
"v_or_b32 v31, 4, v18\n"
"v_mfma_f32_16x16x32_bf16 a[252:255], a[160:163], a[76:79], a[252:255]\n"
"s_waitcnt lgkmcnt(2)\n"
"ds_read_b64_tr_b16 a[182:183], v7 offset:12288\n"
"v_add_u32 v7, 0x5000, v7\n"
"v_mfma_f32_16x16x32_bf16 a[236:239], a[168:171], a[76:79], a[236:239]\n"
"s_and_b32 s54, s62, 1\n"
"v_lshlrev_b32 v31, 1, v31\n"
"v_xor_b32 v31, v31, v19\n"
"v_mfma_f32_16x16x32_bf16 a[236:239], a[172:175], a[92:95], a[236:239]\n"
"s_mul_i32 s54, s54, 18\n"
"ds_read_b64_tr_b16 a[184:185], v8 offset:0\n"
"v_lshl_add_u32 v17, v31, 4, v20\n"
"v_mfma_f32_16x16x32_bf16 a[232:235], a[172:175], a[88:91], a[232:235]\n"
"s_lshl_b32 s55, s61, 2\n"
"buffer_load_dwordx4 a[220:223], v1, s[20:23], s53 offen offset:3072 sc1\n"
"v_mfma_f32_16x16x32_bf16 a[232:235], a[168:171], a[72:75], a[232:235]\n"
"s_add_u32 s56, s54, s55\n"
"ds_read_b64_tr_b16 a[186:187], v8 offset:4096\n"
"v_xor_b32 v31, v18, v19\n"
"v_mfma_f32_16x16x32_bf16 a[228:231], a[168:171], a[68:71], a[228:231]\n"
"s_lshl_b32 s56, s56, 11\n"
"v_lshlrev_b32 v19, 8, v19\n"
"v_lshl_add_u32 v18, v31, 4, v19\n"
"v_mfma_f32_16x16x32_bf16 a[228:231], a[172:175], a[84:87], a[228:231]\n"
"s_add_u32 m0, s9, 66560\n"
"ds_read_b64_tr_b16 a[188:189], v8 offset:8192\n"
"v_xor_b32 v19, 64, v18\n"
"v_mfma_f32_16x16x32_bf16 a[224:227], a[172:175], a[80:83], a[224:227]\n"
"s_mov_b32 s52, 1572992\n"
"buffer_load_dwordx4 v2, s[24:27], s52 offen lds\n"
"v_mfma_f32_16x16x32_bf16 a[224:227], a[168:171], a[64:67], a[224:227]\n"
"s_waitcnt lgkmcnt(2)\n"
"ds_read_b64_tr_b16 a[190:191], v8 offset:12288\n"
"v_add_u32 v8, 0x5000, v8\n"
"v_mfma_f32_16x16x32_bf16 a[144:147], a[176:179], a[64:67], a[144:147]\n"
"s_lshr_b32 s55, s61, 1\n"
"v_xor_b32 v20, 128, v18\n"
"v_xor_b32 v21, 192, v18\n"
"v_mfma_f32_16x16x32_bf16 a[144:147], a[180:183], a[80:83], a[144:147]\n"
"s_add_u32 s54, s54, s55\n"
"ds_read_b64_tr_b16 a[160:161], v5 offset:1024\n"
"v_lshrrev_b32 v23, 2, v0\n"
"v_mfma_f32_16x16x32_bf16 a[148:151], a[180:183], a[84:87], a[148:151]\n"
"s_add_u32 s54, s54, 16\n"
"v_and_b32 v24, 7, v23\n"
"v_and_b32 v25, 3, v0\n"
"v_mfma_f32_16x16x32_bf16 a[148:151], a[176:179], a[68:71], a[148:151]\n"
"s_lshl_b32 s57, s54, 11\n"
"ds_read_b64_tr_b16 a[162:163], v5 offset:5120\n"
"v_lshlrev_b32 v23, 8, v23\n"
"v_mfma_f32_16x16x32_bf16 a[152:155], a[176:179], a[72:75], a[152:155]\n"
"v_lshl_add_u32 v23, v25, 3, v23\n"
"v_add_u32 v23, 49152, v23\n"
"v_mfma_f32_16x16x32_bf16 a[152:155], a[180:183], a[88:91], a[152:155]\n"
"s_add_u32 m0, s9, 65536\n"
"ds_read_b64_tr_b16 a[164:165], v5 offset:9216\n"
"v_lshl_add_u32 v22, v24, 5, v23\n"
"v_mfma_f32_16x16x32_bf16 a[156:159], a[180:183], a[92:95], a[156:159]\n"
"s_mov_b32 s53, 1572864\n"
"buffer_load_dwordx4 v2, s[24:27], s53 offen lds\n"
"v_mfma_f32_16x16x32_bf16 a[156:159], a[176:179], a[76:79], a[156:159]\n"
"s_waitcnt lgkmcnt(2)\n"
"ds_read_b64_tr_b16 a[166:167], v5 offset:13312\n"
"v_add_u32 v5, 0x5000, v5\n"
"v_mfma_f32_16x16x32_bf16 v[172:175], a[184:187], a[76:79], v[172:175]\n"
"v_xor_b32 v23, 32, v22\n"
"v_xor_b32 v24, 64, v22\n"
"v_mfma_f32_16x16x32_bf16 v[172:175], a[188:191], a[92:95], v[172:175]\n"
"ds_read_b64_tr_b16 a[168:169], v6 offset:1024\n"
"v_xor_b32 v25, 96, v22\n"
"v_mfma_f32_16x16x32_bf16 v[168:171], a[188:191], a[88:91], v[168:171]\n"
"v_xor_b32 v26, 128, v22\n"
"v_xor_b32 v27, 160, v22\n"
"v_mfma_f32_16x16x32_bf16 v[168:171], a[184:187], a[72:75], v[168:171]\n"
"ds_read_b64_tr_b16 a[170:171], v6 offset:5120\n"
"v_xor_b32 v28, 192, v22\n"
"v_mfma_f32_16x16x32_bf16 v[164:167], a[184:187], a[68:71], v[164:167]\n"
"v_xor_b32 v29, 224, v22\n"
"v_mov_b32 v30, s61\n"
"v_mfma_f32_16x16x32_bf16 v[164:167], a[188:191], a[84:87], v[164:167]\n"
"s_add_u32 m0, s9, 74752\n"
"ds_read_b64_tr_b16 a[172:173], v6 offset:9216\n"
"v_and_b32 v30, 1, v30\n"
"v_mfma_f32_16x16x32_bf16 v[160:163], a[188:191], a[80:83], v[160:163]\n"
"s_mov_b32 s52, 1835136\n"
"buffer_load_dwordx4 v2, s[24:27], s52 offen lds\n"
"v_mfma_f32_16x16x32_bf16 v[160:163], a[184:187], a[64:67], v[160:163]\n"
"s_waitcnt lgkmcnt(2)\n"
"ds_read_b64_tr_b16 a[174:175], v6 offset:13312\n"
"v_add_u32 v6, 0x5000, v6\n"
"v_mfma_f32_16x16x32_bf16 v[176:179], a[160:163], a[64:67], v[176:179]\n"
"v_lshl_add_u32 v30, v30, 5, v0\n"
"v_and_b32 v31, 32, v0\n"
"v_mfma_f32_16x16x32_bf16 v[176:179], a[164:167], a[80:83], v[176:179]\n"
"ds_read_b64_tr_b16 a[176:177], v11 offset:16384\n"
"v_add_lshl_u32 v30, v30, v31, 4\n"
"v_mfma_f32_16x16x32_bf16 v[180:183], a[164:167], a[84:87], v[180:183]\n"
"v_mfma_f32_16x16x32_bf16 v[180:183], a[160:163], a[68:71], v[180:183]\n"
"s_add_u32 m0, s9, 73728\n"
"ds_read_b64_tr_b16 a[178:179], v11 offset:17408\n"
"v_mfma_f32_16x16x32_bf16 v[184:187], a[160:163], a[72:75], v[184:187]\n"
"s_mov_b32 s53, 1835008\n"
"buffer_load_dwordx4 v2, s[24:27], s53 offen lds\n"
"v_mfma_f32_16x16x32_bf16 v[184:187], a[164:167], a[88:91], v[184:187]\n"
"ds_read_b64_tr_b16 a[180:181], v11 offset:18432\n"
"v_mfma_f32_16x16x32_bf16 v[188:191], a[164:167], a[92:95], v[188:191]\n"
"v_mfma_f32_16x16x32_bf16 v[188:191], a[160:163], a[76:79], v[188:191]\n"
"s_waitcnt lgkmcnt(2)\n"
"ds_read_b64_tr_b16 a[182:183], v11 offset:19456\n"
"v_add_u32 v11, 0x5000, v11\n"
"v_mfma_f32_16x16x32_bf16 v[204:207], a[168:171], a[76:79], v[204:207]\n"
"s_waitcnt vmcnt(17)\n"
"s_barrier\n"
"v_mfma_f32_16x16x32_bf16 v[204:207], a[172:175], a[92:95], v[204:207]\n"
"s_add_u32 m0, s9, 1024\n"
"ds_read_b64_tr_b16 a[184:185], v9 offset:16384\n"
"v_mfma_f32_16x16x32_bf16 v[200:203], a[172:175], a[88:91], v[200:203]\n"
"s_mov_b32 s52, 2359424\n"
"buffer_load_dwordx4 v3, s[28:31], s52 offen lds\n"
"v_mfma_f32_16x16x32_bf16 v[200:203], a[168:171], a[72:75], v[200:203]\n"
"ds_read_b64_tr_b16 a[186:187], v9 offset:20480\n"
"v_mfma_f32_16x16x32_bf16 v[196:199], a[168:171], a[68:71], v[196:199]\n"
"v_mfma_f32_16x16x32_bf16 v[196:199], a[172:175], a[84:87], v[196:199]\n"
"ds_read_b64_tr_b16 a[188:189], v9 offset:24576\n"
"v_mfma_f32_16x16x32_bf16 v[192:195], a[172:175], a[80:83], v[192:195]\n"
"v_mfma_f32_16x16x32_bf16 v[192:195], a[168:171], a[64:67], v[192:195]\n"
"s_waitcnt lgkmcnt(2)\n"
"ds_read_b64_tr_b16 a[190:191], v9 offset:28672\n"
"v_mfma_f32_16x16x32_bf16 v[208:211], a[176:179], a[64:67], v[208:211]\n"
"v_mfma_f32_16x16x32_bf16 v[208:211], a[180:183], a[80:83], v[208:211]\n"
"s_add_u32 m0, s9, 0\n"
"ds_read_b64_tr_b16 a[160:161], v9 offset:16392\n"
"v_mfma_f32_16x16x32_bf16 v[212:215], a[180:183], a[84:87], v[212:215]\n"
"s_mov_b32 s53, 2359296\n"
"buffer_load_dwordx4 v3, s[28:31], s53 offen lds\n"
"v_mfma_f32_16x16x32_bf16 v[212:215], a[176:179], a[68:71], v[212:215]\n"
"ds_read_b64_tr_b16 a[162:163], v9 offset:20488\n"
"v_mfma_f32_16x16x32_bf16 v[216:219], a[176:179], a[72:75], v[216:219]\n"
"v_mfma_f32_16x16x32_bf16 v[216:219], a[180:183], a[88:91], v[216:219]\n"
"ds_read_b64_tr_b16 a[164:165], v9 offset:24584\n"
"v_mfma_f32_16x16x32_bf16 v[220:223], a[180:183], a[92:95], v[220:223]\n"
"v_mfma_f32_16x16x32_bf16 v[220:223], a[176:179], a[76:79], v[220:223]\n"
"s_waitcnt lgkmcnt(2)\n"
"ds_read_b64_tr_b16 a[166:167], v9 offset:28680\n"
"v_mfma_f32_16x16x32_bf16 v[44:47], a[184:187], a[76:79], v[44:47]\n"
"v_mfma_f32_16x16x32_bf16 v[44:47], a[188:191], a[92:95], v[44:47]\n"
"s_add_u32 m0, s9, 9216\n"
"ds_read_b64_tr_b16 a[168:169], v10 offset:16384\n"
"v_mfma_f32_16x16x32_bf16 v[40:43], a[188:191], a[88:91], v[40:43]\n"
"s_mov_b32 s52, 2654336\n"
"buffer_load_dwordx4 v3, s[28:31], s52 offen lds\n"
"v_mfma_f32_16x16x32_bf16 v[40:43], a[184:187], a[72:75], v[40:43]\n"
".LPAD1:\n"
"s_cbranch_execz .LPAD4\n"
"ds_read_b64_tr_b16 a[170:171], v10 offset:20480\n"
"v_mfma_f32_16x16x32_bf16 v[36:39], a[184:187], a[68:71], v[36:39]\n"
"v_mfma_f32_16x16x32_bf16 v[36:39], a[188:191], a[84:87], v[36:39]\n"
"ds_read_b64_tr_b16 a[172:173], v10 offset:24576\n"
"v_mfma_f32_16x16x32_bf16 v[32:35], a[188:191], a[80:83], v[32:35]\n"
"v_mfma_f32_16x16x32_bf16 v[32:35], a[184:187], a[64:67], v[32:35]\n"
"s_waitcnt lgkmcnt(2)\n"
"ds_read_b64_tr_b16 a[174:175], v10 offset:28672\n"
"v_mfma_f32_16x16x32_bf16 v[48:51], a[160:163], a[64:67], v[48:51]\n"
"v_mfma_f32_16x16x32_bf16 v[48:51], a[164:167], a[80:83], v[48:51]\n"
"s_add_u32 m0, s9, 8192\n"
"ds_read_b64_tr_b16 a[176:177], v10 offset:16392\n"
"v_mfma_f32_16x16x32_bf16 v[52:55], a[164:167], a[84:87], v[52:55]\n"
"s_mov_b32 s53, 2654208\n"
"buffer_load_dwordx4 v3, s[28:31], s53 offen lds\n"
"v_mfma_f32_16x16x32_bf16 v[52:55], a[160:163], a[68:71], v[52:55]\n"
"ds_read_b64_tr_b16 a[178:179], v10 offset:20488\n"
"v_mfma_f32_16x16x32_bf16 v[56:59], a[160:163], a[72:75], v[56:59]\n"
"v_mfma_f32_16x16x32_bf16 v[56:59], a[164:167], a[88:91], v[56:59]\n"
"ds_read_b64_tr_b16 a[180:181], v10 offset:24584\n"
"v_mfma_f32_16x16x32_bf16 v[60:63], a[164:167], a[92:95], v[60:63]\n"
"v_mfma_f32_16x16x32_bf16 v[60:63], a[160:163], a[76:79], v[60:63]\n"
"s_waitcnt lgkmcnt(2)\n"
"ds_read_b64_tr_b16 a[182:183], v10 offset:28680\n"
"v_mfma_f32_16x16x32_bf16 v[76:79], a[168:171], a[76:79], v[76:79]\n"
"v_mfma_f32_16x16x32_bf16 v[76:79], a[172:175], a[92:95], v[76:79]\n"
"s_add_u32 m0, s49, 16384\n"
"ds_read_b64_tr_b16 a[184:185], v9 offset:17408\n"
"v_mfma_f32_16x16x32_bf16 v[72:75], a[172:175], a[88:91], v[72:75]\n"
"s_mov_b32 s52, 2359296\n"
"buffer_load_dwordx4 v4, s[28:31], s52 offen lds\n"
"v_mfma_f32_16x16x32_bf16 v[72:75], a[168:171], a[72:75], v[72:75]\n"
"ds_read_b64_tr_b16 a[186:187], v9 offset:21504\n"
"v_mfma_f32_16x16x32_bf16 v[68:71], a[168:171], a[68:71], v[68:71]\n"
"v_mfma_f32_16x16x32_bf16 v[68:71], a[172:175], a[84:87], v[68:71]\n"
"ds_read_b64_tr_b16 a[188:189], v9 offset:25600\n"
"v_mfma_f32_16x16x32_bf16 v[64:67], a[172:175], a[80:83], v[64:67]\n"
"v_mfma_f32_16x16x32_bf16 v[64:67], a[168:171], a[64:67], v[64:67]\n"
"s_waitcnt lgkmcnt(2)\n"
"ds_read_b64_tr_b16 a[190:191], v9 offset:29696\n"
"v_mfma_f32_16x16x32_bf16 v[80:83], a[176:179], a[64:67], v[80:83]\n"
"v_mfma_f32_16x16x32_bf16 v[80:83], a[180:183], a[80:83], v[80:83]\n"
"ds_read_b64_tr_b16 a[160:161], v9 offset:17416\n"
"v_mfma_f32_16x16x32_bf16 v[84:87], a[180:183], a[84:87], v[84:87]\n"
"s_mov_b32 s52, 32768\n"
"buffer_load_dwordx4 a[96:99], v1, s[20:23], s52 offen offset:0 sc1\n"
"v_mfma_f32_16x16x32_bf16 v[84:87], a[176:179], a[68:71], v[84:87]\n"
"ds_read_b64_tr_b16 a[162:163], v9 offset:21512\n"
"v_mfma_f32_16x16x32_bf16 v[88:91], a[176:179], a[72:75], v[88:91]\n"
"v_mfma_f32_16x16x32_bf16 v[88:91], a[180:183], a[88:91], v[88:91]\n"
"ds_read_b64_tr_b16 a[164:165], v9 offset:25608\n"
"v_mfma_f32_16x16x32_bf16 v[92:95], a[180:183], a[92:95], v[92:95]\n"
"v_mfma_f32_16x16x32_bf16 v[92:95], a[176:179], a[76:79], v[92:95]\n"
"s_waitcnt lgkmcnt(2)\n"
"ds_read_b64_tr_b16 a[166:167], v9 offset:29704\n"
"v_mfma_f32_16x16x32_bf16 v[108:111], a[184:187], a[76:79], v[108:111]\n"
"v_mfma_f32_16x16x32_bf16 v[108:111], a[188:191], a[92:95], v[108:111]\n"
"ds_read_b64_tr_b16 a[168:169], v10 offset:17408\n"
"v_mfma_f32_16x16x32_bf16 v[104:107], a[188:191], a[88:91], v[104:107]\n"
"buffer_load_dwordx4 a[100:103], v1, s[20:23], s52 offen offset:1024 sc1\n"
"v_mfma_f32_16x16x32_bf16 v[104:107], a[184:187], a[72:75], v[104:107]\n"
"ds_read_b64_tr_b16 a[170:171], v10 offset:21504\n"
"v_mfma_f32_16x16x32_bf16 v[100:103], a[184:187], a[68:71], v[100:103]\n"
"v_mfma_f32_16x16x32_bf16 v[100:103], a[188:191], a[84:87], v[100:103]\n"
"ds_read_b64_tr_b16 a[172:173], v10 offset:25600\n"
"v_mfma_f32_16x16x32_bf16 v[96:99], a[188:191], a[80:83], v[96:99]\n"
"v_mfma_f32_16x16x32_bf16 v[96:99], a[184:187], a[64:67], v[96:99]\n"
"s_waitcnt lgkmcnt(2)\n"
"ds_read_b64_tr_b16 a[174:175], v10 offset:29696\n"
"v_mfma_f32_16x16x32_bf16 v[112:115], a[160:163], a[64:67], v[112:115]\n"
"v_mfma_f32_16x16x32_bf16 v[112:115], a[164:167], a[80:83], v[112:115]\n"
"ds_read_b64_tr_b16 a[176:177], v10 offset:17416\n"
"v_mfma_f32_16x16x32_bf16 v[116:119], a[164:167], a[84:87], v[116:119]\n"
"buffer_load_dwordx4 a[104:107], v1, s[20:23], s52 offen offset:2048 sc1\n"
"v_mfma_f32_16x16x32_bf16 v[116:119], a[160:163], a[68:71], v[116:119]\n"
"ds_read_b64_tr_b16 a[178:179], v10 offset:21512\n"
"v_mfma_f32_16x16x32_bf16 v[120:123], a[160:163], a[72:75], v[120:123]\n"
"v_mfma_f32_16x16x32_bf16 v[120:123], a[164:167], a[88:91], v[120:123]\n"
"ds_read_b64_tr_b16 a[180:181], v10 offset:25608\n"
"v_mfma_f32_16x16x32_bf16 v[124:127], a[164:167], a[92:95], v[124:127]\n"
"v_mfma_f32_16x16x32_bf16 v[124:127], a[160:163], a[76:79], v[124:127]\n"
"s_waitcnt lgkmcnt(2)\n"
"ds_read_b64_tr_b16 a[182:183], v10 offset:29704\n"
"v_mfma_f32_16x16x32_bf16 v[140:143], a[168:171], a[76:79], v[140:143]\n"
"s_waitcnt vmcnt(8)\n"
"s_barrier\n"
"v_mfma_f32_16x16x32_bf16 v[140:143], a[172:175], a[92:95], v[140:143]\n"
"ds_read_b64_tr_b16 a[184:185], v7 offset:1024\n"
"v_mfma_f32_16x16x32_bf16 v[136:139], a[172:175], a[88:91], v[136:139]\n"
"buffer_load_dwordx4 a[108:111], v1, s[20:23], s52 offen offset:3072 sc1\n"
"v_mfma_f32_16x16x32_bf16 v[136:139], a[168:171], a[72:75], v[136:139]\n"
"ds_read_b64_tr_b16 a[186:187], v7 offset:5120\n"
"v_mfma_f32_16x16x32_bf16 v[132:135], a[168:171], a[68:71], v[132:135]\n"
"v_mfma_f32_16x16x32_bf16 v[132:135], a[172:175], a[84:87], v[132:135]\n"
"ds_read_b64_tr_b16 a[188:189], v7 offset:9216\n"
"v_mfma_f32_16x16x32_bf16 v[128:131], a[172:175], a[80:83], v[128:131]\n"
"v_mfma_f32_16x16x32_bf16 v[128:131], a[168:171], a[64:67], v[128:131]\n"
"s_waitcnt lgkmcnt(2)\n"
"ds_read_b64_tr_b16 a[190:191], v7 offset:13312\n"
"v_mfma_f32_16x16x32_bf16 v[144:147], a[176:179], a[64:67], v[144:147]\n"
"v_mfma_f32_16x16x32_bf16 v[144:147], a[180:183], a[80:83], v[144:147]\n"
"ds_read_b64_tr_b16 a[160:161], v8 offset:1024\n"
"v_mfma_f32_16x16x32_bf16 v[148:151], a[180:183], a[84:87], v[148:151]\n"
"s_mov_b32 s53, 36864\n"
"buffer_load_dwordx4 a[112:115], v1, s[20:23], s53 offen offset:0 sc1\n"
"v_mfma_f32_16x16x32_bf16 v[148:151], a[176:179], a[68:71], v[148:151]\n"
"ds_read_b64_tr_b16 a[162:163], v8 offset:5120\n"
"v_mfma_f32_16x16x32_bf16 v[152:155], a[176:179], a[72:75], v[152:155]\n"
"v_mfma_f32_16x16x32_bf16 v[152:155], a[180:183], a[88:91], v[152:155]\n"
"ds_read_b64_tr_b16 a[164:165], v8 offset:9216\n"
"v_mfma_f32_16x16x32_bf16 v[156:159], a[180:183], a[92:95], v[156:159]\n"
"buffer_load_dwordx4 a[116:119], v1, s[20:23], s53 offen offset:1024 sc1\n"
"v_mfma_f32_16x16x32_bf16 v[156:159], a[176:179], a[76:79], v[156:159]\n"
"s_waitcnt vmcnt(15) lgkmcnt(2)\n"
"ds_read_b64_tr_b16 a[166:167], v8 offset:13312\n"
"v_mfma_f32_16x16x32_bf16 v[236:239], a[184:187], a[204:207], v[236:239]\n"
"v_mfma_f32_16x16x32_bf16 v[236:239], a[188:191], a[220:223], v[236:239]\n"
"ds_read_b64_tr_b16 a[168:169], v5 offset:0\n"
"v_mfma_f32_16x16x32_bf16 v[232:235], a[188:191], a[216:219], v[232:235]\n"
"v_mfma_f32_16x16x32_bf16 v[232:235], a[184:187], a[200:203], v[232:235]\n"
"ds_read_b64_tr_b16 a[170:171], v5 offset:4096\n"
"v_mfma_f32_16x16x32_bf16 v[228:231], a[184:187], a[196:199], v[228:231]\n"
"buffer_load_dwordx4 a[120:123], v1, s[20:23], s53 offen offset:2048 sc1\n"
"v_mfma_f32_16x16x32_bf16 v[228:231], a[188:191], a[212:215], v[228:231]\n"
"ds_read_b64_tr_b16 a[172:173], v5 offset:8192\n"
"v_mfma_f32_16x16x32_bf16 v[224:227], a[188:191], a[208:211], v[224:227]\n"
"v_mfma_f32_16x16x32_bf16 v[224:227], a[184:187], a[192:195], v[224:227]\n"
"s_waitcnt lgkmcnt(2)\n"
"ds_read_b64_tr_b16 a[174:175], v5 offset:12288\n"
"v_mfma_f32_16x16x32_bf16 v[240:243], a[160:163], a[192:195], v[240:243]\n"
"v_mfma_f32_16x16x32_bf16 v[240:243], a[164:167], a[208:211], v[240:243]\n"
"ds_read_b64_tr_b16 a[176:177], v6 offset:0\n"
"v_mfma_f32_16x16x32_bf16 v[244:247], a[164:167], a[212:215], v[244:247]\n"
"buffer_load_dwordx4 a[124:127], v1, s[20:23], s53 offen offset:3072 sc1\n"
"v_mfma_f32_16x16x32_bf16 v[244:247], a[160:163], a[196:199], v[244:247]\n"
"ds_read_b64_tr_b16 a[178:179], v6 offset:4096\n"
"v_mfma_f32_16x16x32_bf16 v[248:251], a[160:163], a[200:203], v[248:251]\n"
"v_mfma_f32_16x16x32_bf16 v[248:251], a[164:167], a[216:219], v[248:251]\n"
"ds_read_b64_tr_b16 a[180:181], v6 offset:8192\n"
"v_mfma_f32_16x16x32_bf16 v[252:255], a[164:167], a[220:223], v[252:255]\n"
"v_mfma_f32_16x16x32_bf16 v[252:255], a[160:163], a[204:207], v[252:255]\n"
"s_waitcnt lgkmcnt(2)\n"
"ds_read_b64_tr_b16 a[182:183], v6 offset:12288\n"
"v_mfma_f32_16x16x32_bf16 a[252:255], a[168:171], a[204:207], a[252:255]\n"
"v_mfma_f32_16x16x32_bf16 a[252:255], a[172:175], a[220:223], a[252:255]\n"
"s_add_u32 m0, s9, 99328\n"
"ds_read_b64_tr_b16 a[184:185], v7 offset:0\n"
"v_mfma_f32_16x16x32_bf16 a[248:251], a[172:175], a[216:219], a[248:251]\n"
"s_mov_b32 s52, 2949248\n"
"buffer_load_dwordx4 v3, s[28:31], s52 offen lds\n"
"v_mfma_f32_16x16x32_bf16 a[248:251], a[168:171], a[200:203], a[248:251]\n"
"ds_read_b64_tr_b16 a[186:187], v7 offset:4096\n"
"v_mfma_f32_16x16x32_bf16 a[244:247], a[168:171], a[196:199], a[244:247]\n"
"v_mfma_f32_16x16x32_bf16 a[244:247], a[172:175], a[212:215], a[244:247]\n"
"ds_read_b64_tr_b16 a[188:189], v7 offset:8192\n"
"v_mfma_f32_16x16x32_bf16 a[240:243], a[172:175], a[208:211], a[240:243]\n"
"v_mfma_f32_16x16x32_bf16 a[240:243], a[168:171], a[192:195], a[240:243]\n"
"s_waitcnt lgkmcnt(2)\n"
"ds_read_b64_tr_b16 a[190:191], v7 offset:12288\n"
"v_add_u32 v7, 0xfffde000, v7\n"
"v_mfma_f32_16x16x32_bf16 a[224:227], a[176:179], a[192:195], a[224:227]\n"
"v_mfma_f32_16x16x32_bf16 a[224:227], a[180:183], a[208:211], a[224:227]\n"
"s_add_u32 m0, s9, 98304\n"
"ds_read_b64_tr_b16 a[160:161], v8 offset:0\n"
"v_mfma_f32_16x16x32_bf16 a[228:231], a[180:183], a[212:215], a[228:231]\n"
"s_mov_b32 s53, 2949120\n"
"buffer_load_dwordx4 v3, s[28:31], s53 offen lds\n"
"v_mfma_f32_16x16x32_bf16 a[228:231], a[176:179], a[196:199], a[228:231]\n"
"ds_read_b64_tr_b16 a[162:163], v8 offset:4096\n"
"v_mfma_f32_16x16x32_bf16 a[232:235], a[176:179], a[200:203], a[232:235]\n"
"v_mfma_f32_16x16x32_bf16 a[232:235], a[180:183], a[216:219], a[232:235]\n"
"ds_read_b64_tr_b16 a[164:165], v8 offset:8192\n"
"v_mfma_f32_16x16x32_bf16 a[236:239], a[180:183], a[220:223], a[236:239]\n"
"v_mfma_f32_16x16x32_bf16 a[236:239], a[176:179], a[204:207], a[236:239]\n"
"s_waitcnt lgkmcnt(2)\n"
"ds_read_b64_tr_b16 a[166:167], v8 offset:12288\n"
"v_add_u32 v8, 0xfffde000, v8\n"
"v_mfma_f32_16x16x32_bf16 a[156:159], a[184:187], a[204:207], a[156:159]\n"
"v_mfma_f32_16x16x32_bf16 a[156:159], a[188:191], a[220:223], a[156:159]\n"
"s_add_u32 m0, s9, 107520\n"
"ds_read_b64_tr_b16 a[168:169], v5 offset:1024\n"
"v_mfma_f32_16x16x32_bf16 a[152:155], a[188:191], a[216:219], a[152:155]\n"
"s_mov_b32 s52, 3244160\n"
"buffer_load_dwordx4 v3, s[28:31], s52 offen lds\n"
"v_mfma_f32_16x16x32_bf16 a[152:155], a[184:187], a[200:203], a[152:155]\n"
"ds_read_b64_tr_b16 a[170:171], v5 offset:5120\n"
"v_mfma_f32_16x16x32_bf16 a[148:151], a[184:187], a[196:199], a[148:151]\n"
"v_mfma_f32_16x16x32_bf16 a[148:151], a[188:191], a[212:215], a[148:151]\n"
"ds_read_b64_tr_b16 a[172:173], v5 offset:9216\n"
"v_mfma_f32_16x16x32_bf16 a[144:147], a[188:191], a[208:211], a[144:147]\n"
"v_mfma_f32_16x16x32_bf16 a[144:147], a[184:187], a[192:195], a[144:147]\n"
"s_waitcnt lgkmcnt(2)\n"
"ds_read_b64_tr_b16 a[174:175], v5 offset:13312\n"
"v_add_u32 v5, 0xfffde000, v5\n"
"v_mfma_f32_16x16x32_bf16 v[160:163], a[160:163], a[192:195], v[160:163]\n"
"v_mfma_f32_16x16x32_bf16 v[160:163], a[164:167], a[208:211], v[160:163]\n"
"s_add_u32 m0, s9, 106496\n"
"ds_read_b64_tr_b16 a[176:177], v6 offset:1024\n"
"v_mfma_f32_16x16x32_bf16 v[164:167], a[164:167], a[212:215], v[164:167]\n"
"s_mov_b32 s53, 3244032\n"
"buffer_load_dwordx4 v3, s[28:31], s53 offen lds\n"
"v_mfma_f32_16x16x32_bf16 v[164:167], a[160:163], a[196:199], v[164:167]\n"
"ds_read_b64_tr_b16 a[178:179], v6 offset:5120\n"
"v_mfma_f32_16x16x32_bf16 v[168:171], a[160:163], a[200:203], v[168:171]\n"
"v_mfma_f32_16x16x32_bf16 v[168:171], a[164:167], a[216:219], v[168:171]\n"
"ds_read_b64_tr_b16 a[180:181], v6 offset:9216\n"
"v_mfma_f32_16x16x32_bf16 v[172:175], a[164:167], a[220:223], v[172:175]\n"
"v_mfma_f32_16x16x32_bf16 v[172:175], a[160:163], a[204:207], v[172:175]\n"
"s_waitcnt lgkmcnt(2)\n"
"ds_read_b64_tr_b16 a[182:183], v6 offset:13312\n"
"v_add_u32 v6, 0xfffde000, v6\n"
"v_mfma_f32_16x16x32_bf16 v[188:191], a[168:171], a[204:207], v[188:191]\n"
"v_mfma_f32_16x16x32_bf16 v[188:191], a[172:175], a[220:223], v[188:191]\n"
"s_add_u32 m0, s49, 114688\n"
"ds_read_b64_tr_b16 a[184:185], v11 offset:16384\n"
"v_mfma_f32_16x16x32_bf16 v[184:187], a[172:175], a[216:219], v[184:187]\n"
"s_mov_b32 s52, 2949120\n"
"buffer_load_dwordx4 v4, s[28:31], s52 offen lds\n"
"v_mfma_f32_16x16x32_bf16 v[184:187], a[168:171], a[200:203], v[184:187]\n"
"ds_read_b64_tr_b16 a[186:187], v11 offset:17408\n"
"v_mfma_f32_16x16x32_bf16 v[180:183], a[168:171], a[196:199], v[180:183]\n"
"v_mfma_f32_16x16x32_bf16 v[180:183], a[172:175], a[212:215], v[180:183]\n"
"ds_read_b64_tr_b16 a[188:189], v11 offset:18432\n"
"v_mfma_f32_16x16x32_bf16 v[176:179], a[172:175], a[208:211], v[176:179]\n"
"s_mov_b32 s52, 40960\n"
"buffer_load_dwordx4 a[0:3], v1, s[20:23], s52 offen offset:0 sc1\n"
"v_mfma_f32_16x16x32_bf16 v[176:179], a[168:171], a[192:195], v[176:179]\n"
"s_waitcnt lgkmcnt(2)\n"
"ds_read_b64_tr_b16 a[190:191], v11 offset:19456\n"
"v_add_u32 v11, 0xfffde000, v11\n"
"v_mfma_f32_16x16x32_bf16 v[192:195], a[176:179], a[192:195], v[192:195]\n"
"v_mfma_f32_16x16x32_bf16 v[192:195], a[180:183], a[208:211], v[192:195]\n"
"ds_read_b64_tr_b16 a[160:161], v9 offset:32768\n"
"v_mfma_f32_16x16x32_bf16 v[196:199], a[180:183], a[212:215], v[196:199]\n"
"v_mfma_f32_16x16x32_bf16 v[196:199], a[176:179], a[196:199], v[196:199]\n"
"ds_read_b64_tr_b16 a[162:163], v9 offset:36864\n"
"v_mfma_f32_16x16x32_bf16 v[200:203], a[176:179], a[200:203], v[200:203]\n"
"v_mfma_f32_16x16x32_bf16 v[200:203], a[180:183], a[216:219], v[200:203]\n"
"ds_read_b64_tr_b16 a[164:165], v9 offset:40960\n"
"v_mfma_f32_16x16x32_bf16 v[204:207], a[180:183], a[220:223], v[204:207]\n"
"buffer_load_dwordx4 a[4:7], v1, s[20:23], s52 offen offset:1024 sc1\n"
"v_mfma_f32_16x16x32_bf16 v[204:207], a[176:179], a[204:207], v[204:207]\n"
"s_waitcnt lgkmcnt(2)\n"
"ds_read_b64_tr_b16 a[166:167], v9 offset:45056\n"
"v_mfma_f32_16x16x32_bf16 v[220:223], a[184:187], a[204:207], v[220:223]\n"
"v_mfma_f32_16x16x32_bf16 v[220:223], a[188:191], a[220:223], v[220:223]\n"
"ds_read_b64_tr_b16 a[168:169], v9 offset:32776\n"
"v_mfma_f32_16x16x32_bf16 v[216:219], a[188:191], a[216:219], v[216:219]\n"
"v_mfma_f32_16x16x32_bf16 v[216:219], a[184:187], a[200:203], v[216:219]\n"
"ds_read_b64_tr_b16 a[170:171], v9 offset:36872\n"
"v_mfma_f32_16x16x32_bf16 v[212:215], a[184:187], a[196:199], v[212:215]\n"
"buffer_load_dwordx4 a[8:11], v1, s[20:23], s52 offen offset:2048 sc1\n"
"v_mfma_f32_16x16x32_bf16 v[212:215], a[188:191], a[212:215], v[212:215]\n"
"ds_read_b64_tr_b16 a[172:173], v9 offset:40968\n"
"v_mfma_f32_16x16x32_bf16 v[208:211], a[188:191], a[208:211], v[208:211]\n"
"v_mfma_f32_16x16x32_bf16 v[208:211], a[184:187], a[192:195], v[208:211]\n"
"s_waitcnt lgkmcnt(2)\n"
"ds_read_b64_tr_b16 a[174:175], v9 offset:45064\n"
"v_mfma_f32_16x16x32_bf16 v[32:35], a[160:163], a[192:195], v[32:35]\n"
"v_mfma_f32_16x16x32_bf16 v[32:35], a[164:167], a[208:211], v[32:35]\n"
"ds_read_b64_tr_b16 a[176:177], v10 offset:32768\n"
"v_mfma_f32_16x16x32_bf16 v[36:39], a[164:167], a[212:215], v[36:39]\n"
"buffer_load_dwordx4 a[12:15], v1, s[20:23], s52 offen offset:3072 sc1\n"
"v_mfma_f32_16x16x32_bf16 v[36:39], a[160:163], a[196:199], v[36:39]\n"
"ds_read_b64_tr_b16 a[178:179], v10 offset:36864\n"
"v_mfma_f32_16x16x32_bf16 v[40:43], a[160:163], a[200:203], v[40:43]\n"
"v_mfma_f32_16x16x32_bf16 v[40:43], a[164:167], a[216:219], v[40:43]\n"
"ds_read_b64_tr_b16 a[180:181], v10 offset:40960\n"
"v_mfma_f32_16x16x32_bf16 v[44:47], a[164:167], a[220:223], v[44:47]\n"
"v_mfma_f32_16x16x32_bf16 v[44:47], a[160:163], a[204:207], v[44:47]\n"
"s_waitcnt lgkmcnt(2)\n"
"ds_read_b64_tr_b16 a[182:183], v10 offset:45056\n"
"v_mfma_f32_16x16x32_bf16 v[60:63], a[168:171], a[204:207], v[60:63]\n"
"v_mfma_f32_16x16x32_bf16 v[60:63], a[172:175], a[220:223], v[60:63]\n"
"ds_read_b64_tr_b16 a[184:185], v10 offset:32776\n"
"v_mfma_f32_16x16x32_bf16 v[56:59], a[172:175], a[216:219], v[56:59]\n"
"s_mov_b32 s53, 45056\n"
"buffer_load_dwordx4 a[16:19], v1, s[20:23], s53 offen offset:0 sc1\n"
"v_mfma_f32_16x16x32_bf16 v[56:59], a[168:171], a[200:203], v[56:59]\n"
"ds_read_b64_tr_b16 a[186:187], v10 offset:36872\n"
"v_mfma_f32_16x16x32_bf16 v[52:55], a[168:171], a[196:199], v[52:55]\n"
"v_mfma_f32_16x16x32_bf16 v[52:55], a[172:175], a[212:215], v[52:55]\n"
"ds_read_b64_tr_b16 a[188:189], v10 offset:40968\n"
"v_mfma_f32_16x16x32_bf16 v[48:51], a[172:175], a[208:211], v[48:51]\n"
"buffer_load_dwordx4 a[20:23], v1, s[20:23], s53 offen offset:1024 sc1\n"
"v_mfma_f32_16x16x32_bf16 v[48:51], a[168:171], a[192:195], v[48:51]\n"
"s_waitcnt lgkmcnt(2)\n"
"ds_read_b64_tr_b16 a[190:191], v10 offset:45064\n"
"v_mfma_f32_16x16x32_bf16 v[64:67], a[176:179], a[192:195], v[64:67]\n"
"v_mfma_f32_16x16x32_bf16 v[64:67], a[180:183], a[208:211], v[64:67]\n"
"ds_read_b64_tr_b16 a[160:161], v9 offset:33792\n"
"v_mfma_f32_16x16x32_bf16 v[68:71], a[180:183], a[212:215], v[68:71]\n"
"v_mfma_f32_16x16x32_bf16 v[68:71], a[176:179], a[196:199], v[68:71]\n"
"ds_read_b64_tr_b16 a[162:163], v9 offset:37888\n"
"v_mfma_f32_16x16x32_bf16 v[72:75], a[176:179], a[200:203], v[72:75]\n"
"buffer_load_dwordx4 a[24:27], v1, s[20:23], s53 offen offset:2048 sc1\n"
"v_mfma_f32_16x16x32_bf16 v[72:75], a[180:183], a[216:219], v[72:75]\n"
"ds_read_b64_tr_b16 a[164:165], v9 offset:41984\n"
"v_mfma_f32_16x16x32_bf16 v[76:79], a[180:183], a[220:223], v[76:79]\n"
"v_mfma_f32_16x16x32_bf16 v[76:79], a[176:179], a[204:207], v[76:79]\n"
"s_waitcnt lgkmcnt(2)\n"
"ds_read_b64_tr_b16 a[166:167], v9 offset:46080\n"
"v_mfma_f32_16x16x32_bf16 v[92:95], a[184:187], a[204:207], v[92:95]\n"
"v_mfma_f32_16x16x32_bf16 v[92:95], a[188:191], a[220:223], v[92:95]\n"
"ds_read_b64_tr_b16 a[168:169], v9 offset:33800\n"
"v_mfma_f32_16x16x32_bf16 v[88:91], a[188:191], a[216:219], v[88:91]\n"
"buffer_load_dwordx4 a[28:31], v1, s[20:23], s53 offen offset:3072 sc1\n"
"v_mfma_f32_16x16x32_bf16 v[88:91], a[184:187], a[200:203], v[88:91]\n"
"ds_read_b64_tr_b16 a[170:171], v9 offset:37896\n"
"v_mfma_f32_16x16x32_bf16 v[84:87], a[184:187], a[196:199], v[84:87]\n"
"v_mfma_f32_16x16x32_bf16 v[84:87], a[188:191], a[212:215], v[84:87]\n"
"ds_read_b64_tr_b16 a[172:173], v9 offset:41992\n"
"v_mfma_f32_16x16x32_bf16 v[80:83], a[188:191], a[208:211], v[80:83]\n"
"v_mfma_f32_16x16x32_bf16 v[80:83], a[184:187], a[192:195], v[80:83]\n"
"s_waitcnt lgkmcnt(2)\n"
"ds_read_b64_tr_b16 a[174:175], v9 offset:46088\n"
"v_mfma_f32_16x16x32_bf16 v[96:99], a[160:163], a[192:195], v[96:99]\n"
"v_mfma_f32_16x16x32_bf16 v[96:99], a[164:167], a[208:211], v[96:99]\n"
"s_add_u32 m0, s9, 82944\n"
"ds_read_b64_tr_b16 a[176:177], v10 offset:33792\n"
"v_mfma_f32_16x16x32_bf16 v[100:103], a[164:167], a[212:215], v[100:103]\n"
"s_mov_b32 s52, 2097280\n"
"buffer_load_dwordx4 v2, s[24:27], s52 offen lds\n"
"v_mfma_f32_16x16x32_bf16 v[100:103], a[160:163], a[196:199], v[100:103]\n"
"ds_read_b64_tr_b16 a[178:179], v10 offset:37888\n"
"v_mfma_f32_16x16x32_bf16 v[104:107], a[160:163], a[200:203], v[104:107]\n"
"v_mfma_f32_16x16x32_bf16 v[104:107], a[164:167], a[216:219], v[104:107]\n"
"ds_read_b64_tr_b16 a[180:181], v10 offset:41984\n"
"v_mfma_f32_16x16x32_bf16 v[108:111], a[164:167], a[220:223], v[108:111]\n"
"v_mfma_f32_16x16x32_bf16 v[108:111], a[160:163], a[204:207], v[108:111]\n"
"s_waitcnt lgkmcnt(2)\n"
"ds_read_b64_tr_b16 a[182:183], v10 offset:46080\n"
"v_mfma_f32_16x16x32_bf16 v[124:127], a[168:171], a[204:207], v[124:127]\n"
"v_mfma_f32_16x16x32_bf16 v[124:127], a[172:175], a[220:223], v[124:127]\n"
"s_add_u32 m0, s9, 81920\n"
"ds_read_b64_tr_b16 a[184:185], v10 offset:33800\n"
"v_mfma_f32_16x16x32_bf16 v[120:123], a[172:175], a[216:219], v[120:123]\n"
"s_mov_b32 s53, 2097152\n"
"buffer_load_dwordx4 v2, s[24:27], s53 offen lds\n"
"v_mfma_f32_16x16x32_bf16 v[120:123], a[168:171], a[200:203], v[120:123]\n"
"ds_read_b64_tr_b16 a[186:187], v10 offset:37896\n"
"v_mfma_f32_16x16x32_bf16 v[116:119], a[168:171], a[196:199], v[116:119]\n"
"v_mfma_f32_16x16x32_bf16 v[116:119], a[172:175], a[212:215], v[116:119]\n"
"ds_read_b64_tr_b16 a[188:189], v10 offset:41992\n"
"v_mfma_f32_16x16x32_bf16 v[112:115], a[172:175], a[208:211], v[112:115]\n"
"v_mfma_f32_16x16x32_bf16 v[112:115], a[168:171], a[192:195], v[112:115]\n"
"s_waitcnt lgkmcnt(2)\n"
"ds_read_b64_tr_b16 a[190:191], v10 offset:46088\n"
"v_mfma_f32_16x16x32_bf16 v[128:131], a[176:179], a[192:195], v[128:131]\n"
"s_waitcnt vmcnt(23)\n"
"s_barrier\n"
"v_mfma_f32_16x16x32_bf16 v[128:131], a[180:183], a[208:211], v[128:131]\n"
"s_add_u32 m0, s9, 91136\n"
"ds_read_b64_tr_b16 a[160:161], v7 offset:1024\n"
"v_mfma_f32_16x16x32_bf16 v[132:135], a[180:183], a[212:215], v[132:135]\n"
"s_mov_b32 s52, 2359424\n"
"buffer_load_dwordx4 v2, s[24:27], s52 offen lds\n"
"v_mfma_f32_16x16x32_bf16 v[132:135], a[176:179], a[196:199], v[132:135]\n"
"ds_read_b64_tr_b16 a[162:163], v7 offset:5120\n"
"v_mfma_f32_16x16x32_bf16 v[136:139], a[176:179], a[200:203], v[136:139]\n"
"v_mfma_f32_16x16x32_bf16 v[136:139], a[180:183], a[216:219], v[136:139]\n"
"s_add_u32 m0, s9, 90112\n"
"ds_read_b64_tr_b16 a[164:165], v7 offset:9216\n"
"v_mfma_f32_16x16x32_bf16 v[140:143], a[180:183], a[220:223], v[140:143]\n"
"s_mov_b32 s53, 2359296\n"
"buffer_load_dwordx4 v2, s[24:27], s53 offen lds\n"
"v_mfma_f32_16x16x32_bf16 v[140:143], a[176:179], a[204:207], v[140:143]\n"
"s_waitcnt lgkmcnt(2)\n"
"ds_read_b64_tr_b16 a[166:167], v7 offset:13312\n"
"v_mfma_f32_16x16x32_bf16 v[156:159], a[184:187], a[204:207], v[156:159]\n"
"v_mfma_f32_16x16x32_bf16 v[156:159], a[188:191], a[220:223], v[156:159]\n"
"ds_read_b64_tr_b16 a[168:169], v8 offset:1024\n"
"v_mfma_f32_16x16x32_bf16 v[152:155], a[188:191], a[216:219], v[152:155]\n"
"v_mfma_f32_16x16x32_bf16 v[152:155], a[184:187], a[200:203], v[152:155]\n"
"s_add_u32 m0, s9, 119808\n"
"ds_read_b64_tr_b16 a[170:171], v8 offset:5120\n"
"v_mfma_f32_16x16x32_bf16 v[148:151], a[184:187], a[196:199], v[148:151]\n"
"s_mov_b32 s52, 3539072\n"
"buffer_load_dwordx4 v3, s[28:31], s52 offen lds\n"
"v_mfma_f32_16x16x32_bf16 v[148:151], a[188:191], a[212:215], v[148:151]\n"
"ds_read_b64_tr_b16 a[172:173], v8 offset:9216\n"
"v_mfma_f32_16x16x32_bf16 v[144:147], a[188:191], a[208:211], v[144:147]\n"
"v_mfma_f32_16x16x32_bf16 v[144:147], a[184:187], a[192:195], v[144:147]\n"
"s_waitcnt vmcnt(18) lgkmcnt(2)\n"
"ds_read_b64_tr_b16 a[174:175], v8 offset:13312\n"
"v_mfma_f32_16x16x32_bf16 v[224:227], a[160:163], a[96:99], v[224:227]\n"
"v_mfma_f32_16x16x32_bf16 v[224:227], a[164:167], a[112:115], v[224:227]\n"
"ds_read_b64_tr_b16 a[176:177], v5 offset:0\n"
"v_mfma_f32_16x16x32_bf16 v[228:231], a[164:167], a[116:119], v[228:231]\n"
"v_mfma_f32_16x16x32_bf16 v[228:231], a[160:163], a[100:103], v[228:231]\n"
"s_add_u32 m0, s9, 118784\n"
"ds_read_b64_tr_b16 a[178:179], v5 offset:4096\n"
"v_mfma_f32_16x16x32_bf16 v[232:235], a[160:163], a[104:107], v[232:235]\n"
"s_mov_b32 s53, 3538944\n"
"buffer_load_dwordx4 v3, s[28:31], s53 offen lds\n"
"v_mfma_f32_16x16x32_bf16 v[232:235], a[164:167], a[120:123], v[232:235]\n"
"ds_read_b64_tr_b16 a[180:181], v5 offset:8192\n"
"v_mfma_f32_16x16x32_bf16 v[236:239], a[164:167], a[124:127], v[236:239]\n"
"v_mfma_f32_16x16x32_bf16 v[236:239], a[160:163], a[108:111], v[236:239]\n"
"s_waitcnt lgkmcnt(2)\n"
"ds_read_b64_tr_b16 a[182:183], v5 offset:12288\n"
"v_mfma_f32_16x16x32_bf16 v[252:255], a[168:171], a[108:111], v[252:255]\n"
"v_mfma_f32_16x16x32_bf16 v[252:255], a[172:175], a[124:127], v[252:255]\n"
"ds_read_b64_tr_b16 a[184:185], v6 offset:0\n"
"v_mfma_f32_16x16x32_bf16 v[248:251], a[172:175], a[120:123], v[248:251]\n"
"v_mfma_f32_16x16x32_bf16 v[248:251], a[168:171], a[104:107], v[248:251]\n"
"s_add_u32 m0, s9, 128000\n"
"ds_read_b64_tr_b16 a[186:187], v6 offset:4096\n"
"v_mfma_f32_16x16x32_bf16 v[244:247], a[168:171], a[100:103], v[244:247]\n"
"s_mov_b32 s52, 3833984\n"
"buffer_load_dwordx4 v3, s[28:31], s52 offen lds\n"
"v_mfma_f32_16x16x32_bf16 v[244:247], a[172:175], a[116:119], v[244:247]\n"
"ds_read_b64_tr_b16 a[188:189], v6 offset:8192\n"
"v_mfma_f32_16x16x32_bf16 v[240:243], a[172:175], a[112:115], v[240:243]\n"
"v_mfma_f32_16x16x32_bf16 v[240:243], a[168:171], a[96:99], v[240:243]\n"
"s_waitcnt lgkmcnt(2)\n"
"ds_read_b64_tr_b16 a[190:191], v6 offset:12288\n"
"v_mfma_f32_16x16x32_bf16 a[240:243], a[176:179], a[96:99], a[240:243]\n"
"v_mfma_f32_16x16x32_bf16 a[240:243], a[180:183], a[112:115], a[240:243]\n"
"ds_read_b64_tr_b16 a[160:161], v7 offset:0\n"
"v_mfma_f32_16x16x32_bf16 a[244:247], a[180:183], a[116:119], a[244:247]\n"
"v_mfma_f32_16x16x32_bf16 a[244:247], a[176:179], a[100:103], a[244:247]\n"
"s_add_u32 m0, s9, 126976\n"
"ds_read_b64_tr_b16 a[162:163], v7 offset:4096\n"
"v_mfma_f32_16x16x32_bf16 a[248:251], a[176:179], a[104:107], a[248:251]\n"
"s_mov_b32 s53, 3833856\n"
"buffer_load_dwordx4 v3, s[28:31], s53 offen lds\n"
"v_mfma_f32_16x16x32_bf16 a[248:251], a[180:183], a[120:123], a[248:251]\n"
"ds_read_b64_tr_b16 a[164:165], v7 offset:8192\n"
"v_mfma_f32_16x16x32_bf16 a[252:255], a[180:183], a[124:127], a[252:255]\n"
"v_mfma_f32_16x16x32_bf16 a[252:255], a[176:179], a[108:111], a[252:255]\n"
"s_waitcnt lgkmcnt(2)\n"
"ds_read_b64_tr_b16 a[166:167], v7 offset:12288\n"
"v_add_u32 v7, 0x18000, v7\n"
"v_mfma_f32_16x16x32_bf16 a[236:239], a[184:187], a[108:111], a[236:239]\n"
"v_mfma_f32_16x16x32_bf16 a[236:239], a[188:191], a[124:127], a[236:239]\n"
"ds_read_b64_tr_b16 a[168:169], v8 offset:0\n"
"v_mfma_f32_16x16x32_bf16 a[232:235], a[188:191], a[120:123], a[232:235]\n"
"v_mfma_f32_16x16x32_bf16 a[232:235], a[184:187], a[104:107], a[232:235]\n"
"s_add_u32 m0, s49, 135168\n"
"ds_read_b64_tr_b16 a[170:171], v8 offset:4096\n"
"v_mfma_f32_16x16x32_bf16 a[228:231], a[184:187], a[100:103], a[228:231]\n"
"s_mov_b32 s52, 3538944\n"
"buffer_load_dwordx4 v4, s[28:31], s52 offen lds\n"
"v_mfma_f32_16x16x32_bf16 a[228:231], a[188:191], a[116:119], a[228:231]\n"
"ds_read_b64_tr_b16 a[172:173], v8 offset:8192\n"
"v_mfma_f32_16x16x32_bf16 a[224:227], a[188:191], a[112:115], a[224:227]\n"
"v_mfma_f32_16x16x32_bf16 a[224:227], a[184:187], a[96:99], a[224:227]\n"
"s_waitcnt lgkmcnt(2)\n"
"ds_read_b64_tr_b16 a[174:175], v8 offset:12288\n"
"v_add_u32 v8, 0x18000, v8\n"
"v_mfma_f32_16x16x32_bf16 a[144:147], a[160:163], a[96:99], a[144:147]\n"
"v_mfma_f32_16x16x32_bf16 a[144:147], a[164:167], a[112:115], a[144:147]\n"
"ds_read_b64_tr_b16 a[176:177], v5 offset:1024\n"
"v_mfma_f32_16x16x32_bf16 a[148:151], a[164:167], a[116:119], a[148:151]\n"
"v_mfma_f32_16x16x32_bf16 a[148:151], a[160:163], a[100:103], a[148:151]\n"
"ds_read_b64_tr_b16 a[178:179], v5 offset:5120\n"
"v_mfma_f32_16x16x32_bf16 a[152:155], a[160:163], a[104:107], a[152:155]\n"
"s_mov_b32 s52, 49152\n"
"buffer_load_dwordx4 a[32:35], v1, s[20:23], s52 offen offset:0 sc1\n"
"v_mfma_f32_16x16x32_bf16 a[152:155], a[164:167], a[120:123], a[152:155]\n"
"ds_read_b64_tr_b16 a[180:181], v5 offset:9216\n"
"v_mfma_f32_16x16x32_bf16 a[156:159], a[164:167], a[124:127], a[156:159]\n"
"v_mfma_f32_16x16x32_bf16 a[156:159], a[160:163], a[108:111], a[156:159]\n"
"s_waitcnt lgkmcnt(2)\n"
"ds_read_b64_tr_b16 a[182:183], v5 offset:13312\n"
"v_add_u32 v5, 0x18000, v5\n"
"v_mfma_f32_16x16x32_bf16 v[172:175], a[168:171], a[108:111], v[172:175]\n"
"v_mfma_f32_16x16x32_bf16 v[172:175], a[172:175], a[124:127], v[172:175]\n"
"ds_read_b64_tr_b16 a[184:185], v6 offset:1024\n"
"v_mfma_f32_16x16x32_bf16 v[168:171], a[172:175], a[120:123], v[168:171]\n"
"v_mfma_f32_16x16x32_bf16 v[168:171], a[168:171], a[104:107], v[168:171]\n"
"ds_read_b64_tr_b16 a[186:187], v6 offset:5120\n"
"v_mfma_f32_16x16x32_bf16 v[164:167], a[168:171], a[100:103], v[164:167]\n"
"buffer_load_dwordx4 a[36:39], v1, s[20:23], s52 offen offset:1024 sc1\n"
"v_mfma_f32_16x16x32_bf16 v[164:167], a[172:175], a[116:119], v[164:167]\n"
"ds_read_b64_tr_b16 a[188:189], v6 offset:9216\n"
"v_mfma_f32_16x16x32_bf16 v[160:163], a[172:175], a[112:115], v[160:163]\n"
"v_mfma_f32_16x16x32_bf16 v[160:163], a[168:171], a[96:99], v[160:163]\n"
"s_waitcnt lgkmcnt(2)\n"
"ds_read_b64_tr_b16 a[190:191], v6 offset:13312\n"
"v_add_u32 v6, 0x18000, v6\n"
"v_mfma_f32_16x16x32_bf16 v[176:179], a[176:179], a[96:99], v[176:179]\n"
"v_mfma_f32_16x16x32_bf16 v[176:179], a[180:183], a[112:115], v[176:179]\n"
"ds_read_b64_tr_b16 a[160:161], v11 offset:16384\n"
"v_mfma_f32_16x16x32_bf16 v[180:183], a[180:183], a[116:119], v[180:183]\n"
"v_mfma_f32_16x16x32_bf16 v[180:183], a[176:179], a[100:103], v[180:183]\n"
"ds_read_b64_tr_b16 a[162:163], v11 offset:17408\n"
"v_mfma_f32_16x16x32_bf16 v[184:187], a[176:179], a[104:107], v[184:187]\n"
"buffer_load_dwordx4 a[40:43], v1, s[20:23], s52 offen offset:2048 sc1\n"
"v_mfma_f32_16x16x32_bf16 v[184:187], a[180:183], a[120:123], v[184:187]\n"
"ds_read_b64_tr_b16 a[164:165], v11 offset:18432\n"
"v_mfma_f32_16x16x32_bf16 v[188:191], a[180:183], a[124:127], v[188:191]\n"
"v_mfma_f32_16x16x32_bf16 v[188:191], a[176:179], a[108:111], v[188:191]\n"
"s_waitcnt lgkmcnt(2)\n"
"ds_read_b64_tr_b16 a[166:167], v11 offset:19456\n"
"v_add_u32 v11, 0x18000, v11\n"
"v_mfma_f32_16x16x32_bf16 v[204:207], a[184:187], a[108:111], v[204:207]\n"
"s_waitcnt vmcnt(8)\n"
"s_barrier\n"
"v_mfma_f32_16x16x32_bf16 v[204:207], a[188:191], a[124:127], v[204:207]\n"
"ds_read_b64_tr_b16 a[168:169], v7 offset:1024\n"
"v_mfma_f32_16x16x32_bf16 v[200:203], a[188:191], a[120:123], v[200:203]\n"
"v_mfma_f32_16x16x32_bf16 v[200:203], a[184:187], a[104:107], v[200:203]\n"
"ds_read_b64_tr_b16 a[170:171], v7 offset:5120\n"
"v_mfma_f32_16x16x32_bf16 v[196:199], a[184:187], a[100:103], v[196:199]\n"
"buffer_load_dwordx4 a[44:47], v1, s[20:23], s52 offen offset:3072 sc1\n"
"v_mfma_f32_16x16x32_bf16 v[196:199], a[188:191], a[116:119], v[196:199]\n"
"ds_read_b64_tr_b16 a[172:173], v7 offset:9216\n"
"v_mfma_f32_16x16x32_bf16 v[192:195], a[188:191], a[112:115], v[192:195]\n"
"v_mfma_f32_16x16x32_bf16 v[192:195], a[184:187], a[96:99], v[192:195]\n"
"s_waitcnt lgkmcnt(2)\n"
"ds_read_b64_tr_b16 a[174:175], v7 offset:13312\n"
"v_mfma_f32_16x16x32_bf16 v[208:211], a[160:163], a[96:99], v[208:211]\n"
"v_mfma_f32_16x16x32_bf16 v[208:211], a[164:167], a[112:115], v[208:211]\n"
"ds_read_b64_tr_b16 a[176:177], v8 offset:1024\n"
"v_mfma_f32_16x16x32_bf16 v[212:215], a[164:167], a[116:119], v[212:215]\n"
"s_mov_b32 s53, 53248\n"
"buffer_load_dwordx4 a[48:51], v1, s[20:23], s53 offen offset:0 sc1\n"
"v_mfma_f32_16x16x32_bf16 v[212:215], a[160:163], a[100:103], v[212:215]\n"
"ds_read_b64_tr_b16 a[178:179], v8 offset:5120\n"
"v_mfma_f32_16x16x32_bf16 v[216:219], a[160:163], a[104:107], v[216:219]\n"
"v_mfma_f32_16x16x32_bf16 v[216:219], a[164:167], a[120:123], v[216:219]\n"
"ds_read_b64_tr_b16 a[180:181], v8 offset:9216\n"
"v_mfma_f32_16x16x32_bf16 v[220:223], a[164:167], a[124:127], v[220:223]\n"
"v_mfma_f32_16x16x32_bf16 v[220:223], a[160:163], a[108:111], v[220:223]\n"
"s_waitcnt vmcnt(14) lgkmcnt(2)\n"
"ds_read_b64_tr_b16 a[182:183], v8 offset:13312\n"
"v_mfma_f32_16x16x32_bf16 v[236:239], a[168:171], a[12:15], v[236:239]\n"
"v_mfma_f32_16x16x32_bf16 v[236:239], a[172:175], a[28:31], v[236:239]\n"
"ds_read_b64_tr_b16 a[184:185], v5 offset:0\n"
"v_mfma_f32_16x16x32_bf16 v[232:235], a[172:175], a[24:27], v[232:235]\n"
".LPAD2:\n"
"s_cbranch_execz .LPAD5\n"
"buffer_load_dwordx4 a[52:55], v1, s[20:23], s53 offen offset:1024 sc1\n"
"v_mfma_f32_16x16x32_bf16 v[232:235], a[168:171], a[8:11], v[232:235]\n"
"ds_read_b64_tr_b16 a[186:187], v5 offset:4096\n"
"v_mfma_f32_16x16x32_bf16 v[228:231], a[168:171], a[4:7], v[228:231]\n"
"v_mfma_f32_16x16x32_bf16 v[228:231], a[172:175], a[20:23], v[228:231]\n"
"ds_read_b64_tr_b16 a[188:189], v5 offset:8192\n"
"v_mfma_f32_16x16x32_bf16 v[224:227], a[172:175], a[16:19], v[224:227]\n"
"buffer_load_dwordx4 a[56:59], v1, s[20:23], s53 offen offset:2048 sc1\n"
"v_mfma_f32_16x16x32_bf16 v[224:227], a[168:171], a[0:3], v[224:227]\n"
"s_waitcnt lgkmcnt(2)\n"
"ds_read_b64_tr_b16 a[190:191], v5 offset:12288\n"
"v_mfma_f32_16x16x32_bf16 v[240:243], a[176:179], a[0:3], v[240:243]\n"
"v_mfma_f32_16x16x32_bf16 v[240:243], a[180:183], a[16:19], v[240:243]\n"
"ds_read_b64_tr_b16 a[160:161], v6 offset:0\n"
"v_mfma_f32_16x16x32_bf16 v[244:247], a[180:183], a[20:23], v[244:247]\n"
"v_mfma_f32_16x16x32_bf16 v[244:247], a[176:179], a[4:7], v[244:247]\n"
"ds_read_b64_tr_b16 a[162:163], v6 offset:4096\n"
"v_mfma_f32_16x16x32_bf16 v[248:251], a[176:179], a[8:11], v[248:251]\n"
"buffer_load_dwordx4 a[60:63], v1, s[20:23], s53 offen offset:3072 sc1\n"
"v_mfma_f32_16x16x32_bf16 v[248:251], a[180:183], a[24:27], v[248:251]\n"
"ds_read_b64_tr_b16 a[164:165], v6 offset:8192\n"
"v_mfma_f32_16x16x32_bf16 v[252:255], a[180:183], a[28:31], v[252:255]\n"
"v_mfma_f32_16x16x32_bf16 v[252:255], a[176:179], a[12:15], v[252:255]\n"
"s_waitcnt lgkmcnt(2)\n"
"ds_read_b64_tr_b16 a[166:167], v6 offset:12288\n"
"v_mfma_f32_16x16x32_bf16 a[252:255], a[184:187], a[12:15], a[252:255]\n"
"v_mfma_f32_16x16x32_bf16 a[252:255], a[188:191], a[28:31], a[252:255]\n"
"s_add_u32 m0, s9, 140288\n"
"ds_read_b64_tr_b16 a[168:169], v7 offset:0\n"
"v_mfma_f32_16x16x32_bf16 a[248:251], a[188:191], a[24:27], a[248:251]\n"
"s_mov_b32 s52, 4128896\n"
"buffer_load_dwordx4 v3, s[28:31], s52 offen lds\n"
"v_mfma_f32_16x16x32_bf16 a[248:251], a[184:187], a[8:11], a[248:251]\n"
"ds_read_b64_tr_b16 a[170:171], v7 offset:4096\n"
"v_mfma_f32_16x16x32_bf16 a[244:247], a[184:187], a[4:7], a[244:247]\n"
"v_mfma_f32_16x16x32_bf16 a[244:247], a[188:191], a[20:23], a[244:247]\n"
"ds_read_b64_tr_b16 a[172:173], v7 offset:8192\n"
"v_mfma_f32_16x16x32_bf16 a[240:243], a[188:191], a[16:19], a[240:243]\n"
"v_mfma_f32_16x16x32_bf16 a[240:243], a[184:187], a[0:3], a[240:243]\n"
"s_waitcnt lgkmcnt(2)\n"
"ds_read_b64_tr_b16 a[174:175], v7 offset:12288\n"
"v_add_u32 v7, 0x5000, v7\n"
"v_mfma_f32_16x16x32_bf16 a[224:227], a[160:163], a[0:3], a[224:227]\n"
"v_mfma_f32_16x16x32_bf16 a[224:227], a[164:167], a[16:19], a[224:227]\n"
"s_add_u32 m0, s9, 139264\n"
"ds_read_b64_tr_b16 a[176:177], v8 offset:0\n"
"v_mfma_f32_16x16x32_bf16 a[228:231], a[164:167], a[20:23], a[228:231]\n"
"s_mov_b32 s53, 4128768\n"
"buffer_load_dwordx4 v3, s[28:31], s53 offen lds\n"
"v_mfma_f32_16x16x32_bf16 a[228:231], a[160:163], a[4:7], a[228:231]\n"
"ds_read_b64_tr_b16 a[178:179], v8 offset:4096\n"
"v_mfma_f32_16x16x32_bf16 a[232:235], a[160:163], a[8:11], a[232:235]\n"
"v_mfma_f32_16x16x32_bf16 a[232:235], a[164:167], a[24:27], a[232:235]\n"
"ds_read_b64_tr_b16 a[180:181], v8 offset:8192\n"
"v_mfma_f32_16x16x32_bf16 a[236:239], a[164:167], a[28:31], a[236:239]\n"
"v_mfma_f32_16x16x32_bf16 a[236:239], a[160:163], a[12:15], a[236:239]\n"
"s_waitcnt lgkmcnt(2)\n"
"ds_read_b64_tr_b16 a[182:183], v8 offset:12288\n"
"v_add_u32 v8, 0x5000, v8\n"
"v_mfma_f32_16x16x32_bf16 a[156:159], a[168:171], a[12:15], a[156:159]\n"
"v_mfma_f32_16x16x32_bf16 a[156:159], a[172:175], a[28:31], a[156:159]\n"
"s_add_u32 m0, s9, 148480\n"
"ds_read_b64_tr_b16 a[184:185], v5 offset:1024\n"
"v_mfma_f32_16x16x32_bf16 a[152:155], a[172:175], a[24:27], a[152:155]\n"
"s_mov_b32 s52, 4423808\n"
"buffer_load_dwordx4 v3, s[28:31], s52 offen lds\n"
"v_mfma_f32_16x16x32_bf16 a[152:155], a[168:171], a[8:11], a[152:155]\n"
"ds_read_b64_tr_b16 a[186:187], v5 offset:5120\n"
"v_mfma_f32_16x16x32_bf16 a[148:151], a[168:171], a[4:7], a[148:151]\n"
"v_mfma_f32_16x16x32_bf16 a[148:151], a[172:175], a[20:23], a[148:151]\n"
"ds_read_b64_tr_b16 a[188:189], v5 offset:9216\n"
"v_mfma_f32_16x16x32_bf16 a[144:147], a[172:175], a[16:19], a[144:147]\n"
"v_mfma_f32_16x16x32_bf16 a[144:147], a[168:171], a[0:3], a[144:147]\n"
"s_waitcnt lgkmcnt(2)\n"
"ds_read_b64_tr_b16 a[190:191], v5 offset:13312\n"
"v_add_u32 v5, 0x5000, v5\n"
"v_mfma_f32_16x16x32_bf16 v[160:163], a[176:179], a[0:3], v[160:163]\n"
"v_mfma_f32_16x16x32_bf16 v[160:163], a[180:183], a[16:19], v[160:163]\n"
"s_add_u32 m0, s9, 147456\n"
"ds_read_b64_tr_b16 a[160:161], v6 offset:1024\n"
"v_mfma_f32_16x16x32_bf16 v[164:167], a[180:183], a[20:23], v[164:167]\n"
"s_mov_b32 s53, 4423680\n"
"buffer_load_dwordx4 v3, s[28:31], s53 offen lds\n"
"v_mfma_f32_16x16x32_bf16 v[164:167], a[176:179], a[4:7], v[164:167]\n"
"ds_read_b64_tr_b16 a[162:163], v6 offset:5120\n"
"v_mfma_f32_16x16x32_bf16 v[168:171], a[176:179], a[8:11], v[168:171]\n"
"v_mfma_f32_16x16x32_bf16 v[168:171], a[180:183], a[24:27], v[168:171]\n"
"ds_read_b64_tr_b16 a[164:165], v6 offset:9216\n"
"v_mfma_f32_16x16x32_bf16 v[172:175], a[180:183], a[28:31], v[172:175]\n"
"v_mfma_f32_16x16x32_bf16 v[172:175], a[176:179], a[12:15], v[172:175]\n"
"s_waitcnt lgkmcnt(2)\n"
"ds_read_b64_tr_b16 a[166:167], v6 offset:13312\n"
"v_add_u32 v6, 0x5000, v6\n"
"v_mfma_f32_16x16x32_bf16 v[188:191], a[184:187], a[12:15], v[188:191]\n"
"v_mfma_f32_16x16x32_bf16 v[188:191], a[188:191], a[28:31], v[188:191]\n"
"s_add_u32 m0, s49, 155648\n"
"ds_read_b64_tr_b16 a[168:169], v11 offset:16384\n"
"v_mfma_f32_16x16x32_bf16 v[184:187], a[188:191], a[24:27], v[184:187]\n"
"s_mov_b32 s52, 4128768\n"
"buffer_load_dwordx4 v4, s[28:31], s52 offen lds\n"
"v_mfma_f32_16x16x32_bf16 v[184:187], a[184:187], a[8:11], v[184:187]\n"
"ds_read_b64_tr_b16 a[170:171], v11 offset:17408\n"
"v_mfma_f32_16x16x32_bf16 v[180:183], a[184:187], a[4:7], v[180:183]\n"
"v_mfma_f32_16x16x32_bf16 v[180:183], a[188:191], a[20:23], v[180:183]\n"
"ds_read_b64_tr_b16 a[172:173], v11 offset:18432\n"
"v_mfma_f32_16x16x32_bf16 v[176:179], a[188:191], a[16:19], v[176:179]\n"
"s_mov_b32 s52, 57344\n"
"buffer_load_dwordx4 a[64:67], v1, s[20:23], s52 offen offset:0 sc1\n"
"v_mfma_f32_16x16x32_bf16 v[176:179], a[184:187], a[0:3], v[176:179]\n"
"s_waitcnt lgkmcnt(2)\n"
"ds_read_b64_tr_b16 a[174:175], v11 offset:19456\n"
"v_add_u32 v11, 0x5000, v11\n"
"v_mfma_f32_16x16x32_bf16 v[192:195], a[160:163], a[0:3], v[192:195]\n"
"v_mfma_f32_16x16x32_bf16 v[192:195], a[164:167], a[16:19], v[192:195]\n"
"ds_read_b64_tr_b16 a[176:177], v9 offset:49152\n"
"v_mfma_f32_16x16x32_bf16 v[196:199], a[164:167], a[20:23], v[196:199]\n"
"v_mfma_f32_16x16x32_bf16 v[196:199], a[160:163], a[4:7], v[196:199]\n"
"ds_read_b64_tr_b16 a[178:179], v9 offset:53248\n"
"v_mfma_f32_16x16x32_bf16 v[200:203], a[160:163], a[8:11], v[200:203]\n"
"v_mfma_f32_16x16x32_bf16 v[200:203], a[164:167], a[24:27], v[200:203]\n"
"ds_read_b64_tr_b16 a[180:181], v9 offset:57344\n"
"v_mfma_f32_16x16x32_bf16 v[204:207], a[164:167], a[28:31], v[204:207]\n"
"buffer_load_dwordx4 a[68:71], v1, s[20:23], s52 offen offset:1024 sc1\n"
"v_mfma_f32_16x16x32_bf16 v[204:207], a[160:163], a[12:15], v[204:207]\n"
"s_waitcnt lgkmcnt(2)\n"
"ds_read_b64_tr_b16 a[182:183], v9 offset:61440\n"
"v_mfma_f32_16x16x32_bf16 v[220:223], a[168:171], a[12:15], v[220:223]\n"
"v_mfma_f32_16x16x32_bf16 v[220:223], a[172:175], a[28:31], v[220:223]\n"
"ds_read_b64_tr_b16 a[184:185], v9 offset:49160\n"
"v_mfma_f32_16x16x32_bf16 v[216:219], a[172:175], a[24:27], v[216:219]\n"
"v_mfma_f32_16x16x32_bf16 v[216:219], a[168:171], a[8:11], v[216:219]\n"
"ds_read_b64_tr_b16 a[186:187], v9 offset:53256\n"
"v_mfma_f32_16x16x32_bf16 v[212:215], a[168:171], a[4:7], v[212:215]\n"
"buffer_load_dwordx4 a[72:75], v1, s[20:23], s52 offen offset:2048 sc1\n"
"v_mfma_f32_16x16x32_bf16 v[212:215], a[172:175], a[20:23], v[212:215]\n"
"ds_read_b64_tr_b16 a[188:189], v9 offset:57352\n"
"v_mfma_f32_16x16x32_bf16 v[208:211], a[172:175], a[16:19], v[208:211]\n"
"v_mfma_f32_16x16x32_bf16 v[208:211], a[168:171], a[0:3], v[208:211]\n"
"s_waitcnt lgkmcnt(2)\n"
"ds_read_b64_tr_b16 a[190:191], v9 offset:61448\n"
"v_mfma_f32_16x16x32_bf16 v[32:35], a[176:179], a[96:99], v[32:35]\n"
"v_mfma_f32_16x16x32_bf16 v[32:35], a[180:183], a[112:115], v[32:35]\n"
"ds_read_b64_tr_b16 a[160:161], v10 offset:49152\n"
"v_mfma_f32_16x16x32_bf16 v[36:39], a[180:183], a[116:119], v[36:39]\n"
"buffer_load_dwordx4 a[76:79], v1, s[20:23], s52 offen offset:3072 sc1\n"
"v_mfma_f32_16x16x32_bf16 v[36:39], a[176:179], a[100:103], v[36:39]\n"
"ds_read_b64_tr_b16 a[162:163], v10 offset:53248\n"
"v_mfma_f32_16x16x32_bf16 v[40:43], a[176:179], a[104:107], v[40:43]\n"
"v_mfma_f32_16x16x32_bf16 v[40:43], a[180:183], a[120:123], v[40:43]\n"
"ds_read_b64_tr_b16 a[164:165], v10 offset:57344\n"
"v_mfma_f32_16x16x32_bf16 v[44:47], a[180:183], a[124:127], v[44:47]\n"
"v_mfma_f32_16x16x32_bf16 v[44:47], a[176:179], a[108:111], v[44:47]\n"
"s_waitcnt lgkmcnt(2)\n"
"ds_read_b64_tr_b16 a[166:167], v10 offset:61440\n"
"v_mfma_f32_16x16x32_bf16 v[60:63], a[184:187], a[108:111], v[60:63]\n"
"v_mfma_f32_16x16x32_bf16 v[60:63], a[188:191], a[124:127], v[60:63]\n"
"ds_read_b64_tr_b16 a[168:169], v10 offset:49160\n"
"v_mfma_f32_16x16x32_bf16 v[56:59], a[188:191], a[120:123], v[56:59]\n"
"s_mov_b32 s53, 61440\n"
"buffer_load_dwordx4 a[80:83], v1, s[20:23], s53 offen offset:0 sc1\n"
"v_mfma_f32_16x16x32_bf16 v[56:59], a[184:187], a[104:107], v[56:59]\n"
"ds_read_b64_tr_b16 a[170:171], v10 offset:53256\n"
"v_mfma_f32_16x16x32_bf16 v[52:55], a[184:187], a[100:103], v[52:55]\n"
"v_mfma_f32_16x16x32_bf16 v[52:55], a[188:191], a[116:119], v[52:55]\n"
"ds_read_b64_tr_b16 a[172:173], v10 offset:57352\n"
"v_mfma_f32_16x16x32_bf16 v[48:51], a[188:191], a[112:115], v[48:51]\n"
"buffer_load_dwordx4 a[84:87], v1, s[20:23], s53 offen offset:1024 sc1\n"
"v_mfma_f32_16x16x32_bf16 v[48:51], a[184:187], a[96:99], v[48:51]\n"
"s_waitcnt lgkmcnt(2)\n"
"ds_read_b64_tr_b16 a[174:175], v10 offset:61448\n"
"v_mfma_f32_16x16x32_bf16 v[64:67], a[160:163], a[96:99], v[64:67]\n"
"v_mfma_f32_16x16x32_bf16 v[64:67], a[164:167], a[112:115], v[64:67]\n"
"ds_read_b64_tr_b16 a[176:177], v9 offset:50176\n"
"v_mfma_f32_16x16x32_bf16 v[68:71], a[164:167], a[116:119], v[68:71]\n"
"v_mfma_f32_16x16x32_bf16 v[68:71], a[160:163], a[100:103], v[68:71]\n"
"ds_read_b64_tr_b16 a[178:179], v9 offset:54272\n"
"v_mfma_f32_16x16x32_bf16 v[72:75], a[160:163], a[104:107], v[72:75]\n"
"buffer_load_dwordx4 a[88:91], v1, s[20:23], s53 offen offset:2048 sc1\n"
"v_mfma_f32_16x16x32_bf16 v[72:75], a[164:167], a[120:123], v[72:75]\n"
"ds_read_b64_tr_b16 a[180:181], v9 offset:58368\n"
"v_mfma_f32_16x16x32_bf16 v[76:79], a[164:167], a[124:127], v[76:79]\n"
"v_mfma_f32_16x16x32_bf16 v[76:79], a[160:163], a[108:111], v[76:79]\n"
"s_waitcnt lgkmcnt(2)\n"
"ds_read_b64_tr_b16 a[182:183], v9 offset:62464\n"
"v_mfma_f32_16x16x32_bf16 v[92:95], a[168:171], a[108:111], v[92:95]\n"
"v_mfma_f32_16x16x32_bf16 v[92:95], a[172:175], a[124:127], v[92:95]\n"
"ds_read_b64_tr_b16 a[184:185], v9 offset:50184\n"
"v_mfma_f32_16x16x32_bf16 v[88:91], a[172:175], a[120:123], v[88:91]\n"
"buffer_load_dwordx4 a[92:95], v1, s[20:23], s53 offen offset:3072 sc1\n"
"v_mfma_f32_16x16x32_bf16 v[88:91], a[168:171], a[104:107], v[88:91]\n"
"ds_read_b64_tr_b16 a[186:187], v9 offset:54280\n"
"v_mfma_f32_16x16x32_bf16 v[84:87], a[168:171], a[100:103], v[84:87]\n"
"v_mfma_f32_16x16x32_bf16 v[84:87], a[172:175], a[116:119], v[84:87]\n"
"ds_read_b64_tr_b16 a[188:189], v9 offset:58376\n"
"v_mfma_f32_16x16x32_bf16 v[80:83], a[172:175], a[112:115], v[80:83]\n"
"v_mfma_f32_16x16x32_bf16 v[80:83], a[168:171], a[96:99], v[80:83]\n"
"s_waitcnt lgkmcnt(2)\n"
"ds_read_b64_tr_b16 a[190:191], v9 offset:62472\n"
"v_mfma_f32_16x16x32_bf16 v[96:99], a[176:179], a[96:99], v[96:99]\n"
"v_mfma_f32_16x16x32_bf16 v[96:99], a[180:183], a[112:115], v[96:99]\n"
"s_add_u32 m0, s9, 33792\n"
"ds_read_b64_tr_b16 a[160:161], v10 offset:50176\n"
"v_mfma_f32_16x16x32_bf16 v[100:103], a[180:183], a[116:119], v[100:103]\n"
"s_mov_b32 s52, 2621568\n"
"buffer_load_dwordx4 v2, s[24:27], s52 offen lds\n"
"v_mfma_f32_16x16x32_bf16 v[100:103], a[176:179], a[100:103], v[100:103]\n"
"ds_read_b64_tr_b16 a[162:163], v10 offset:54272\n"
"v_mfma_f32_16x16x32_bf16 v[104:107], a[176:179], a[104:107], v[104:107]\n"
"v_mfma_f32_16x16x32_bf16 v[104:107], a[180:183], a[120:123], v[104:107]\n"
"ds_read_b64_tr_b16 a[164:165], v10 offset:58368\n"
"v_mfma_f32_16x16x32_bf16 v[108:111], a[180:183], a[124:127], v[108:111]\n"
"v_mfma_f32_16x16x32_bf16 v[108:111], a[176:179], a[108:111], v[108:111]\n"
"s_waitcnt lgkmcnt(2)\n"
"ds_read_b64_tr_b16 a[166:167], v10 offset:62464\n"
"v_mfma_f32_16x16x32_bf16 v[124:127], a[184:187], a[108:111], v[124:127]\n"
"v_mfma_f32_16x16x32_bf16 v[124:127], a[188:191], a[124:127], v[124:127]\n"
"s_add_u32 m0, s9, 32768\n"
"ds_read_b64_tr_b16 a[168:169], v10 offset:50184\n"
"v_mfma_f32_16x16x32_bf16 v[120:123], a[188:191], a[120:123], v[120:123]\n"
"s_mov_b32 s53, 2621440\n"
"buffer_load_dwordx4 v2, s[24:27], s53 offen lds\n"
"v_mfma_f32_16x16x32_bf16 v[120:123], a[184:187], a[104:107], v[120:123]\n"
"ds_read_b64_tr_b16 a[170:171], v10 offset:54280\n"
"v_mfma_f32_16x16x32_bf16 v[116:119], a[184:187], a[100:103], v[116:119]\n"
"v_mfma_f32_16x16x32_bf16 v[116:119], a[188:191], a[116:119], v[116:119]\n"
"ds_read_b64_tr_b16 a[172:173], v10 offset:58376\n"
"v_mfma_f32_16x16x32_bf16 v[112:115], a[188:191], a[112:115], v[112:115]\n"
"v_mfma_f32_16x16x32_bf16 v[112:115], a[184:187], a[96:99], v[112:115]\n"
"s_waitcnt lgkmcnt(2)\n"
"ds_read_b64_tr_b16 a[174:175], v10 offset:62472\n"
"v_mfma_f32_16x16x32_bf16 v[128:131], a[160:163], a[96:99], v[128:131]\n"
"s_waitcnt vmcnt(23)\n"
"s_barrier\n"
"v_mfma_f32_16x16x32_bf16 v[128:131], a[164:167], a[112:115], v[128:131]\n"
"s_add_u32 m0, s9, 41984\n"
"ds_read_b64_tr_b16 a[176:177], v7 offset:1024\n"
"v_mfma_f32_16x16x32_bf16 v[132:135], a[164:167], a[116:119], v[132:135]\n"
"s_mov_b32 s52, 2883712\n"
"buffer_load_dwordx4 v2, s[24:27], s52 offen lds\n"
"v_mfma_f32_16x16x32_bf16 v[132:135], a[160:163], a[100:103], v[132:135]\n"
"ds_read_b64_tr_b16 a[178:179], v7 offset:5120\n"
"v_mfma_f32_16x16x32_bf16 v[136:139], a[160:163], a[104:107], v[136:139]\n"
"v_mfma_f32_16x16x32_bf16 v[136:139], a[164:167], a[120:123], v[136:139]\n"
"s_add_u32 m0, s9, 40960\n"
"ds_read_b64_tr_b16 a[180:181], v7 offset:9216\n"
"v_mfma_f32_16x16x32_bf16 v[140:143], a[164:167], a[124:127], v[140:143]\n"
"s_mov_b32 s53, 2883584\n"
"buffer_load_dwordx4 v2, s[24:27], s53 offen lds\n"
"v_mfma_f32_16x16x32_bf16 v[140:143], a[160:163], a[108:111], v[140:143]\n"
"s_waitcnt lgkmcnt(2)\n"
"ds_read_b64_tr_b16 a[182:183], v7 offset:13312\n"
"v_mfma_f32_16x16x32_bf16 v[156:159], a[168:171], a[108:111], v[156:159]\n"
"v_mfma_f32_16x16x32_bf16 v[156:159], a[172:175], a[124:127], v[156:159]\n"
"ds_read_b64_tr_b16 a[184:185], v8 offset:1024\n"
"v_mfma_f32_16x16x32_bf16 v[152:155], a[172:175], a[120:123], v[152:155]\n"
"v_mfma_f32_16x16x32_bf16 v[152:155], a[168:171], a[104:107], v[152:155]\n"
"s_add_u32 m0, s9, 1024\n"
"ds_read_b64_tr_b16 a[186:187], v8 offset:5120\n"
"v_mfma_f32_16x16x32_bf16 v[148:151], a[168:171], a[100:103], v[148:151]\n"
"s_mov_b32 s52, 4718720\n"
"buffer_load_dwordx4 v3, s[28:31], s52 offen lds\n"
"v_mfma_f32_16x16x32_bf16 v[148:151], a[172:175], a[116:119], v[148:151]\n"
"ds_read_b64_tr_b16 a[188:189], v8 offset:9216\n"
"v_mfma_f32_16x16x32_bf16 v[144:147], a[172:175], a[112:115], v[144:147]\n"
"v_mfma_f32_16x16x32_bf16 v[144:147], a[168:171], a[96:99], v[144:147]\n"
"s_waitcnt vmcnt(18) lgkmcnt(2)\n"
"ds_read_b64_tr_b16 a[190:191], v8 offset:13312\n"
"v_mfma_f32_16x16x32_bf16 v[224:227], a[176:179], a[32:35], v[224:227]\n"
"v_mfma_f32_16x16x32_bf16 v[224:227], a[180:183], a[48:51], v[224:227]\n"
"ds_read_b64_tr_b16 a[160:161], v5 offset:0\n"
"v_mfma_f32_16x16x32_bf16 v[228:231], a[180:183], a[52:55], v[228:231]\n"
"v_mfma_f32_16x16x32_bf16 v[228:231], a[176:179], a[36:39], v[228:231]\n"
"s_add_u32 m0, s9, 0\n"
"ds_read_b64_tr_b16 a[162:163], v5 offset:4096\n"
"v_mfma_f32_16x16x32_bf16 v[232:235], a[176:179], a[40:43], v[232:235]\n"
"s_mov_b32 s53, 4718592\n"
"buffer_load_dwordx4 v3, s[28:31], s53 offen lds\n"
"v_mfma_f32_16x16x32_bf16 v[232:235], a[180:183], a[56:59], v[232:235]\n"
"ds_read_b64_tr_b16 a[164:165], v5 offset:8192\n"
"v_mfma_f32_16x16x32_bf16 v[236:239], a[180:183], a[60:63], v[236:239]\n"
"v_mfma_f32_16x16x32_bf16 v[236:239], a[176:179], a[44:47], v[236:239]\n"
"s_waitcnt lgkmcnt(2)\n"
"ds_read_b64_tr_b16 a[166:167], v5 offset:12288\n"
"v_mfma_f32_16x16x32_bf16 v[252:255], a[184:187], a[44:47], v[252:255]\n"
"v_mfma_f32_16x16x32_bf16 v[252:255], a[188:191], a[60:63], v[252:255]\n"
"ds_read_b64_tr_b16 a[168:169], v6 offset:0\n"
"v_mfma_f32_16x16x32_bf16 v[248:251], a[188:191], a[56:59], v[248:251]\n"
"v_mfma_f32_16x16x32_bf16 v[248:251], a[184:187], a[40:43], v[248:251]\n"
"s_add_u32 m0, s9, 9216\n"
"ds_read_b64_tr_b16 a[170:171], v6 offset:4096\n"
"v_mfma_f32_16x16x32_bf16 v[244:247], a[184:187], a[36:39], v[244:247]\n"
"s_mov_b32 s52, 5013632\n"
"buffer_load_dwordx4 v3, s[28:31], s52 offen lds\n"
"v_mfma_f32_16x16x32_bf16 v[244:247], a[188:191], a[52:55], v[244:247]\n"
"ds_read_b64_tr_b16 a[172:173], v6 offset:8192\n"
"v_mfma_f32_16x16x32_bf16 v[240:243], a[188:191], a[48:51], v[240:243]\n"
"v_mfma_f32_16x16x32_bf16 v[240:243], a[184:187], a[32:35], v[240:243]\n"
"s_waitcnt lgkmcnt(2)\n"
"ds_read_b64_tr_b16 a[174:175], v6 offset:12288\n"
"v_mfma_f32_16x16x32_bf16 a[240:243], a[160:163], a[32:35], a[240:243]\n"
"v_mfma_f32_16x16x32_bf16 a[240:243], a[164:167], a[48:51], a[240:243]\n"
"ds_read_b64_tr_b16 a[176:177], v7 offset:0\n"
"v_mfma_f32_16x16x32_bf16 a[244:247], a[164:167], a[52:55], a[244:247]\n"
"v_mfma_f32_16x16x32_bf16 a[244:247], a[160:163], a[36:39], a[244:247]\n"
"s_add_u32 m0, s9, 8192\n"
"ds_read_b64_tr_b16 a[178:179], v7 offset:4096\n"
"v_mfma_f32_16x16x32_bf16 a[248:251], a[160:163], a[40:43], a[248:251]\n"
"s_mov_b32 s53, 5013504\n"
"buffer_load_dwordx4 v3, s[28:31], s53 offen lds\n"
"v_mfma_f32_16x16x32_bf16 a[248:251], a[164:167], a[56:59], a[248:251]\n"
"ds_read_b64_tr_b16 a[180:181], v7 offset:8192\n"
"v_mfma_f32_16x16x32_bf16 a[252:255], a[164:167], a[60:63], a[252:255]\n"
"v_mfma_f32_16x16x32_bf16 a[252:255], a[160:163], a[44:47], a[252:255]\n"
"s_waitcnt lgkmcnt(2)\n"
"ds_read_b64_tr_b16 a[182:183], v7 offset:12288\n"
"v_add_u32 v7, 0x5000, v7\n"
"v_mfma_f32_16x16x32_bf16 a[236:239], a[168:171], a[44:47], a[236:239]\n"
"v_mfma_f32_16x16x32_bf16 a[236:239], a[172:175], a[60:63], a[236:239]\n"
"ds_read_b64_tr_b16 a[184:185], v8 offset:0\n"
"v_mfma_f32_16x16x32_bf16 a[232:235], a[172:175], a[56:59], a[232:235]\n"
"v_mfma_f32_16x16x32_bf16 a[232:235], a[168:171], a[40:43], a[232:235]\n"
"s_add_u32 m0, s49, 16384\n"
"ds_read_b64_tr_b16 a[186:187], v8 offset:4096\n"
"v_mfma_f32_16x16x32_bf16 a[228:231], a[168:171], a[36:39], a[228:231]\n"
"s_mov_b32 s52, 4718592\n"
"buffer_load_dwordx4 v4, s[28:31], s52 offen lds\n"
"v_mfma_f32_16x16x32_bf16 a[228:231], a[172:175], a[52:55], a[228:231]\n"
"ds_read_b64_tr_b16 a[188:189], v8 offset:8192\n"
"v_mov_b32 v3, s48\n"
"v_mfma_f32_16x16x32_bf16 a[224:227], a[172:175], a[48:51], a[224:227]\n"
"v_lshl_add_u32 v3, v3, 2, v1\n"
"v_add_u32 v3, 98304, v3\n"
"v_mfma_f32_16x16x32_bf16 a[224:227], a[168:171], a[32:35], a[224:227]\n"
"s_waitcnt lgkmcnt(2)\n"
"ds_read_b64_tr_b16 a[190:191], v8 offset:12288\n"
"v_add_u32 v8, 0x5000, v8\n"
"v_mfma_f32_16x16x32_bf16 a[144:147], a[176:179], a[32:35], a[144:147]\n"
"v_mfma_f32_16x16x32_bf16 a[144:147], a[180:183], a[48:51], a[144:147]\n"
"ds_read_b64_tr_b16 a[160:161], v5 offset:1024\n"
"v_mfma_f32_16x16x32_bf16 a[148:151], a[180:183], a[52:55], a[148:151]\n"
"v_mfma_f32_16x16x32_bf16 a[148:151], a[176:179], a[36:39], a[148:151]\n"
"ds_read_b64_tr_b16 a[162:163], v5 offset:5120\n"
"v_mfma_f32_16x16x32_bf16 a[152:155], a[176:179], a[40:43], a[152:155]\n"
"s_mov_b32 s52, 65536\n"
"buffer_load_dwordx4 a[192:195], v1, s[20:23], s52 offen offset:0 sc1\n"
"v_mfma_f32_16x16x32_bf16 a[152:155], a[180:183], a[56:59], a[152:155]\n"
"ds_read_b64_tr_b16 a[164:165], v5 offset:9216\n"
"v_mfma_f32_16x16x32_bf16 a[156:159], a[180:183], a[60:63], a[156:159]\n"
"v_mfma_f32_16x16x32_bf16 a[156:159], a[176:179], a[44:47], a[156:159]\n"
"s_waitcnt lgkmcnt(2)\n"
"ds_read_b64_tr_b16 a[166:167], v5 offset:13312\n"
"v_add_u32 v5, 0x5000, v5\n"
"v_mfma_f32_16x16x32_bf16 v[172:175], a[184:187], a[44:47], v[172:175]\n"
"v_mfma_f32_16x16x32_bf16 v[172:175], a[188:191], a[60:63], v[172:175]\n"
"ds_read_b64_tr_b16 a[168:169], v6 offset:1024\n"
"v_mfma_f32_16x16x32_bf16 v[168:171], a[188:191], a[56:59], v[168:171]\n"
"v_mfma_f32_16x16x32_bf16 v[168:171], a[184:187], a[40:43], v[168:171]\n"
"ds_read_b64_tr_b16 a[170:171], v6 offset:5120\n"
"v_mfma_f32_16x16x32_bf16 v[164:167], a[184:187], a[36:39], v[164:167]\n"
"buffer_load_dwordx4 a[196:199], v1, s[20:23], s52 offen offset:1024 sc1\n"
"v_mfma_f32_16x16x32_bf16 v[164:167], a[188:191], a[52:55], v[164:167]\n"
"ds_read_b64_tr_b16 a[172:173], v6 offset:9216\n"
"v_mfma_f32_16x16x32_bf16 v[160:163], a[188:191], a[48:51], v[160:163]\n"
"v_mfma_f32_16x16x32_bf16 v[160:163], a[184:187], a[32:35], v[160:163]\n"
"s_waitcnt lgkmcnt(2)\n"
"ds_read_b64_tr_b16 a[174:175], v6 offset:13312\n"
"v_add_u32 v6, 0x5000, v6\n"
"v_mfma_f32_16x16x32_bf16 v[176:179], a[160:163], a[32:35], v[176:179]\n"
"v_mfma_f32_16x16x32_bf16 v[176:179], a[164:167], a[48:51], v[176:179]\n"
"ds_read_b64_tr_b16 a[176:177], v11 offset:16384\n"
"v_mfma_f32_16x16x32_bf16 v[180:183], a[164:167], a[52:55], v[180:183]\n"
"v_mfma_f32_16x16x32_bf16 v[180:183], a[160:163], a[36:39], v[180:183]\n"
"ds_read_b64_tr_b16 a[178:179], v11 offset:17408\n"
"v_mfma_f32_16x16x32_bf16 v[184:187], a[160:163], a[40:43], v[184:187]\n"
"buffer_load_dwordx4 a[200:203], v1, s[20:23], s52 offen offset:2048 sc1\n"
"v_mfma_f32_16x16x32_bf16 v[184:187], a[164:167], a[56:59], v[184:187]\n"
"ds_read_b64_tr_b16 a[180:181], v11 offset:18432\n"
"v_mfma_f32_16x16x32_bf16 v[188:191], a[164:167], a[60:63], v[188:191]\n"
"v_mfma_f32_16x16x32_bf16 v[188:191], a[160:163], a[44:47], v[188:191]\n"
"s_waitcnt lgkmcnt(2)\n"
"ds_read_b64_tr_b16 a[182:183], v11 offset:19456\n"
"v_add_u32 v11, 0x5000, v11\n"
"v_mfma_f32_16x16x32_bf16 v[204:207], a[168:171], a[44:47], v[204:207]\n"
"s_waitcnt vmcnt(8)\n"
"s_barrier\n"
"v_mfma_f32_16x16x32_bf16 v[204:207], a[172:175], a[60:63], v[204:207]\n"
"ds_read_b64_tr_b16 a[184:185], v7 offset:1024\n"
"v_mfma_f32_16x16x32_bf16 v[200:203], a[172:175], a[56:59], v[200:203]\n"
"v_mfma_f32_16x16x32_bf16 v[200:203], a[168:171], a[40:43], v[200:203]\n"
"ds_read_b64_tr_b16 a[186:187], v7 offset:5120\n"
"v_mfma_f32_16x16x32_bf16 v[196:199], a[168:171], a[36:39], v[196:199]\n"
"buffer_load_dwordx4 a[204:207], v1, s[20:23], s52 offen offset:3072 sc1\n"
"v_mfma_f32_16x16x32_bf16 v[196:199], a[172:175], a[52:55], v[196:199]\n"
"ds_read_b64_tr_b16 a[188:189], v7 offset:9216\n"
"v_mfma_f32_16x16x32_bf16 v[192:195], a[172:175], a[48:51], v[192:195]\n"
"v_mfma_f32_16x16x32_bf16 v[192:195], a[168:171], a[32:35], v[192:195]\n"
"s_waitcnt lgkmcnt(2)\n"
"ds_read_b64_tr_b16 a[190:191], v7 offset:13312\n"
"v_mfma_f32_16x16x32_bf16 v[208:211], a[176:179], a[32:35], v[208:211]\n"
"v_mfma_f32_16x16x32_bf16 v[208:211], a[180:183], a[48:51], v[208:211]\n"
"ds_read_b64_tr_b16 a[160:161], v8 offset:1024\n"
"v_mfma_f32_16x16x32_bf16 v[212:215], a[180:183], a[52:55], v[212:215]\n"
"s_mov_b32 s53, 69632\n"
"buffer_load_dwordx4 a[208:211], v1, s[20:23], s53 offen offset:0 sc1\n"
"v_mfma_f32_16x16x32_bf16 v[212:215], a[176:179], a[36:39], v[212:215]\n"
"ds_read_b64_tr_b16 a[162:163], v8 offset:5120\n"
"v_mfma_f32_16x16x32_bf16 v[216:219], a[176:179], a[40:43], v[216:219]\n"
"v_mfma_f32_16x16x32_bf16 v[216:219], a[180:183], a[56:59], v[216:219]\n"
"ds_read_b64_tr_b16 a[164:165], v8 offset:9216\n"
"v_mfma_f32_16x16x32_bf16 v[220:223], a[180:183], a[60:63], v[220:223]\n"
"v_mfma_f32_16x16x32_bf16 v[220:223], a[176:179], a[44:47], v[220:223]\n"
"s_waitcnt vmcnt(14) lgkmcnt(2)\n"
"ds_read_b64_tr_b16 a[166:167], v8 offset:13312\n"
"v_mfma_f32_16x16x32_bf16 v[236:239], a[184:187], a[76:79], v[236:239]\n"
"v_mfma_f32_16x16x32_bf16 v[236:239], a[188:191], a[92:95], v[236:239]\n"
"ds_read_b64_tr_b16 a[168:169], v5 offset:0\n"
"v_mfma_f32_16x16x32_bf16 v[232:235], a[188:191], a[88:91], v[232:235]\n"
"buffer_load_dwordx4 a[212:215], v1, s[20:23], s53 offen offset:1024 sc1\n"
"v_mfma_f32_16x16x32_bf16 v[232:235], a[184:187], a[72:75], v[232:235]\n"
"ds_read_b64_tr_b16 a[170:171], v5 offset:4096\n"
"v_mfma_f32_16x16x32_bf16 v[228:231], a[184:187], a[68:71], v[228:231]\n"
"v_mfma_f32_16x16x32_bf16 v[228:231], a[188:191], a[84:87], v[228:231]\n"
"ds_read_b64_tr_b16 a[172:173], v5 offset:8192\n"
"v_mfma_f32_16x16x32_bf16 v[224:227], a[188:191], a[80:83], v[224:227]\n"
"buffer_load_dwordx4 a[216:219], v1, s[20:23], s53 offen offset:2048 sc1\n"
"v_mfma_f32_16x16x32_bf16 v[224:227], a[184:187], a[64:67], v[224:227]\n"
"s_waitcnt lgkmcnt(2)\n"
"ds_read_b64_tr_b16 a[174:175], v5 offset:12288\n"
"v_mfma_f32_16x16x32_bf16 v[240:243], a[160:163], a[64:67], v[240:243]\n"
"v_mfma_f32_16x16x32_bf16 v[240:243], a[164:167], a[80:83], v[240:243]\n"
"ds_read_b64_tr_b16 a[176:177], v6 offset:0\n"
"v_mfma_f32_16x16x32_bf16 v[244:247], a[164:167], a[84:87], v[244:247]\n"
"v_mfma_f32_16x16x32_bf16 v[244:247], a[160:163], a[68:71], v[244:247]\n"
"ds_read_b64_tr_b16 a[178:179], v6 offset:4096\n"
"v_mfma_f32_16x16x32_bf16 v[248:251], a[160:163], a[72:75], v[248:251]\n"
"buffer_load_dwordx4 a[220:223], v1, s[20:23], s53 offen offset:3072 sc1\n"
"v_mfma_f32_16x16x32_bf16 v[248:251], a[164:167], a[88:91], v[248:251]\n"
"ds_read_b64_tr_b16 a[180:181], v6 offset:8192\n"
"v_mfma_f32_16x16x32_bf16 v[252:255], a[164:167], a[92:95], v[252:255]\n"
"v_mfma_f32_16x16x32_bf16 v[252:255], a[160:163], a[76:79], v[252:255]\n"
"s_waitcnt lgkmcnt(2)\n"
"ds_read_b64_tr_b16 a[182:183], v6 offset:12288\n"
"v_mfma_f32_16x16x32_bf16 a[252:255], a[168:171], a[76:79], a[252:255]\n"
"v_mfma_f32_16x16x32_bf16 a[252:255], a[172:175], a[92:95], a[252:255]\n"
"ds_read_b64_tr_b16 a[184:185], v7 offset:0\n"
"v_mfma_f32_16x16x32_bf16 a[248:251], a[172:175], a[88:91], a[248:251]\n"
"s_add_u32 s52, s10, .LATT_ENTRY-.LK_START\n"
"buffer_load_dword a128, off, s[12:15], s52\n"
"v_mfma_f32_16x16x32_bf16 a[248:251], a[168:171], a[72:75], a[248:251]\n"
"ds_read_b64_tr_b16 a[186:187], v7 offset:4096\n"
"v_mfma_f32_16x16x32_bf16 a[244:247], a[168:171], a[68:71], a[244:247]\n"
"v_mfma_f32_16x16x32_bf16 a[244:247], a[172:175], a[84:87], a[244:247]\n"
"ds_read_b64_tr_b16 a[188:189], v7 offset:8192\n"
"v_mfma_f32_16x16x32_bf16 a[240:243], a[172:175], a[80:83], a[240:243]\n"
"v_mfma_f32_16x16x32_bf16 a[240:243], a[168:171], a[64:67], a[240:243]\n"
"s_waitcnt lgkmcnt(2)\n"
"ds_read_b64_tr_b16 a[190:191], v7 offset:12288\n"
"v_add_u32 v7, 0xfffde000, v7\n"
"v_mfma_f32_16x16x32_bf16 a[224:227], a[176:179], a[64:67], a[224:227]\n"
"v_mfma_f32_16x16x32_bf16 a[224:227], a[180:183], a[80:83], a[224:227]\n"
"s_add_u32 m0, s9, 50176\n"
"ds_read_b64_tr_b16 a[160:161], v8 offset:0\n"
"v_mfma_f32_16x16x32_bf16 a[228:231], a[180:183], a[84:87], a[228:231]\n"
"s_mov_b32 s52, 3145856\n"
"buffer_load_dwordx4 v2, s[24:27], s52 offen lds\n"
"v_mfma_f32_16x16x32_bf16 a[228:231], a[176:179], a[68:71], a[228:231]\n"
"ds_read_b64_tr_b16 a[162:163], v8 offset:4096\n"
"v_mfma_f32_16x16x32_bf16 a[232:235], a[176:179], a[72:75], a[232:235]\n"
"v_mfma_f32_16x16x32_bf16 a[232:235], a[180:183], a[88:91], a[232:235]\n"
"ds_read_b64_tr_b16 a[164:165], v8 offset:8192\n"
"v_mfma_f32_16x16x32_bf16 a[236:239], a[180:183], a[92:95], a[236:239]\n"
"v_mfma_f32_16x16x32_bf16 a[236:239], a[176:179], a[76:79], a[236:239]\n"
"s_waitcnt lgkmcnt(2)\n"
"ds_read_b64_tr_b16 a[166:167], v8 offset:12288\n"
"v_add_u32 v8, 0xfffde000, v8\n"
"v_mfma_f32_16x16x32_bf16 a[156:159], a[184:187], a[76:79], a[156:159]\n"
"v_mfma_f32_16x16x32_bf16 a[156:159], a[188:191], a[92:95], a[156:159]\n"
"s_add_u32 m0, s9, 49152\n"
"ds_read_b64_tr_b16 a[168:169], v5 offset:1024\n"
"v_mfma_f32_16x16x32_bf16 a[152:155], a[188:191], a[88:91], a[152:155]\n"
"s_mov_b32 s53, 3145728\n"
"buffer_load_dwordx4 v2, s[24:27], s53 offen lds\n"
"v_mfma_f32_16x16x32_bf16 a[152:155], a[184:187], a[72:75], a[152:155]\n"
"ds_read_b64_tr_b16 a[170:171], v5 offset:5120\n"
"v_mfma_f32_16x16x32_bf16 a[148:151], a[184:187], a[68:71], a[148:151]\n"
"v_mfma_f32_16x16x32_bf16 a[148:151], a[188:191], a[84:87], a[148:151]\n"
"ds_read_b64_tr_b16 a[172:173], v5 offset:9216\n"
"v_mfma_f32_16x16x32_bf16 a[144:147], a[188:191], a[80:83], a[144:147]\n"
"v_mfma_f32_16x16x32_bf16 a[144:147], a[184:187], a[64:67], a[144:147]\n"
"s_waitcnt lgkmcnt(2)\n"
"ds_read_b64_tr_b16 a[174:175], v5 offset:13312\n"
"v_add_u32 v5, 0xfffde000, v5\n"
"v_mfma_f32_16x16x32_bf16 v[160:163], a[160:163], a[64:67], v[160:163]\n"
"v_mfma_f32_16x16x32_bf16 v[160:163], a[164:167], a[80:83], v[160:163]\n"
"s_add_u32 m0, s9, 58368\n"
"ds_read_b64_tr_b16 a[176:177], v6 offset:1024\n"
"v_mfma_f32_16x16x32_bf16 v[164:167], a[164:167], a[84:87], v[164:167]\n"
"s_mov_b32 s52, 3408000\n"
"buffer_load_dwordx4 v2, s[24:27], s52 offen lds\n"
"v_mfma_f32_16x16x32_bf16 v[164:167], a[160:163], a[68:71], v[164:167]\n"
"ds_read_b64_tr_b16 a[178:179], v6 offset:5120\n"
"v_mfma_f32_16x16x32_bf16 v[168:171], a[160:163], a[72:75], v[168:171]\n"
"v_mfma_f32_16x16x32_bf16 v[168:171], a[164:167], a[88:91], v[168:171]\n"
"ds_read_b64_tr_b16 a[180:181], v6 offset:9216\n"
"v_mfma_f32_16x16x32_bf16 v[172:175], a[164:167], a[92:95], v[172:175]\n"
"v_mfma_f32_16x16x32_bf16 v[172:175], a[160:163], a[76:79], v[172:175]\n"
"s_waitcnt lgkmcnt(2)\n"
"ds_read_b64_tr_b16 a[182:183], v6 offset:13312\n"
"v_add_u32 v6, 0xfffde000, v6\n"
"v_mfma_f32_16x16x32_bf16 v[188:191], a[168:171], a[76:79], v[188:191]\n"
"v_mfma_f32_16x16x32_bf16 v[188:191], a[172:175], a[92:95], v[188:191]\n"
"s_add_u32 m0, s9, 57344\n"
"ds_read_b64_tr_b16 a[184:185], v11 offset:16384\n"
"v_mfma_f32_16x16x32_bf16 v[184:187], a[172:175], a[88:91], v[184:187]\n"
"s_mov_b32 s53, 3407872\n"
"buffer_load_dwordx4 v2, s[24:27], s53 offen lds\n"
"v_mfma_f32_16x16x32_bf16 v[184:187], a[168:171], a[72:75], v[184:187]\n"
"ds_read_b64_tr_b16 a[186:187], v11 offset:17408\n"
"v_mfma_f32_16x16x32_bf16 v[180:183], a[168:171], a[68:71], v[180:183]\n"
"v_mfma_f32_16x16x32_bf16 v[180:183], a[172:175], a[84:87], v[180:183]\n"
"s_add_u32 m0, s9, 66560\n"
"ds_read_b64_tr_b16 a[188:189], v11 offset:18432\n"
"v_mfma_f32_16x16x32_bf16 v[176:179], a[172:175], a[80:83], v[176:179]\n"
"s_mov_b32 s52, 3670144\n"
"buffer_load_dwordx4 v2, s[24:27], s52 offen lds\n"
"v_mfma_f32_16x16x32_bf16 v[176:179], a[168:171], a[64:67], v[176:179]\n"
"s_waitcnt lgkmcnt(2)\n"
"ds_read_b64_tr_b16 a[190:191], v11 offset:19456\n"
"v_add_u32 v11, 0xfffde000, v11\n"
"v_mfma_f32_16x16x32_bf16 v[192:195], a[176:179], a[64:67], v[192:195]\n"
".LPAD3:\n"
"s_cbranch_execz .LPAD6\n"
"v_mfma_f32_16x16x32_bf16 v[192:195], a[180:183], a[80:83], v[192:195]\n"
"ds_read_b64_tr_b16 a[160:161], v9 offset:0\n"
"v_mfma_f32_16x16x32_bf16 v[196:199], a[180:183], a[84:87], v[196:199]\n"
"v_mfma_f32_16x16x32_bf16 v[196:199], a[176:179], a[68:71], v[196:199]\n"
"ds_read_b64_tr_b16 a[162:163], v9 offset:4096\n"
"v_mfma_f32_16x16x32_bf16 v[200:203], a[176:179], a[72:75], v[200:203]\n"
"v_mfma_f32_16x16x32_bf16 v[200:203], a[180:183], a[88:91], v[200:203]\n"
"s_add_u32 m0, s9, 65536\n"
"ds_read_b64_tr_b16 a[164:165], v9 offset:8192\n"
"v_mfma_f32_16x16x32_bf16 v[204:207], a[180:183], a[92:95], v[204:207]\n"
"s_mov_b32 s53, 3670016\n"
"buffer_load_dwordx4 v2, s[24:27], s53 offen lds\n"
"v_mfma_f32_16x16x32_bf16 v[204:207], a[176:179], a[76:79], v[204:207]\n"
"s_waitcnt lgkmcnt(2)\n"
"ds_read_b64_tr_b16 a[166:167], v9 offset:12288\n"
"v_mfma_f32_16x16x32_bf16 v[220:223], a[184:187], a[76:79], v[220:223]\n"
"v_mfma_f32_16x16x32_bf16 v[220:223], a[188:191], a[92:95], v[220:223]\n"
"ds_read_b64_tr_b16 a[168:169], v9 offset:8\n"
"v_mfma_f32_16x16x32_bf16 v[216:219], a[188:191], a[88:91], v[216:219]\n"
"v_mfma_f32_16x16x32_bf16 v[216:219], a[184:187], a[72:75], v[216:219]\n"
"ds_read_b64_tr_b16 a[170:171], v9 offset:4104\n"
"v_mfma_f32_16x16x32_bf16 v[212:215], a[184:187], a[68:71], v[212:215]\n"
"v_mfma_f32_16x16x32_bf16 v[212:215], a[188:191], a[84:87], v[212:215]\n"
"s_add_u32 m0, s9, 74752\n"
"ds_read_b64_tr_b16 a[172:173], v9 offset:8200\n"
"v_mfma_f32_16x16x32_bf16 v[208:211], a[188:191], a[80:83], v[208:211]\n"
"s_mov_b32 s52, 3932288\n"
"buffer_load_dwordx4 v2, s[24:27], s52 offen lds\n"
"v_mfma_f32_16x16x32_bf16 v[208:211], a[184:187], a[64:67], v[208:211]\n"
"s_waitcnt lgkmcnt(2)\n"
"ds_read_b64_tr_b16 a[174:175], v9 offset:12296\n"
"v_mfma_f32_16x16x32_bf16 v[32:35], a[160:163], a[0:3], v[32:35]\n"
"v_mfma_f32_16x16x32_bf16 v[32:35], a[164:167], a[16:19], v[32:35]\n"
"ds_read_b64_tr_b16 a[176:177], v10 offset:0\n"
"v_mfma_f32_16x16x32_bf16 v[36:39], a[164:167], a[20:23], v[36:39]\n"
"v_mfma_f32_16x16x32_bf16 v[36:39], a[160:163], a[4:7], v[36:39]\n"
"ds_read_b64_tr_b16 a[178:179], v10 offset:4096\n"
"v_mfma_f32_16x16x32_bf16 v[40:43], a[160:163], a[8:11], v[40:43]\n"
"v_mfma_f32_16x16x32_bf16 v[40:43], a[164:167], a[24:27], v[40:43]\n"
"s_add_u32 m0, s9, 73728\n"
"ds_read_b64_tr_b16 a[180:181], v10 offset:8192\n"
"v_mfma_f32_16x16x32_bf16 v[44:47], a[164:167], a[28:31], v[44:47]\n"
"s_mov_b32 s53, 3932160\n"
"buffer_load_dwordx4 v2, s[24:27], s53 offen lds\n"
"v_mfma_f32_16x16x32_bf16 v[44:47], a[160:163], a[12:15], v[44:47]\n"
"s_waitcnt lgkmcnt(2)\n"
"ds_read_b64_tr_b16 a[182:183], v10 offset:12288\n"
"v_mfma_f32_16x16x32_bf16 v[60:63], a[168:171], a[12:15], v[60:63]\n"
"v_mfma_f32_16x16x32_bf16 v[60:63], a[172:175], a[28:31], v[60:63]\n"
"ds_read_b64_tr_b16 a[184:185], v10 offset:8\n"
"v_mfma_f32_16x16x32_bf16 v[56:59], a[172:175], a[24:27], v[56:59]\n"
"v_mfma_f32_16x16x32_bf16 v[56:59], a[168:171], a[8:11], v[56:59]\n"
"ds_read_b64_tr_b16 a[186:187], v10 offset:4104\n"
"v_mfma_f32_16x16x32_bf16 v[52:55], a[168:171], a[4:7], v[52:55]\n"
"v_mfma_f32_16x16x32_bf16 v[52:55], a[172:175], a[20:23], v[52:55]\n"
"s_add_u32 m0, s9, 82944\n"
"ds_read_b64_tr_b16 a[188:189], v10 offset:8200\n"
"v_mfma_f32_16x16x32_bf16 v[48:51], a[172:175], a[16:19], v[48:51]\n"
"s_mov_b32 s52, 4194432\n"
"buffer_load_dwordx4 v2, s[24:27], s52 offen lds\n"
"v_mfma_f32_16x16x32_bf16 v[48:51], a[168:171], a[0:3], v[48:51]\n"
"s_waitcnt lgkmcnt(2)\n"
"ds_read_b64_tr_b16 a[190:191], v10 offset:12296\n"
"v_mfma_f32_16x16x32_bf16 v[64:67], a[176:179], a[0:3], v[64:67]\n"
"v_mfma_f32_16x16x32_bf16 v[64:67], a[180:183], a[16:19], v[64:67]\n"
"ds_read_b64_tr_b16 a[160:161], v9 offset:1024\n"
"v_mfma_f32_16x16x32_bf16 v[68:71], a[180:183], a[20:23], v[68:71]\n"
"v_mfma_f32_16x16x32_bf16 v[68:71], a[176:179], a[4:7], v[68:71]\n"
"ds_read_b64_tr_b16 a[162:163], v9 offset:5120\n"
"v_mfma_f32_16x16x32_bf16 v[72:75], a[176:179], a[8:11], v[72:75]\n"
"v_mfma_f32_16x16x32_bf16 v[72:75], a[180:183], a[24:27], v[72:75]\n"
"s_add_u32 m0, s9, 81920\n"
"ds_read_b64_tr_b16 a[164:165], v9 offset:9216\n"
"v_mfma_f32_16x16x32_bf16 v[76:79], a[180:183], a[28:31], v[76:79]\n"
"s_mov_b32 s53, 4194304\n"
"buffer_load_dwordx4 v2, s[24:27], s53 offen lds\n"
"v_mfma_f32_16x16x32_bf16 v[76:79], a[176:179], a[12:15], v[76:79]\n"
"s_waitcnt lgkmcnt(2)\n"
"ds_read_b64_tr_b16 a[166:167], v9 offset:13312\n"
"v_mfma_f32_16x16x32_bf16 v[92:95], a[184:187], a[12:15], v[92:95]\n"
"v_mfma_f32_16x16x32_bf16 v[92:95], a[188:191], a[28:31], v[92:95]\n"
"ds_read_b64_tr_b16 a[168:169], v9 offset:1032\n"
"v_mfma_f32_16x16x32_bf16 v[88:91], a[188:191], a[24:27], v[88:91]\n"
"v_mfma_f32_16x16x32_bf16 v[88:91], a[184:187], a[8:11], v[88:91]\n"
"ds_read_b64_tr_b16 a[170:171], v9 offset:5128\n"
"v_mfma_f32_16x16x32_bf16 v[84:87], a[184:187], a[4:7], v[84:87]\n"
"v_mfma_f32_16x16x32_bf16 v[84:87], a[188:191], a[20:23], v[84:87]\n"
"s_add_u32 m0, s9, 91136\n"
"ds_read_b64_tr_b16 a[172:173], v9 offset:9224\n"
"v_mfma_f32_16x16x32_bf16 v[80:83], a[188:191], a[16:19], v[80:83]\n"
"s_mov_b32 s52, 4456576\n"
"buffer_load_dwordx4 v2, s[24:27], s52 offen lds\n"
"v_mfma_f32_16x16x32_bf16 v[80:83], a[184:187], a[0:3], v[80:83]\n"
"s_waitcnt lgkmcnt(2)\n"
"ds_read_b64_tr_b16 a[174:175], v9 offset:13320\n"
"v_mfma_f32_16x16x32_bf16 v[96:99], a[160:163], a[0:3], v[96:99]\n"
"v_mfma_f32_16x16x32_bf16 v[96:99], a[164:167], a[16:19], v[96:99]\n"
"ds_read_b64_tr_b16 a[176:177], v10 offset:1024\n"
"v_mfma_f32_16x16x32_bf16 v[100:103], a[164:167], a[20:23], v[100:103]\n"
"v_mfma_f32_16x16x32_bf16 v[100:103], a[160:163], a[4:7], v[100:103]\n"
"ds_read_b64_tr_b16 a[178:179], v10 offset:5120\n"
"v_mfma_f32_16x16x32_bf16 v[104:107], a[160:163], a[8:11], v[104:107]\n"
"v_mfma_f32_16x16x32_bf16 v[104:107], a[164:167], a[24:27], v[104:107]\n"
"s_add_u32 m0, s9, 90112\n"
"ds_read_b64_tr_b16 a[180:181], v10 offset:9216\n"
"v_mfma_f32_16x16x32_bf16 v[108:111], a[164:167], a[28:31], v[108:111]\n"
"s_mov_b32 s53, 4456448\n"
"buffer_load_dwordx4 v2, s[24:27], s53 offen lds\n"
"v_mfma_f32_16x16x32_bf16 v[108:111], a[160:163], a[12:15], v[108:111]\n"
"s_waitcnt lgkmcnt(2)\n"
"ds_read_b64_tr_b16 a[182:183], v10 offset:13312\n"
"v_mfma_f32_16x16x32_bf16 v[124:127], a[168:171], a[12:15], v[124:127]\n"
"s_and_b32 s54, s63, 7\n"
"v_mfma_f32_16x16x32_bf16 v[124:127], a[172:175], a[28:31], v[124:127]\n"
"s_lshl_b32 s54, s54, 5\n"
"ds_read_b64_tr_b16 a[184:185], v10 offset:1032\n"
"v_mfma_f32_16x16x32_bf16 v[120:123], a[172:175], a[24:27], v[120:123]\n"
"s_add_u32 s8, s54, s82\n"
"v_mfma_f32_16x16x32_bf16 v[120:123], a[168:171], a[8:11], v[120:123]\n"
"s_add_u32 s44, s80, 0\n"
"ds_read_b64_tr_b16 a[186:187], v10 offset:5128\n"
"v_mfma_f32_16x16x32_bf16 v[116:119], a[168:171], a[4:7], v[116:119]\n"
"s_addc_u32 s45, s81, 0\n"
"v_mfma_f32_16x16x32_bf16 v[116:119], a[172:175], a[20:23], v[116:119]\n"
"s_mov_b32 s46, 0\n"
"ds_read_b64_tr_b16 a[188:189], v10 offset:9224\n"
"v_mfma_f32_16x16x32_bf16 v[112:115], a[172:175], a[16:19], v[112:115]\n"
"s_mov_b32 s47, 0x20000\n"
"v_mfma_f32_16x16x32_bf16 v[112:115], a[168:171], a[0:3], v[112:115]\n"
"s_waitcnt lgkmcnt(2)\n"
"ds_read_b64_tr_b16 a[190:191], v10 offset:13320\n"
"v_mfma_f32_16x16x32_bf16 v[128:131], a[176:179], a[0:3], v[128:131]\n"
"s_waitcnt vmcnt(8)\n"
"s_barrier\n"
"v_mfma_f32_16x16x32_bf16 v[128:131], a[180:183], a[16:19], v[128:131]\n"
"s_mul_i32 s54, s58, 2506752\n"
"ds_read_b64_tr_b16 a[160:161], v7 offset:1024\n"
"v_mfma_f32_16x16x32_bf16 v[132:135], a[180:183], a[20:23], v[132:135]\n"
"s_add_u32 s54, s54, 41943040\n"
"v_mfma_f32_16x16x32_bf16 v[132:135], a[176:179], a[4:7], v[132:135]\n"
"s_add_u32 s12, s80, s54\n"
"ds_read_b64_tr_b16 a[162:163], v7 offset:5120\n"
"v_mfma_f32_16x16x32_bf16 v[136:139], a[176:179], a[8:11], v[136:139]\n"
"s_addc_u32 s13, s81, 0\n"
"v_mfma_f32_16x16x32_bf16 v[136:139], a[180:183], a[24:27], v[136:139]\n"
"s_mov_b32 s14, 0\n"
"ds_read_b64_tr_b16 a[164:165], v7 offset:9216\n"
"v_mfma_f32_16x16x32_bf16 v[140:143], a[180:183], a[28:31], v[140:143]\n"
"s_mov_b32 s15, 0x20000\n"
"v_mfma_f32_16x16x32_bf16 v[140:143], a[176:179], a[12:15], v[140:143]\n"
"s_waitcnt lgkmcnt(2)\n"
"ds_read_b64_tr_b16 a[166:167], v7 offset:13312\n"
"v_mfma_f32_16x16x32_bf16 v[156:159], a[184:187], a[12:15], v[156:159]\n"
"s_mov_b32 s84, 0\n"
"v_mfma_f32_16x16x32_bf16 v[156:159], a[188:191], a[28:31], v[156:159]\n"
"s_mov_b32 s85, 0\n"
"ds_read_b64_tr_b16 a[168:169], v8 offset:1024\n"
"v_mfma_f32_16x16x32_bf16 v[152:155], a[188:191], a[24:27], v[152:155]\n"
"s_lshl_b32 s74, s83, 14\n"
"v_mfma_f32_16x16x32_bf16 v[152:155], a[184:187], a[8:11], v[152:155]\n"
"s_add_u32 s74, s74, 98304\n"
"ds_read_b64_tr_b16 a[170:171], v8 offset:5120\n"
"v_mfma_f32_16x16x32_bf16 v[148:151], a[184:187], a[4:7], v[148:151]\n"
"s_add_u32 s49, s56, 557056\n"
"v_mfma_f32_16x16x32_bf16 v[148:151], a[188:191], a[20:23], v[148:151]\n"
"s_lshl_b32 s54, s59, 3\n"
"ds_read_b64_tr_b16 a[172:173], v8 offset:9216\n"
"v_mfma_f32_16x16x32_bf16 v[144:147], a[188:191], a[16:19], v[144:147]\n"
"s_and_b32 s55, s63, 7\n"
"v_mfma_f32_16x16x32_bf16 v[144:147], a[184:187], a[0:3], v[144:147]\n"
"s_waitcnt vmcnt(13) lgkmcnt(2)\n"
"ds_read_b64_tr_b16 a[174:175], v8 offset:13312\n"
"v_mfma_f32_16x16x32_bf16 v[224:227], a[160:163], a[192:195], v[224:227]\n"
"s_add_u32 s54, s54, s55\n"
"v_mfma_f32_16x16x32_bf16 v[224:227], a[164:167], a[208:211], v[224:227]\n"
"s_lshl_b32 s54, s54, 4\n"
"ds_read_b64_tr_b16 a[176:177], v5 offset:0\n"
"v_mfma_f32_16x16x32_bf16 v[228:231], a[164:167], a[212:215], v[228:231]\n"
"s_add_u32 s54, s54, s83\n"
"v_mfma_f32_16x16x32_bf16 v[228:231], a[160:163], a[196:199], v[228:231]\n"
"s_mul_i32 s54, s54, 13824\n"
"ds_read_b64_tr_b16 a[178:179], v5 offset:4096\n"
"v_mfma_f32_16x16x32_bf16 v[232:235], a[160:163], a[200:203], v[232:235]\n"
"s_add_u32 s54, s54, 54525952\n"
"v_mfma_f32_16x16x32_bf16 v[232:235], a[164:167], a[216:219], v[232:235]\n"
"s_add_u32 s20, s80, s54\n"
"ds_read_b64_tr_b16 a[180:181], v5 offset:8192\n"
"v_mfma_f32_16x16x32_bf16 v[236:239], a[164:167], a[220:223], v[236:239]\n"
"s_addc_u32 s21, s81, 0\n"
"v_mfma_f32_16x16x32_bf16 v[236:239], a[160:163], a[204:207], v[236:239]\n"
"s_waitcnt lgkmcnt(2)\n"
"ds_read_b64_tr_b16 a[182:183], v5 offset:12288\n"
"v_mfma_f32_16x16x32_bf16 v[252:255], a[168:171], a[204:207], v[252:255]\n"
"s_mov_b32 s22, 179712\n"
"v_mfma_f32_16x16x32_bf16 v[252:255], a[172:175], a[220:223], v[252:255]\n"
"s_mov_b32 s23, 0x20000\n"
"ds_read_b64_tr_b16 a[184:185], v6 offset:0\n"
"v_mfma_f32_16x16x32_bf16 v[248:251], a[172:175], a[216:219], v[248:251]\n"
"s_mul_i32 s66, s58, 55296\n"
"v_mfma_f32_16x16x32_bf16 v[248:251], a[168:171], a[200:203], v[248:251]\n"
"s_add_u32 s22, s66, 13824\n"
"ds_read_b64_tr_b16 a[186:187], v6 offset:4096\n"
"v_mfma_f32_16x16x32_bf16 v[244:247], a[168:171], a[196:199], v[244:247]\n"
"s_add_u32 s24, s80, 3145728\n"
"v_mfma_f32_16x16x32_bf16 v[244:247], a[172:175], a[212:215], v[244:247]\n"
"s_addc_u32 s25, s81, 0\n"
"ds_read_b64_tr_b16 a[188:189], v6 offset:8192\n"
"v_mfma_f32_16x16x32_bf16 v[240:243], a[172:175], a[208:211], v[240:243]\n"
"s_mov_b32 s26, 196608\n"
"v_mfma_f32_16x16x32_bf16 v[240:243], a[168:171], a[192:195], v[240:243]\n"
"s_waitcnt lgkmcnt(2)\n"
"ds_read_b64_tr_b16 a[190:191], v6 offset:12288\n"
"v_mfma_f32_16x16x32_bf16 a[240:243], a[176:179], a[192:195], a[240:243]\n"
"s_mov_b32 s27, 0x20000\n"
"v_mfma_f32_16x16x32_bf16 a[240:243], a[180:183], a[208:211], a[240:243]\n"
"s_lshl2_add_u32 s54, s59, s58\n"
"ds_read_b64_tr_b16 a[160:161], v7 offset:0\n"
"v_mfma_f32_16x16x32_bf16 a[244:247], a[180:183], a[212:215], a[244:247]\n"
"s_lshl_b32 s54, s54, 11\n"
"ds_read_b64_tr_b16 a[162:163], v7 offset:4096\n"
"v_mfma_f32_16x16x32_bf16 a[244:247], a[176:179], a[196:199], a[244:247]\n"
"s_cmp_eq_u32 s83, 0\n"
"v_mfma_f32_16x16x32_bf16 a[248:251], a[176:179], a[200:203], a[248:251]\n"
"s_cselect_b32 s75, s54, 2146435072\n"
"v_mfma_f32_16x16x32_bf16 a[248:251], a[180:183], a[216:219], a[248:251]\n"
"s_lshl_b32 s54, s58, 2\n"
"ds_read_b64_tr_b16 a[164:165], v7 offset:8192\n"
"v_cvt_pk_bf16_f32 v224, v224, v225\n"
"v_mfma_f32_16x16x32_bf16 a[252:255], a[180:183], a[220:223], a[252:255]\n"
"s_add_u32 s54, s54, s61\n"
"ds_read_b64_tr_b16 a[166:167], v7 offset:12288\n"
"v_cvt_pk_bf16_f32 v225, v226, v227\n"
"v_mfma_f32_16x16x32_bf16 a[252:255], a[176:179], a[204:207], a[252:255]\n"
"s_waitcnt lgkmcnt(3)\n"
"v_cvt_pk_bf16_f32 v226, v240, v241\n"
"v_cvt_pk_bf16_f32 v227, v242, v243\n"
"v_mfma_f32_16x16x32_bf16 a[236:239], a[184:187], a[204:207], a[236:239]\n"
"s_mul_i32 s54, s54, 626688\n"
"v_pk_max_i16 v224, v224, 0\n"
"v_pk_max_i16 v225, v225, 0\n"
"v_mfma_f32_16x16x32_bf16 a[236:239], a[188:191], a[220:223], a[236:239]\n"
"s_add_u32 s54, s54, 41943040\n"
"ds_read_b64_tr_b16 a[168:169], v8 offset:0\n"
"v_pk_max_i16 v226, v226, 0\n"
"v_mfma_f32_16x16x32_bf16 a[232:235], a[188:191], a[216:219], a[232:235]\n"
"s_add_u32 s28, s80, s54\n"
"ds_read_b64_tr_b16 a[170:171], v8 offset:4096\n"
"v_pk_max_i16 v227, v227, 0\n"
"v_mfma_f32_16x16x32_bf16 a[232:235], a[184:187], a[200:203], a[232:235]\n"
"s_addc_u32 s29, s81, 0\n"
"ds_write_b64 v3, v[224:225] offset:12288\n"
"v_mfma_f32_16x16x32_bf16 a[228:231], a[184:187], a[196:199], a[228:231]\n"
"s_mov_b32 s30, 626688\n"
"v_cvt_pk_bf16_f32 v228, v228, v229\n"
"v_cvt_pk_bf16_f32 v229, v230, v231\n"
"v_mfma_f32_16x16x32_bf16 a[228:231], a[188:191], a[212:215], a[228:231]\n"
"s_mov_b32 s31, 0x20000\n"
"ds_read_b64_tr_b16 a[172:173], v8 offset:8192\n"
"v_cvt_pk_bf16_f32 v230, v244, v245\n"
"v_mfma_f32_16x16x32_bf16 a[224:227], a[188:191], a[208:211], a[224:227]\n"
"ds_read_b64_tr_b16 a[174:175], v8 offset:12288\n"
"v_cvt_pk_bf16_f32 v231, v246, v247\n"
"v_mfma_f32_16x16x32_bf16 a[224:227], a[184:187], a[192:195], a[224:227]\n"
"s_waitcnt lgkmcnt(4)\n"
"ds_write_b64 v3, v[226:227] offset:12296\n"
"v_mfma_f32_16x16x32_bf16 a[144:147], a[160:163], a[192:195], a[144:147]\n"
"v_pk_max_i16 v228, v228, 0\n"
"v_pk_max_i16 v229, v229, 0\n"
"v_mfma_f32_16x16x32_bf16 a[144:147], a[164:167], a[208:211], a[144:147]\n"
"ds_read_b64_tr_b16 a[176:177], v5 offset:1024\n"
"v_pk_max_i16 v230, v230, 0\n"
"v_mfma_f32_16x16x32_bf16 a[148:151], a[164:167], a[212:215], a[148:151]\n"
"ds_read_b64_tr_b16 a[178:179], v5 offset:5120\n"
"v_pk_max_i16 v231, v231, 0\n"
"v_mfma_f32_16x16x32_bf16 a[148:151], a[160:163], a[196:199], a[148:151]\n"
"ds_write_b64 v3, v[228:229] offset:13312\n"
"v_mfma_f32_16x16x32_bf16 a[152:155], a[160:163], a[200:203], a[152:155]\n"
"v_cvt_pk_bf16_f32 v232, v232, v233\n"
"v_cvt_pk_bf16_f32 v233, v234, v235\n"
"v_mfma_f32_16x16x32_bf16 a[152:155], a[164:167], a[216:219], a[152:155]\n"
"ds_read_b64_tr_b16 a[180:181], v5 offset:9216\n"
"v_cvt_pk_bf16_f32 v234, v248, v249\n"
"v_mfma_f32_16x16x32_bf16 a[156:159], a[164:167], a[220:223], a[156:159]\n"
"ds_read_b64_tr_b16 a[182:183], v5 offset:13312\n"
"v_cvt_pk_bf16_f32 v235, v250, v251\n"
"v_mfma_f32_16x16x32_bf16 a[156:159], a[160:163], a[204:207], a[156:159]\n"
"s_waitcnt lgkmcnt(4)\n"
"ds_write_b64 v3, v[230:231] offset:13320\n"
"v_mfma_f32_16x16x32_bf16 v[172:175], a[168:171], a[204:207], v[172:175]\n"
"v_pk_max_i16 v232, v232, 0\n"
"v_pk_max_i16 v233, v233, 0\n"
"v_mfma_f32_16x16x32_bf16 v[172:175], a[172:175], a[220:223], v[172:175]\n"
"ds_read_b64_tr_b16 a[184:185], v6 offset:1024\n"
"v_pk_max_i16 v234, v234, 0\n"
"v_mfma_f32_16x16x32_bf16 v[168:171], a[172:175], a[216:219], v[168:171]\n"
"ds_read_b64_tr_b16 a[186:187], v6 offset:5120\n"
"v_pk_max_i16 v235, v235, 0\n"
"v_mfma_f32_16x16x32_bf16 v[168:171], a[168:171], a[200:203], v[168:171]\n"
"ds_write_b64 v3, v[232:233] offset:14336\n"
"v_mfma_f32_16x16x32_bf16 v[164:167], a[168:171], a[196:199], v[164:167]\n"
"v_cvt_pk_bf16_f32 v236, v236, v237\n"
"v_cvt_pk_bf16_f32 v237, v238, v239\n"
"v_mfma_f32_16x16x32_bf16 v[164:167], a[172:175], a[212:215], v[164:167]\n"
"ds_read_b64_tr_b16 a[188:189], v6 offset:9216\n"
"v_cvt_pk_bf16_f32 v238, v252, v253\n"
"v_mfma_f32_16x16x32_bf16 v[160:163], a[172:175], a[208:211], v[160:163]\n"
"ds_read_b64_tr_b16 a[190:191], v6 offset:13312\n"
"v_cvt_pk_bf16_f32 v239, v254, v255\n"
"v_mfma_f32_16x16x32_bf16 v[160:163], a[168:171], a[192:195], v[160:163]\n"
"s_waitcnt lgkmcnt(4)\n"
"ds_write_b64 v3, v[234:235] offset:14344\n"
"v_mfma_f32_16x16x32_bf16 v[176:179], a[176:179], a[192:195], v[176:179]\n"
"v_pk_max_i16 v236, v236, 0\n"
"v_pk_max_i16 v237, v237, 0\n"
"v_mfma_f32_16x16x32_bf16 v[176:179], a[180:183], a[208:211], v[176:179]\n"
"ds_read_b64_tr_b16 a[160:161], v11 offset:16384\n"
"v_pk_max_i16 v238, v238, 0\n"
"v_mfma_f32_16x16x32_bf16 v[180:183], a[180:183], a[212:215], v[180:183]\n"
"ds_read_b64_tr_b16 a[162:163], v11 offset:17408\n"
"v_pk_max_i16 v239, v239, 0\n"
"v_mfma_f32_16x16x32_bf16 v[180:183], a[176:179], a[196:199], v[180:183]\n"
"ds_write_b64 v3, v[236:237] offset:15360\n"
"v_mfma_f32_16x16x32_bf16 v[184:187], a[176:179], a[200:203], v[184:187]\n"
"v_accvgpr_read_b32 v240, a240\n"
"v_accvgpr_read_b32 v241, a241\n"
"v_mfma_f32_16x16x32_bf16 v[184:187], a[180:183], a[216:219], v[184:187]\n"
"ds_read_b64_tr_b16 a[164:165], v11 offset:18432\n"
"v_accvgpr_read_b32 v242, a242\n"
"v_mfma_f32_16x16x32_bf16 v[188:191], a[180:183], a[220:223], v[188:191]\n"
"ds_read_b64_tr_b16 a[166:167], v11 offset:19456\n"
"v_accvgpr_read_b32 v243, a243\n"
"v_mfma_f32_16x16x32_bf16 v[188:191], a[176:179], a[204:207], v[188:191]\n"
"s_waitcnt lgkmcnt(4)\n"
"ds_write_b64 v3, v[238:239] offset:15368\n"
"v_mfma_f32_16x16x32_bf16 v[204:207], a[184:187], a[204:207], v[204:207]\n"
"v_accvgpr_read_b32 v244, a224\n"
"v_accvgpr_read_b32 v245, a225\n"
"v_mfma_f32_16x16x32_bf16 v[204:207], a[188:191], a[220:223], v[204:207]\n"
"ds_read_b64_tr_b16 a[168:169], v9 offset:16384\n"
"v_accvgpr_read_b32 v246, a226\n"
"v_mfma_f32_16x16x32_bf16 v[200:203], a[188:191], a[216:219], v[200:203]\n"
"ds_read_b64_tr_b16 a[170:171], v9 offset:20480\n"
"v_accvgpr_read_b32 v247, a227\n"
"v_mfma_f32_16x16x32_bf16 v[200:203], a[184:187], a[200:203], v[200:203]\n"
"v_cvt_pk_bf16_f32 v240, v240, v241\n"
"v_cvt_pk_bf16_f32 v241, v242, v243\n"
"v_mfma_f32_16x16x32_bf16 v[196:199], a[184:187], a[196:199], v[196:199]\n"
"v_cvt_pk_bf16_f32 v242, v244, v245\n"
"v_cvt_pk_bf16_f32 v243, v246, v247\n"
"v_mfma_f32_16x16x32_bf16 v[196:199], a[188:191], a[212:215], v[196:199]\n"
"ds_read_b64_tr_b16 a[172:173], v9 offset:24576\n"
"v_pk_max_i16 v240, v240, 0\n"
"v_mfma_f32_16x16x32_bf16 v[192:195], a[188:191], a[208:211], v[192:195]\n"
"ds_read_b64_tr_b16 a[174:175], v9 offset:28672\n"
"v_pk_max_i16 v241, v241, 0\n"
"v_mfma_f32_16x16x32_bf16 v[192:195], a[184:187], a[192:195], v[192:195]\n"
"s_waitcnt lgkmcnt(3)\n"
"v_pk_max_i16 v242, v242, 0\n"
"v_pk_max_i16 v243, v243, 0\n"
"v_mfma_f32_16x16x32_bf16 v[208:211], a[160:163], a[192:195], v[208:211]\n"
"v_accvgpr_write_b32 a240, v240\n"
"v_accvgpr_write_b32 a241, v241\n"
"v_mfma_f32_16x16x32_bf16 v[208:211], a[164:167], a[208:211], v[208:211]\n"
"ds_read_b64_tr_b16 a[176:177], v9 offset:16392\n"
"v_accvgpr_write_b32 a242, v242\n"
"v_mfma_f32_16x16x32_bf16 v[212:215], a[164:167], a[212:215], v[212:215]\n"
"ds_read_b64_tr_b16 a[178:179], v9 offset:20488\n"
"v_accvgpr_write_b32 a243, v243\n"
"v_mfma_f32_16x16x32_bf16 v[212:215], a[160:163], a[196:199], v[212:215]\n"
"v_accvgpr_read_b32 v248, a244\n"
"v_accvgpr_read_b32 v249, a245\n"
"v_mfma_f32_16x16x32_bf16 v[216:219], a[160:163], a[200:203], v[216:219]\n"
"v_accvgpr_read_b32 v250, a246\n"
"v_accvgpr_read_b32 v251, a247\n"
"v_mfma_f32_16x16x32_bf16 v[216:219], a[164:167], a[216:219], v[216:219]\n"
"ds_read_b64_tr_b16 a[180:181], v9 offset:24584\n"
"v_accvgpr_read_b32 v252, a228\n"
"v_mfma_f32_16x16x32_bf16 v[220:223], a[164:167], a[220:223], v[220:223]\n"
"ds_read_b64_tr_b16 a[182:183], v9 offset:28680\n"
"v_accvgpr_read_b32 v253, a229\n"
"v_mfma_f32_16x16x32_bf16 v[220:223], a[160:163], a[204:207], v[220:223]\n"
"s_waitcnt lgkmcnt(3)\n"
"v_accvgpr_read_b32 v254, a230\n"
"v_accvgpr_read_b32 v255, a231\n"
"v_mfma_f32_16x16x32_bf16 v[44:47], a[168:171], a[44:47], v[44:47]\n"
"v_cvt_pk_bf16_f32 v248, v248, v249\n"
"v_cvt_pk_bf16_f32 v249, v250, v251\n"
"v_mfma_f32_16x16x32_bf16 v[44:47], a[172:175], a[60:63], v[44:47]\n"
"ds_read_b64_tr_b16 a[184:185], v10 offset:16384\n"
"v_cvt_pk_bf16_f32 v250, v252, v253\n"
"v_mfma_f32_16x16x32_bf16 v[40:43], a[172:175], a[56:59], v[40:43]\n"
"ds_read_b64_tr_b16 a[186:187], v10 offset:20480\n"
"v_cvt_pk_bf16_f32 v251, v254, v255\n"
"v_mfma_f32_16x16x32_bf16 v[40:43], a[168:171], a[40:43], v[40:43]\n"
"v_pk_max_i16 v248, v248, 0\n"
"v_pk_max_i16 v249, v249, 0\n"
"v_mfma_f32_16x16x32_bf16 v[36:39], a[168:171], a[36:39], v[36:39]\n"
"v_pk_max_i16 v250, v250, 0\n"
"v_pk_max_i16 v251, v251, 0\n"
"v_mfma_f32_16x16x32_bf16 v[36:39], a[172:175], a[52:55], v[36:39]\n"
"ds_read_b64_tr_b16 a[188:189], v10 offset:24576\n"
"v_accvgpr_write_b32 a244, v248\n"
"v_mfma_f32_16x16x32_bf16 v[32:35], a[172:175], a[48:51], v[32:35]\n"
"ds_read_b64_tr_b16 a[190:191], v10 offset:28672\n"
"v_accvgpr_write_b32 a245, v249\n"
"v_mfma_f32_16x16x32_bf16 v[32:35], a[168:171], a[32:35], v[32:35]\n"
"s_waitcnt lgkmcnt(3)\n"
"v_accvgpr_write_b32 a246, v250\n"
"v_accvgpr_write_b32 a247, v251\n"
"v_mfma_f32_16x16x32_bf16 v[48:51], a[176:179], a[32:35], v[48:51]\n"
"v_accvgpr_read_b32 v240, a248\n"
"v_accvgpr_read_b32 v241, a249\n"
"v_mfma_f32_16x16x32_bf16 v[48:51], a[180:183], a[48:51], v[48:51]\n"
"ds_read_b64_tr_b16 a[160:161], v10 offset:16392\n"
"v_accvgpr_read_b32 v242, a250\n"
"v_mfma_f32_16x16x32_bf16 v[52:55], a[180:183], a[52:55], v[52:55]\n"
"ds_read_b64_tr_b16 a[162:163], v10 offset:20488\n"
"v_accvgpr_read_b32 v243, a251\n"
"v_mfma_f32_16x16x32_bf16 v[52:55], a[176:179], a[36:39], v[52:55]\n"
"v_accvgpr_read_b32 v244, a232\n"
"v_accvgpr_read_b32 v245, a233\n"
"v_mfma_f32_16x16x32_bf16 v[56:59], a[176:179], a[40:43], v[56:59]\n"
"v_accvgpr_read_b32 v246, a234\n"
"v_accvgpr_read_b32 v247, a235\n"
"v_mfma_f32_16x16x32_bf16 v[56:59], a[180:183], a[56:59], v[56:59]\n"
"ds_read_b64_tr_b16 a[164:165], v10 offset:24584\n"
"v_cvt_pk_bf16_f32 v240, v240, v241\n"
"v_mfma_f32_16x16x32_bf16 v[60:63], a[180:183], a[60:63], v[60:63]\n"
"ds_read_b64_tr_b16 a[166:167], v10 offset:28680\n"
"v_cvt_pk_bf16_f32 v241, v242, v243\n"
"v_mfma_f32_16x16x32_bf16 v[60:63], a[176:179], a[44:47], v[60:63]\n"
"s_waitcnt lgkmcnt(3)\n"
"v_cvt_pk_bf16_f32 v242, v244, v245\n"
"v_cvt_pk_bf16_f32 v243, v246, v247\n"
"v_mfma_f32_16x16x32_bf16 v[76:79], a[184:187], a[44:47], v[76:79]\n"
"v_pk_max_i16 v240, v240, 0\n"
"v_pk_max_i16 v241, v241, 0\n"
"v_mfma_f32_16x16x32_bf16 v[76:79], a[188:191], a[60:63], v[76:79]\n"
"ds_read_b64_tr_b16 a[168:169], v9 offset:17408\n"
"v_pk_max_i16 v242, v242, 0\n"
"v_mfma_f32_16x16x32_bf16 v[72:75], a[188:191], a[56:59], v[72:75]\n"
"ds_read_b64_tr_b16 a[170:171], v9 offset:21504\n"
"v_pk_max_i16 v243, v243, 0\n"
"v_mfma_f32_16x16x32_bf16 v[72:75], a[184:187], a[40:43], v[72:75]\n"
"v_accvgpr_write_b32 a248, v240\n"
"v_accvgpr_write_b32 a249, v241\n"
"v_mfma_f32_16x16x32_bf16 v[68:71], a[184:187], a[36:39], v[68:71]\n"
"v_accvgpr_write_b32 a250, v242\n"
"v_accvgpr_write_b32 a251, v243\n"
"v_mfma_f32_16x16x32_bf16 v[68:71], a[188:191], a[52:55], v[68:71]\n"
"ds_read_b64_tr_b16 a[172:173], v9 offset:25600\n"
"v_accvgpr_read_b32 v248, a252\n"
"v_mfma_f32_16x16x32_bf16 v[64:67], a[188:191], a[48:51], v[64:67]\n"
"ds_read_b64_tr_b16 a[174:175], v9 offset:29696\n"
"v_accvgpr_read_b32 v249, a253\n"
"v_mfma_f32_16x16x32_bf16 v[64:67], a[184:187], a[32:35], v[64:67]\n"
"s_waitcnt lgkmcnt(3)\n"
"v_accvgpr_read_b32 v250, a254\n"
"v_accvgpr_read_b32 v251, a255\n"
"v_mfma_f32_16x16x32_bf16 v[80:83], a[160:163], a[32:35], v[80:83]\n"
"v_accvgpr_read_b32 v252, a236\n"
"v_accvgpr_read_b32 v253, a237\n"
"v_mfma_f32_16x16x32_bf16 v[80:83], a[164:167], a[48:51], v[80:83]\n"
"ds_read_b64_tr_b16 a[176:177], v9 offset:17416\n"
"v_accvgpr_read_b32 v254, a238\n"
"v_mfma_f32_16x16x32_bf16 v[84:87], a[164:167], a[52:55], v[84:87]\n"
"ds_read_b64_tr_b16 a[178:179], v9 offset:21512\n"
"v_accvgpr_read_b32 v255, a239\n"
"v_mfma_f32_16x16x32_bf16 v[84:87], a[160:163], a[36:39], v[84:87]\n"
"v_cvt_pk_bf16_f32 v248, v248, v249\n"
"v_cvt_pk_bf16_f32 v249, v250, v251\n"
"v_mfma_f32_16x16x32_bf16 v[88:91], a[160:163], a[40:43], v[88:91]\n"
"v_cvt_pk_bf16_f32 v250, v252, v253\n"
"v_cvt_pk_bf16_f32 v251, v254, v255\n"
"v_mfma_f32_16x16x32_bf16 v[88:91], a[164:167], a[56:59], v[88:91]\n"
"ds_read_b64_tr_b16 a[180:181], v9 offset:25608\n"
"v_pk_max_i16 v248, v248, 0\n"
"v_mfma_f32_16x16x32_bf16 v[92:95], a[164:167], a[60:63], v[92:95]\n"
"ds_read_b64_tr_b16 a[182:183], v9 offset:29704\n"
"v_pk_max_i16 v249, v249, 0\n"
"v_mfma_f32_16x16x32_bf16 v[92:95], a[160:163], a[44:47], v[92:95]\n"
"s_waitcnt lgkmcnt(3)\n"
"v_pk_max_i16 v250, v250, 0\n"
"v_pk_max_i16 v251, v251, 0\n"
"v_mfma_f32_16x16x32_bf16 v[108:111], a[168:171], a[44:47], v[108:111]\n"
"v_accvgpr_write_b32 a252, v248\n"
"v_accvgpr_write_b32 a253, v249\n"
"v_mfma_f32_16x16x32_bf16 v[108:111], a[172:175], a[60:63], v[108:111]\n"
"ds_read_b64_tr_b16 a[184:185], v10 offset:17408\n"
"v_accvgpr_write_b32 a254, v250\n"
"v_mfma_f32_16x16x32_bf16 v[104:107], a[172:175], a[56:59], v[104:107]\n"
"ds_read_b64_tr_b16 a[186:187], v10 offset:21504\n"
"v_accvgpr_write_b32 a255, v251\n"
"v_mfma_f32_16x16x32_bf16 v[104:107], a[168:171], a[40:43], v[104:107]\n"
"v_accvgpr_read_b32 v240, a144\n"
"v_accvgpr_read_b32 v241, a145\n"
"v_mfma_f32_16x16x32_bf16 v[100:103], a[168:171], a[36:39], v[100:103]\n"
"v_accvgpr_read_b32 v242, a146\n"
"v_accvgpr_read_b32 v243, a147\n"
"v_mfma_f32_16x16x32_bf16 v[100:103], a[172:175], a[52:55], v[100:103]\n"
"ds_read_b64_tr_b16 a[188:189], v10 offset:25600\n"
"v_cvt_pk_bf16_f32 v240, v240, v241\n"
"v_mfma_f32_16x16x32_bf16 v[96:99], a[172:175], a[48:51], v[96:99]\n"
"ds_read_b64_tr_b16 a[190:191], v10 offset:29696\n"
"v_cvt_pk_bf16_f32 v241, v242, v243\n"
"v_mfma_f32_16x16x32_bf16 v[96:99], a[168:171], a[32:35], v[96:99]\n"
"s_waitcnt lgkmcnt(3)\n"
"v_cvt_pk_bf16_f32 v242, v160, v161\n"
"v_cvt_pk_bf16_f32 v243, v162, v163\n"
"v_mfma_f32_16x16x32_bf16 v[112:115], a[176:179], a[32:35], v[112:115]\n"
"v_pk_max_i16 v240, v240, 0\n"
"v_pk_max_i16 v241, v241, 0\n"
"v_mfma_f32_16x16x32_bf16 v[112:115], a[180:183], a[48:51], v[112:115]\n"
"ds_read_b64_tr_b16 a[160:161], v10 offset:17416\n"
"v_pk_max_i16 v242, v242, 0\n"
"v_mfma_f32_16x16x32_bf16 v[116:119], a[180:183], a[52:55], v[116:119]\n"
"ds_read_b64_tr_b16 a[162:163], v10 offset:21512\n"
"v_pk_max_i16 v243, v243, 0\n"
"v_mfma_f32_16x16x32_bf16 v[116:119], a[176:179], a[36:39], v[116:119]\n"
"ds_write_b64 v3, v[240:241] offset:4096\n"
"v_mfma_f32_16x16x32_bf16 v[120:123], a[176:179], a[40:43], v[120:123]\n"
"v_accvgpr_read_b32 v248, a148\n"
"v_accvgpr_read_b32 v249, a149\n"
"v_mfma_f32_16x16x32_bf16 v[120:123], a[180:183], a[56:59], v[120:123]\n"
"ds_read_b64_tr_b16 a[164:165], v10 offset:25608\n"
"v_accvgpr_read_b32 v250, a150\n"
"v_mfma_f32_16x16x32_bf16 v[124:127], a[180:183], a[60:63], v[124:127]\n"
"ds_read_b64_tr_b16 a[166:167], v10 offset:29704\n"
"v_accvgpr_read_b32 v251, a151\n"
"v_mfma_f32_16x16x32_bf16 v[124:127], a[176:179], a[44:47], v[124:127]\n"
"s_waitcnt lgkmcnt(4)\n"
"ds_write_b64 v3, v[242:243] offset:4104\n"
"v_mfma_f32_16x16x32_bf16 v[140:143], a[184:187], a[44:47], v[140:143]\n"
"s_waitcnt vmcnt(4)\n"
"s_barrier\n"
"v_mfma_f32_16x16x32_bf16 v[140:143], a[188:191], a[60:63], v[140:143]\n"
"ds_read_b64_tr_b16 a[168:169], v9 offset:32768\n"
"v_cvt_pk_bf16_f32 v248, v248, v249\n"
"v_mfma_f32_16x16x32_bf16 v[136:139], a[188:191], a[56:59], v[136:139]\n"
"ds_read_b64_tr_b16 a[170:171], v9 offset:36864\n"
"v_cvt_pk_bf16_f32 v249, v250, v251\n"
"v_mfma_f32_16x16x32_bf16 v[136:139], a[184:187], a[40:43], v[136:139]\n"
"s_add_u32 m0, s48, 0\n"
"v_cvt_pk_bf16_f32 v250, v164, v165\n"
"v_cvt_pk_bf16_f32 v251, v166, v167\n"
"v_mfma_f32_16x16x32_bf16 v[132:135], a[184:187], a[36:39], v[132:135]\n"
"s_mov_b32 s52, 0\n"
".LPAD4:\n"
"s_cbranch_execz .LPAD7\n"
"buffer_load_dwordx4 v12, s[32:35], s52 offen offset:0 lds\n"
"v_mfma_f32_16x16x32_bf16 v[132:135], a[188:191], a[52:55], v[132:135]\n"
"ds_read_b64_tr_b16 a[172:173], v9 offset:40960\n"
"v_pk_max_i16 v248, v248, 0\n"
"v_mfma_f32_16x16x32_bf16 v[128:131], a[188:191], a[48:51], v[128:131]\n"
"ds_read_b64_tr_b16 a[174:175], v9 offset:45056\n"
"v_pk_max_i16 v249, v249, 0\n"
"v_mfma_f32_16x16x32_bf16 v[128:131], a[184:187], a[32:35], v[128:131]\n"
"s_waitcnt lgkmcnt(3)\n"
"v_pk_max_i16 v250, v250, 0\n"
"v_pk_max_i16 v251, v251, 0\n"
"v_mfma_f32_16x16x32_bf16 v[144:147], a[160:163], a[32:35], v[144:147]\n"
"v_accvgpr_read_b32 v240, a152\n"
"v_accvgpr_read_b32 v241, a153\n"
"v_mfma_f32_16x16x32_bf16 v[144:147], a[164:167], a[48:51], v[144:147]\n"
"ds_read_b64_tr_b16 a[176:177], v9 offset:32776\n"
"v_accvgpr_read_b32 v242, a154\n"
"v_mfma_f32_16x16x32_bf16 v[148:151], a[164:167], a[52:55], v[148:151]\n"
"ds_read_b64_tr_b16 a[178:179], v9 offset:36872\n"
"v_accvgpr_read_b32 v243, a155\n"
"v_mfma_f32_16x16x32_bf16 v[148:151], a[160:163], a[36:39], v[148:151]\n"
"ds_write_b64 v3, v[248:249] offset:5120\n"
"v_mfma_f32_16x16x32_bf16 v[152:155], a[160:163], a[40:43], v[152:155]\n"
"buffer_load_dwordx4 v13, s[32:35], s52 offen offset:1024 lds\n"
"v_mfma_f32_16x16x32_bf16 v[152:155], a[164:167], a[56:59], v[152:155]\n"
"ds_read_b64_tr_b16 a[180:181], v9 offset:40968\n"
"v_cvt_pk_bf16_f32 v240, v240, v241\n"
"v_mfma_f32_16x16x32_bf16 v[156:159], a[164:167], a[60:63], v[156:159]\n"
"ds_read_b64_tr_b16 a[182:183], v9 offset:45064\n"
"v_cvt_pk_bf16_f32 v241, v242, v243\n"
"v_mfma_f32_16x16x32_bf16 v[156:159], a[160:163], a[44:47], v[156:159]\n"
"s_waitcnt lgkmcnt(4)\n"
"ds_write_b64 v3, v[250:251] offset:5128\n"
"v_mfma_f32_16x16x32_bf16 v[44:47], a[168:171], a[76:79], v[44:47]\n"
"v_cvt_pk_bf16_f32 v242, v168, v169\n"
"v_cvt_pk_bf16_f32 v243, v170, v171\n"
"v_mfma_f32_16x16x32_bf16 v[44:47], a[172:175], a[92:95], v[44:47]\n"
"ds_read_b64_tr_b16 a[184:185], v10 offset:32768\n"
"v_pk_max_i16 v240, v240, 0\n"
"v_mfma_f32_16x16x32_bf16 v[40:43], a[172:175], a[88:91], v[40:43]\n"
"ds_read_b64_tr_b16 a[186:187], v10 offset:36864\n"
"v_pk_max_i16 v241, v241, 0\n"
"v_mfma_f32_16x16x32_bf16 v[40:43], a[168:171], a[72:75], v[40:43]\n"
"v_pk_max_i16 v242, v242, 0\n"
"v_pk_max_i16 v243, v243, 0\n"
"v_mfma_f32_16x16x32_bf16 v[36:39], a[168:171], a[68:71], v[36:39]\n"
"buffer_load_dwordx4 v14, s[32:35], s52 offen offset:2048 lds\n"
"v_mfma_f32_16x16x32_bf16 v[36:39], a[172:175], a[84:87], v[36:39]\n"
"ds_read_b64_tr_b16 a[188:189], v10 offset:40960\n"
"v_accvgpr_read_b32 v248, a156\n"
"v_mfma_f32_16x16x32_bf16 v[32:35], a[172:175], a[80:83], v[32:35]\n"
"ds_read_b64_tr_b16 a[190:191], v10 offset:45056\n"
"v_accvgpr_read_b32 v249, a157\n"
"v_mfma_f32_16x16x32_bf16 v[32:35], a[168:171], a[64:67], v[32:35]\n"
"s_waitcnt lgkmcnt(3)\n"
"ds_write_b64 v3, v[240:241] offset:6144\n"
"v_mfma_f32_16x16x32_bf16 v[48:51], a[176:179], a[64:67], v[48:51]\n"
"v_accvgpr_read_b32 v250, a158\n"
"v_accvgpr_read_b32 v251, a159\n"
"v_mfma_f32_16x16x32_bf16 v[48:51], a[180:183], a[80:83], v[48:51]\n"
"ds_read_b64_tr_b16 a[160:161], v10 offset:32776\n"
"v_cvt_pk_bf16_f32 v248, v248, v249\n"
"v_mfma_f32_16x16x32_bf16 v[52:55], a[180:183], a[84:87], v[52:55]\n"
"ds_read_b64_tr_b16 a[162:163], v10 offset:36872\n"
"v_cvt_pk_bf16_f32 v249, v250, v251\n"
"v_mfma_f32_16x16x32_bf16 v[52:55], a[176:179], a[68:71], v[52:55]\n"
"ds_write_b64 v3, v[242:243] offset:6152\n"
"v_mfma_f32_16x16x32_bf16 v[56:59], a[176:179], a[72:75], v[56:59]\n"
"buffer_load_dwordx4 v15, s[32:35], s52 offen offset:3072 lds\n"
"v_mfma_f32_16x16x32_bf16 v[56:59], a[180:183], a[88:91], v[56:59]\n"
"ds_read_b64_tr_b16 a[164:165], v10 offset:40968\n"
"v_cvt_pk_bf16_f32 v250, v172, v173\n"
"v_mfma_f32_16x16x32_bf16 v[60:63], a[180:183], a[92:95], v[60:63]\n"
"ds_read_b64_tr_b16 a[166:167], v10 offset:45064\n"
"v_cvt_pk_bf16_f32 v251, v174, v175\n"
"v_mfma_f32_16x16x32_bf16 v[60:63], a[176:179], a[76:79], v[60:63]\n"
"s_waitcnt lgkmcnt(4)\n"
"v_pk_max_i16 v248, v248, 0\n"
"v_pk_max_i16 v249, v249, 0\n"
"v_mfma_f32_16x16x32_bf16 v[76:79], a[184:187], a[76:79], v[76:79]\n"
"v_pk_max_i16 v250, v250, 0\n"
"v_pk_max_i16 v251, v251, 0\n"
"v_mfma_f32_16x16x32_bf16 v[76:79], a[188:191], a[92:95], v[76:79]\n"
"ds_read_b64_tr_b16 a[168:169], v9 offset:33792\n"
"v_cvt_pk_bf16_f32 v176, v176, v177\n"
"v_mfma_f32_16x16x32_bf16 v[72:75], a[188:191], a[88:91], v[72:75]\n"
"ds_read_b64_tr_b16 a[170:171], v9 offset:37888\n"
"v_cvt_pk_bf16_f32 v177, v178, v179\n"
"v_mfma_f32_16x16x32_bf16 v[72:75], a[184:187], a[72:75], v[72:75]\n"
"s_add_u32 m0, s48, 16384\n"
"ds_write_b64 v3, v[248:249] offset:7168\n"
"v_mfma_f32_16x16x32_bf16 v[68:71], a[184:187], a[68:71], v[68:71]\n"
"s_mov_b32 s52, 16384\n"
"buffer_load_dwordx4 v12, s[32:35], s52 offen offset:0 lds\n"
"v_mfma_f32_16x16x32_bf16 v[68:71], a[188:191], a[84:87], v[68:71]\n"
"ds_read_b64_tr_b16 a[172:173], v9 offset:41984\n"
"v_cvt_pk_bf16_f32 v178, v192, v193\n"
"v_mfma_f32_16x16x32_bf16 v[64:67], a[188:191], a[80:83], v[64:67]\n"
"ds_read_b64_tr_b16 a[174:175], v9 offset:46080\n"
"v_cvt_pk_bf16_f32 v179, v194, v195\n"
"v_mfma_f32_16x16x32_bf16 v[64:67], a[184:187], a[64:67], v[64:67]\n"
"s_waitcnt lgkmcnt(4)\n"
"ds_write_b64 v3, v[250:251] offset:7176\n"
"v_mfma_f32_16x16x32_bf16 v[80:83], a[160:163], a[64:67], v[80:83]\n"
"v_pk_max_i16 v176, v176, 0\n"
"v_pk_max_i16 v177, v177, 0\n"
"v_mfma_f32_16x16x32_bf16 v[80:83], a[164:167], a[80:83], v[80:83]\n"
"ds_read_b64_tr_b16 a[176:177], v9 offset:33800\n"
"v_pk_max_i16 v178, v178, 0\n"
"v_mfma_f32_16x16x32_bf16 v[84:87], a[164:167], a[84:87], v[84:87]\n"
"ds_read_b64_tr_b16 a[178:179], v9 offset:37896\n"
"v_pk_max_i16 v179, v179, 0\n"
"v_mfma_f32_16x16x32_bf16 v[84:87], a[160:163], a[68:71], v[84:87]\n"
"ds_write_b64 v3, v[176:177] offset:8192\n"
"v_mfma_f32_16x16x32_bf16 v[88:91], a[160:163], a[72:75], v[88:91]\n"
"buffer_load_dwordx4 v13, s[32:35], s52 offen offset:1024 lds\n"
"v_mfma_f32_16x16x32_bf16 v[88:91], a[164:167], a[88:91], v[88:91]\n"
"ds_read_b64_tr_b16 a[180:181], v9 offset:41992\n"
"v_cvt_pk_bf16_f32 v180, v180, v181\n"
"v_mfma_f32_16x16x32_bf16 v[92:95], a[164:167], a[92:95], v[92:95]\n"
"ds_read_b64_tr_b16 a[182:183], v9 offset:46088\n"
"v_cvt_pk_bf16_f32 v181, v182, v183\n"
"v_mfma_f32_16x16x32_bf16 v[92:95], a[160:163], a[76:79], v[92:95]\n"
"s_waitcnt lgkmcnt(4)\n"
"ds_write_b64 v3, v[178:179] offset:8200\n"
"v_mfma_f32_16x16x32_bf16 v[108:111], a[168:171], a[76:79], v[108:111]\n"
"v_cvt_pk_bf16_f32 v182, v196, v197\n"
"v_cvt_pk_bf16_f32 v183, v198, v199\n"
"v_mfma_f32_16x16x32_bf16 v[108:111], a[172:175], a[92:95], v[108:111]\n"
"ds_read_b64_tr_b16 a[184:185], v10 offset:33792\n"
"v_pk_max_i16 v180, v180, 0\n"
"v_mfma_f32_16x16x32_bf16 v[104:107], a[172:175], a[88:91], v[104:107]\n"
"ds_read_b64_tr_b16 a[186:187], v10 offset:37888\n"
"v_pk_max_i16 v181, v181, 0\n"
"v_mfma_f32_16x16x32_bf16 v[104:107], a[168:171], a[72:75], v[104:107]\n"
"v_pk_max_i16 v182, v182, 0\n"
"v_pk_max_i16 v183, v183, 0\n"
"v_mfma_f32_16x16x32_bf16 v[100:103], a[168:171], a[68:71], v[100:103]\n"
"buffer_load_dwordx4 v14, s[32:35], s52 offen offset:2048 lds\n"
"v_mfma_f32_16x16x32_bf16 v[100:103], a[172:175], a[84:87], v[100:103]\n"
"ds_read_b64_tr_b16 a[188:189], v10 offset:41984\n"
"v_cvt_pk_bf16_f32 v184, v184, v185\n"
"v_mfma_f32_16x16x32_bf16 v[96:99], a[172:175], a[80:83], v[96:99]\n"
"ds_read_b64_tr_b16 a[190:191], v10 offset:46080\n"
"v_cvt_pk_bf16_f32 v185, v186, v187\n"
"v_mfma_f32_16x16x32_bf16 v[96:99], a[168:171], a[64:67], v[96:99]\n"
"s_waitcnt lgkmcnt(3)\n"
"ds_write_b64 v3, v[180:181] offset:9216\n"
"v_mfma_f32_16x16x32_bf16 v[112:115], a[176:179], a[64:67], v[112:115]\n"
"v_cvt_pk_bf16_f32 v186, v200, v201\n"
"v_cvt_pk_bf16_f32 v187, v202, v203\n"
"v_mfma_f32_16x16x32_bf16 v[112:115], a[180:183], a[80:83], v[112:115]\n"
"ds_read_b64_tr_b16 a[160:161], v10 offset:33800\n"
"v_pk_max_i16 v184, v184, 0\n"
"v_mfma_f32_16x16x32_bf16 v[116:119], a[180:183], a[84:87], v[116:119]\n"
"ds_read_b64_tr_b16 a[162:163], v10 offset:37896\n"
"v_pk_max_i16 v185, v185, 0\n"
"v_mfma_f32_16x16x32_bf16 v[116:119], a[176:179], a[68:71], v[116:119]\n"
"ds_write_b64 v3, v[182:183] offset:9224\n"
"v_mfma_f32_16x16x32_bf16 v[120:123], a[176:179], a[72:75], v[120:123]\n"
"buffer_load_dwordx4 v15, s[32:35], s52 offen offset:3072 lds\n"
"v_mfma_f32_16x16x32_bf16 v[120:123], a[180:183], a[88:91], v[120:123]\n"
"ds_read_b64_tr_b16 a[164:165], v10 offset:41992\n"
"v_pk_max_i16 v186, v186, 0\n"
"v_mfma_f32_16x16x32_bf16 v[124:127], a[180:183], a[92:95], v[124:127]\n"
"ds_read_b64_tr_b16 a[166:167], v10 offset:46088\n"
"v_pk_max_i16 v187, v187, 0\n"
"v_mfma_f32_16x16x32_bf16 v[124:127], a[176:179], a[76:79], v[124:127]\n"
"s_waitcnt lgkmcnt(4)\n"
"ds_write_b64 v3, v[184:185] offset:10240\n"
"v_mfma_f32_16x16x32_bf16 v[140:143], a[184:187], a[76:79], v[140:143]\n"
"s_waitcnt vmcnt(8)\n"
"s_barrier\n"
"v_mfma_f32_16x16x32_bf16 v[140:143], a[188:191], a[92:95], v[140:143]\n"
"ds_read_b64_tr_b16 a[168:169], v9 offset:49152\n"
"v_cvt_pk_bf16_f32 v188, v188, v189\n"
"v_mfma_f32_16x16x32_bf16 v[136:139], a[188:191], a[88:91], v[136:139]\n"
"ds_read_b64_tr_b16 a[170:171], v9 offset:53248\n"
"v_cvt_pk_bf16_f32 v189, v190, v191\n"
"v_mfma_f32_16x16x32_bf16 v[136:139], a[184:187], a[72:75], v[136:139]\n"
"s_add_u32 m0, s48, 49152\n"
"ds_write_b64 v3, v[186:187] offset:10248\n"
"v_mfma_f32_16x16x32_bf16 v[132:135], a[184:187], a[68:71], v[132:135]\n"
"s_mov_b32 s52, 0\n"
"buffer_load_dwordx4 v16, s[36:39], s52 offen offset:0 lds\n"
"v_mfma_f32_16x16x32_bf16 v[132:135], a[188:191], a[84:87], v[132:135]\n"
"ds_read_b64_tr_b16 a[172:173], v9 offset:57344\n"
"v_cvt_pk_bf16_f32 v190, v204, v205\n"
"v_mfma_f32_16x16x32_bf16 v[128:131], a[188:191], a[80:83], v[128:131]\n"
"ds_read_b64_tr_b16 a[174:175], v9 offset:61440\n"
"v_cvt_pk_bf16_f32 v191, v206, v207\n"
"v_mfma_f32_16x16x32_bf16 v[128:131], a[184:187], a[64:67], v[128:131]\n"
"s_waitcnt lgkmcnt(4)\n"
"v_pk_max_i16 v188, v188, 0\n"
"v_pk_max_i16 v189, v189, 0\n"
"v_mfma_f32_16x16x32_bf16 v[144:147], a[160:163], a[64:67], v[144:147]\n"
"v_pk_max_i16 v190, v190, 0\n"
"v_pk_max_i16 v191, v191, 0\n"
"v_mfma_f32_16x16x32_bf16 v[144:147], a[164:167], a[80:83], v[144:147]\n"
"ds_read_b64_tr_b16 a[176:177], v9 offset:49160\n"
"v_mov_b32 v240, 0x3f803f80\n"
"v_mfma_f32_16x16x32_bf16 v[148:151], a[164:167], a[84:87], v[148:151]\n"
"ds_read_b64_tr_b16 a[178:179], v9 offset:53256\n"
"v_mov_b32 v241, 0x3f803f80\n"
"v_mfma_f32_16x16x32_bf16 v[148:151], a[160:163], a[68:71], v[148:151]\n"
"ds_write_b64 v3, v[188:189] offset:11264\n"
"v_mfma_f32_16x16x32_bf16 v[152:155], a[160:163], a[72:75], v[152:155]\n"
"buffer_load_dwordx4 v17, s[36:39], s52 offen offset:1024 lds\n"
"v_mfma_f32_16x16x32_bf16 v[152:155], a[164:167], a[88:91], v[152:155]\n"
"ds_read_b64_tr_b16 a[180:181], v9 offset:57352\n"
"v_mov_b32 v242, 0x3f803f80\n"
"v_mfma_f32_16x16x32_bf16 v[156:159], a[164:167], a[92:95], v[156:159]\n"
"ds_read_b64_tr_b16 a[182:183], v9 offset:61448\n"
"v_mov_b32 v243, 0x3f803f80\n"
"v_mfma_f32_16x16x32_bf16 v[156:159], a[160:163], a[76:79], v[156:159]\n"
"s_waitcnt lgkmcnt(4)\n"
"ds_write_b64 v3, v[190:191] offset:11272\n"
"v_mfma_f32_16x16x32_bf16 v[44:47], a[168:171], a[204:207], v[44:47]\n"
"v_and_b32 v244, 1, v0\n"
"v_sub_u32 v244, 0, v244\n"
"v_mfma_f32_16x16x32_bf16 v[44:47], a[172:175], a[220:223], v[44:47]\n"
"ds_read_b64_tr_b16 a[184:185], v10 offset:49152\n"
"v_and_b32 v244, 0xaaaaaaaa, v244\n"
"v_mfma_f32_16x16x32_bf16 v[40:43], a[172:175], a[216:219], v[40:43]\n"
"ds_read_b64_tr_b16 a[186:187], v10 offset:53248\n"
"v_or_b32 v244, 0x44444444, v244\n"
"v_mfma_f32_16x16x32_bf16 v[40:43], a[168:171], a[200:203], v[40:43]\n"
"v_cvt_pk_bf16_f32 v208, v208, v209\n"
"v_cvt_pk_bf16_f32 v209, v210, v211\n"
"v_mfma_f32_16x16x32_bf16 v[36:39], a[168:171], a[196:199], v[36:39]\n"
"buffer_load_dwordx4 v16, s[36:39], s52 offen offset:2048 lds\n"
"v_mfma_f32_16x16x32_bf16 v[36:39], a[172:175], a[212:215], v[36:39]\n"
"ds_read_b64_tr_b16 a[188:189], v10 offset:57344\n"
"v_pk_max_i16 v208, v208, 0\n"
"v_mfma_f32_16x16x32_bf16 v[32:35], a[172:175], a[208:211], v[32:35]\n"
"ds_read_b64_tr_b16 a[190:191], v10 offset:61440\n"
"v_pk_max_i16 v209, v209, 0\n"
"v_mfma_f32_16x16x32_bf16 v[32:35], a[168:171], a[192:195], v[32:35]\n"
"s_waitcnt lgkmcnt(3)\n"
"v_cvt_pk_bf16_f32 v210, v212, v213\n"
"v_cvt_pk_bf16_f32 v211, v214, v215\n"
"v_mfma_f32_16x16x32_bf16 v[48:51], a[176:179], a[192:195], v[48:51]\n"
"v_pk_max_i16 v210, v210, 0\n"
"v_pk_max_i16 v211, v211, 0\n"
"v_mfma_f32_16x16x32_bf16 v[48:51], a[180:183], a[208:211], v[48:51]\n"
"ds_read_b64_tr_b16 a[160:161], v10 offset:49160\n"
"v_cvt_pk_bf16_f32 v216, v216, v217\n"
"v_mfma_f32_16x16x32_bf16 v[52:55], a[180:183], a[212:215], v[52:55]\n"
"ds_read_b64_tr_b16 a[162:163], v10 offset:53256\n"
"v_cvt_pk_bf16_f32 v217, v218, v219\n"
"v_mfma_f32_16x16x32_bf16 v[52:55], a[176:179], a[196:199], v[52:55]\n"
"v_pk_max_i16 v216, v216, 0\n"
"v_pk_max_i16 v217, v217, 0\n"
"v_mfma_f32_16x16x32_bf16 v[56:59], a[176:179], a[200:203], v[56:59]\n"
"buffer_load_dwordx4 v17, s[36:39], s52 offen offset:3072 lds\n"
"v_mfma_f32_16x16x32_bf16 v[56:59], a[180:183], a[216:219], v[56:59]\n"
"ds_read_b64_tr_b16 a[164:165], v10 offset:57352\n"
"v_cvt_pk_bf16_f32 v218, v220, v221\n"
"v_mfma_f32_16x16x32_bf16 v[60:63], a[180:183], a[220:223], v[60:63]\n"
"ds_read_b64_tr_b16 a[166:167], v10 offset:61448\n"
"v_cvt_pk_bf16_f32 v32, v32, v33\n"
"v_mfma_f32_16x16x32_bf16 v[60:63], a[176:179], a[204:207], v[60:63]\n"
"s_waitcnt lgkmcnt(3)\n"
"v_cvt_pk_bf16_f32 v33, v34, v35\n"
"v_cvt_pk_bf16_f32 v34, v48, v49\n"
"v_mfma_f32_16x16x32_bf16 v[76:79], a[184:187], a[204:207], v[76:79]\n"
"s_waitcnt vmcnt(8)\n"
"s_barrier\n"
"v_mfma_f32_16x16x32_bf16 v[76:79], a[188:191], a[220:223], v[76:79]\n"
"ds_read_b64_tr_b16 a[168:169], v9 offset:50176\n"
"v_cvt_pk_bf16_f32 v35, v50, v51\n"
"v_mfma_f32_16x16x32_bf16 v[72:75], a[188:191], a[216:219], v[72:75]\n"
"ds_read_b64_tr_b16 a[170:171], v9 offset:54272\n"
"v_cvt_pk_bf16_f32 v36, v36, v37\n"
"v_mfma_f32_16x16x32_bf16 v[72:75], a[184:187], a[200:203], v[72:75]\n"
"v_cvt_pk_bf16_f32 v37, v38, v39\n"
"v_cvt_pk_bf16_f32 v38, v52, v53\n"
"v_mfma_f32_16x16x32_bf16 v[68:71], a[184:187], a[196:199], v[68:71]\n"
"v_cvt_pk_bf16_f32 v39, v54, v55\n"
"v_cvt_pk_bf16_f32 v40, v40, v41\n"
"v_mfma_f32_16x16x32_bf16 v[68:71], a[188:191], a[212:215], v[68:71]\n"
"ds_read_b64_tr_b16 a[172:173], v9 offset:58368\n"
"v_cvt_pk_bf16_f32 v41, v42, v43\n"
"v_mfma_f32_16x16x32_bf16 v[64:67], a[188:191], a[208:211], v[64:67]\n"
"ds_read_b64_tr_b16 a[174:175], v9 offset:62464\n"
"v_cvt_pk_bf16_f32 v42, v56, v57\n"
"v_mfma_f32_16x16x32_bf16 v[64:67], a[184:187], a[192:195], v[64:67]\n"
"s_waitcnt lgkmcnt(3)\n"
"v_cvt_pk_bf16_f32 v43, v58, v59\n"
"v_cvt_pk_bf16_f32 v44, v44, v45\n"
"v_mfma_f32_16x16x32_bf16 v[80:83], a[160:163], a[192:195], v[80:83]\n"
"ds_read_b128 a[128:131], v18 offset:0\n"
"v_cvt_pk_bf16_f32 v45, v46, v47\n"
"v_mfma_f32_16x16x32_bf16 v[80:83], a[164:167], a[208:211], v[80:83]\n"
"ds_read_b64_tr_b16 a[176:177], v9 offset:50184\n"
"v_cvt_pk_bf16_f32 v46, v60, v61\n"
"v_mfma_f32_16x16x32_bf16 v[84:87], a[164:167], a[212:215], v[84:87]\n"
"ds_read_b64_tr_b16 a[178:179], v9 offset:54280\n"
"v_cvt_pk_bf16_f32 v47, v62, v63\n"
"v_mfma_f32_16x16x32_bf16 v[84:87], a[160:163], a[196:199], v[84:87]\n"
"ds_read_b128 a[132:135], v19 offset:0\n"
"v_cvt_pk_bf16_f32 v219, v222, v223\n"
"v_mfma_f32_16x16x32_bf16 v[88:91], a[160:163], a[200:203], v[88:91]\n"
"ds_read_b128 a[136:139], v20 offset:0\n"
"v_pk_max_i16 v218, v218, 0\n"
"v_mfma_f32_16x16x32_bf16 v[88:91], a[164:167], a[216:219], v[88:91]\n"
"ds_read_b64_tr_b16 a[180:181], v9 offset:58376\n"
"v_pk_max_i16 v219, v219, 0\n"
"v_mfma_f32_16x16x32_bf16 v[92:95], a[164:167], a[220:223], v[92:95]\n"
"ds_read_b64_tr_b16 a[182:183], v9 offset:62472\n"
"v_cvt_pk_bf16_f32 v64, v64, v65\n"
"v_mfma_f32_16x16x32_bf16 v[92:95], a[160:163], a[204:207], v[92:95]\n"
"s_waitcnt lgkmcnt(5)\n"
"ds_read_b128 a[140:143], v21 offset:0\n"
"v_cvt_pk_bf16_f32 v65, v66, v67\n"
"v_mfma_f32_16x16x32_bf16 v[108:111], a[168:171], a[204:207], v[108:111]\n"
"ds_read_b128 a[144:147], v18 offset:4096\n"
"v_cvt_pk_bf16_f32 v66, v80, v81\n"
"v_mfma_f32_16x16x32_bf16 v[108:111], a[172:175], a[220:223], v[108:111]\n"
"ds_read_b64_tr_b16 a[184:185], v10 offset:50176\n"
"v_cvt_pk_bf16_f32 v67, v82, v83\n"
"v_mfma_f32_16x16x32_bf16 v[104:107], a[172:175], a[216:219], v[104:107]\n"
"ds_read_b64_tr_b16 a[186:187], v10 offset:54272\n"
"v_cvt_pk_bf16_f32 v68, v68, v69\n"
"v_mfma_f32_16x16x32_bf16 v[104:107], a[168:171], a[200:203], v[104:107]\n"
"ds_read_b128 a[148:151], v19 offset:4096\n"
"v_cvt_pk_bf16_f32 v69, v70, v71\n"
"v_mfma_f32_16x16x32_bf16 v[100:103], a[168:171], a[196:199], v[100:103]\n"
"ds_read_b128 a[152:155], v20 offset:4096\n"
"v_cvt_pk_bf16_f32 v70, v84, v85\n"
"v_mfma_f32_16x16x32_bf16 v[100:103], a[172:175], a[212:215], v[100:103]\n"
"ds_read_b64_tr_b16 a[188:189], v10 offset:58368\n"
"v_cvt_pk_bf16_f32 v71, v86, v87\n"
"v_mfma_f32_16x16x32_bf16 v[96:99], a[172:175], a[208:211], v[96:99]\n"
"ds_read_b64_tr_b16 a[190:191], v10 offset:62464\n"
"v_cvt_pk_bf16_f32 v72, v72, v73\n"
"v_mfma_f32_16x16x32_bf16 v[96:99], a[168:171], a[192:195], v[96:99]\n"
"s_waitcnt lgkmcnt(5)\n"
"ds_read_b128 a[156:159], v21 offset:4096\n"
"v_cvt_pk_bf16_f32 v73, v74, v75\n"
"v_mfma_f32_16x16x32_bf16 v[112:115], a[176:179], a[192:195], v[112:115]\n"
"v_cvt_pk_bf16_f32 v74, v88, v89\n"
"v_cvt_pk_bf16_f32 v75, v90, v91\n"
"v_mfma_f32_16x16x32_bf16 v[112:115], a[180:183], a[208:211], v[112:115]\n"
"ds_read_b64_tr_b16 a[160:161], v10 offset:50184\n"
"v_cvt_pk_bf16_f32 v76, v76, v77\n"
"v_mfma_f32_16x16x32_bf16 v[116:119], a[180:183], a[212:215], v[116:119]\n"
"ds_read_b64_tr_b16 a[162:163], v10 offset:54280\n"
"v_cvt_pk_bf16_f32 v77, v78, v79\n"
"v_mfma_f32_16x16x32_bf16 v[116:119], a[176:179], a[196:199], v[116:119]\n"
"v_cvt_pk_bf16_f32 v78, v92, v93\n"
"v_cvt_pk_bf16_f32 v79, v94, v95\n"
"v_mfma_f32_16x16x32_bf16 v[120:123], a[176:179], a[200:203], v[120:123]\n"
"v_lshrrev_b32 v31, 5, v0\n"
"v_lshlrev_b32 v31, 10, v31\n"
"v_mfma_f32_16x16x32_bf16 v[120:123], a[180:183], a[216:219], v[120:123]\n"
"ds_read_b64_tr_b16 a[164:165], v10 offset:58376\n"
"v_add_u32 v31, v31, v1\n"
"v_mfma_f32_16x16x32_bf16 v[124:127], a[180:183], a[220:223], v[124:127]\n"
"ds_read_b64_tr_b16 a[166:167], v10 offset:62472\n"
"v_cvt_pk_bf16_f32 v96, v96, v97\n"
"v_mfma_f32_16x16x32_bf16 v[124:127], a[176:179], a[204:207], v[124:127]\n"
"s_waitcnt lgkmcnt(3)\n"
"v_cvt_pk_bf16_f32 v97, v98, v99\n"
"v_cvt_pk_bf16_f32 v98, v112, v113\n"
"v_mfma_f32_16x16x32_bf16 v[140:143], a[184:187], a[204:207], v[140:143]\n"
"v_cvt_pk_bf16_f32 v99, v114, v115\n"
"v_cvt_pk_bf16_f32 v100, v100, v101\n"
"v_mfma_f32_16x16x32_bf16 v[140:143], a[188:191], a[220:223], v[140:143]\n"
"v_cvt_pk_bf16_f32 v101, v102, v103\n"
"v_cvt_pk_bf16_f32 v102, v116, v117\n"
"v_mfma_f32_16x16x32_bf16 v[136:139], a[188:191], a[216:219], v[136:139]\n"
"v_cvt_pk_bf16_f32 v103, v118, v119\n"
"v_cvt_pk_bf16_f32 v104, v104, v105\n"
"v_mfma_f32_16x16x32_bf16 v[136:139], a[184:187], a[200:203], v[136:139]\n"
"v_cvt_pk_bf16_f32 v105, v106, v107\n"
"v_cvt_pk_bf16_f32 v106, v120, v121\n"
"v_mfma_f32_16x16x32_bf16 v[132:135], a[184:187], a[196:199], v[132:135]\n"
"v_cvt_pk_bf16_f32 v107, v122, v123\n"
"v_cvt_pk_bf16_f32 v108, v108, v109\n"
"v_mfma_f32_16x16x32_bf16 v[132:135], a[188:191], a[212:215], v[132:135]\n"
"v_cvt_pk_bf16_f32 v109, v110, v111\n"
"v_cvt_pk_bf16_f32 v110, v124, v125\n"
"v_mfma_f32_16x16x32_bf16 v[128:131], a[188:191], a[208:211], v[128:131]\n"
"v_cvt_pk_bf16_f32 v111, v126, v127\n"
"v_sub_u32 v31, v31, v3\n"
"v_mfma_f32_16x16x32_bf16 v[128:131], a[184:187], a[192:195], v[128:131]\n"
"s_waitcnt lgkmcnt(0)\n"
"v_sub_u32 v4, v5, v31\n"
"v_sub_u32 v5, v6, v31\n"
"v_mfma_f32_16x16x32_bf16 v[144:147], a[160:163], a[192:195], v[144:147]\n"
"v_sub_u32 v6, v7, v31\n"
"v_sub_u32 v7, v8, v31\n"
"v_mfma_f32_16x16x32_bf16 v[144:147], a[164:167], a[208:211], v[144:147]\n"
"v_lshrrev_b32 v31, 3, v0\n"
"v_mul_u32_u24 v31, 1152, v31\n"
"v_mfma_f32_16x16x32_bf16 v[148:151], a[164:167], a[212:215], v[148:151]\n"
"v_and_b32 v2, 0x7f, v2\n"
"v_add_u32 v2, v2, v31\n"
"v_mfma_f32_16x16x32_bf16 v[148:151], a[160:163], a[196:199], v[148:151]\n"
"v_and_b32 v254, 7, v0\n"
"v_lshl_add_u32 v254, v254, 5, s63\n"
"v_mfma_f32_16x16x32_bf16 v[152:155], a[160:163], a[200:203], v[152:155]\n"
"v_lshlrev_b32 v254, 8, v254\n"
"v_lshrrev_b32 v255, 3, v0\n"
"v_mfma_f32_16x16x32_bf16 v[152:155], a[164:167], a[216:219], v[152:155]\n"
"v_lshl_add_u32 v255, v255, 12, v0\n"
"v_lshlrev_b32 v255, 8, v255\n"
"v_mfma_f32_16x16x32_bf16 v[156:159], a[164:167], a[220:223], v[156:159]\n"
"v_cvt_pk_bf16_f32 v128, v128, v129\n"
"v_cvt_pk_bf16_f32 v129, v130, v131\n"
"v_mfma_f32_16x16x32_bf16 v[156:159], a[160:163], a[204:207], v[156:159]\n"
"v_cvt_pk_bf16_f32 v130, v144, v145\n"
"v_cvt_pk_bf16_f32 v131, v146, v147\n"
"v_mfma_f32_16x16x32_bf16 v[48:51], a[128:131], v[32:35], 0\n"
"v_cvt_pk_bf16_f32 v132, v132, v133\n"
"v_cvt_pk_bf16_f32 v133, v134, v135\n"
"v_mfma_f32_16x16x32_bf16 v[52:55], a[128:131], v[36:39], 0\n"
"v_cvt_pk_bf16_f32 v134, v148, v149\n"
"v_cvt_pk_bf16_f32 v135, v150, v151\n"
"v_mfma_f32_16x16x32_bf16 v[56:59], a[128:131], v[40:43], 0\n"
"v_cvt_pk_bf16_f32 v136, v136, v137\n"
"v_cvt_pk_bf16_f32 v137, v138, v139\n"
"v_mfma_f32_16x16x32_bf16 v[60:63], a[128:131], v[44:47], 0\n"
"v_cvt_pk_bf16_f32 v138, v152, v153\n"
"v_cvt_pk_bf16_f32 v139, v154, v155\n"
"v_mfma_f32_16x16x32_bf16 v[48:51], a[132:135], v[64:67], v[48:51]\n"
"v_cvt_pk_bf16_f32 v140, v140, v141\n"
"v_cvt_pk_bf16_f32 v141, v142, v143\n"
"v_mfma_f32_16x16x32_bf16 v[52:55], a[132:135], v[68:71], v[52:55]\n"
"v_cvt_pk_bf16_f32 v142, v156, v157\n"
"v_cvt_pk_bf16_f32 v143, v158, v159\n"
"v_mfma_f32_16x16x32_bf16 v[56:59], a[132:135], v[72:75], v[56:59]\n"
"v_mov_b32 v31, 1\n"
"v_mov_b32 v245, 0xff800000\n"
"v_mfma_f32_16x16x32_bf16 v[60:63], a[132:135], v[76:79], v[60:63]\n"
"v_mov_b32 v246, 0xff800000\n"
"v_mov_b32 v247, 0xff800000\n"
"v_mfma_f32_16x16x32_bf16 v[48:51], a[136:139], v[96:99], v[48:51]\n"
"v_mov_b32 v248, 0xff800000\n"
"v_mfma_f32_16x16x32_bf16 v[52:55], a[136:139], v[100:103], v[52:55]\n"
"v_mfma_f32_16x16x32_bf16 v[56:59], a[136:139], v[104:107], v[56:59]\n"
"v_mfma_f32_16x16x32_bf16 v[60:63], a[136:139], v[108:111], v[60:63]\n"
".LATT_ENTRY:\n"
"s_waitcnt lgkmcnt(0)\n"
"s_nop 0\n"
"v_mfma_f32_16x16x32_bf16 v[48:51], a[140:143], v[128:131], v[48:51]\n"
"v_mov_b32 v192, 0\n"
"v_mov_b32 v193, 0\n"
"v_mfma_f32_16x16x32_bf16 v[52:55], a[140:143], v[132:135], v[52:55]\n"
"v_mov_b32 v194, 0\n"
"v_mov_b32 v195, 0\n"
"v_mfma_f32_16x16x32_bf16 v[56:59], a[140:143], v[136:139], v[56:59]\n"
"v_mov_b32 v8, -1\n"
"v_mov_b32 v9, -1\n"
"v_mfma_f32_16x16x32_bf16 v[60:63], a[140:143], v[140:143], v[60:63]\n"
"s_add_u32 m0, s48, 32768\n"
"v_mov_b32 v10, -1\n"
"v_mov_b32 v11, -1\n"
"v_mfma_f32_32x32x16_bf16 a[0:15], v[192:195], v[192:195], 0\n"
"s_mov_b32 s52, 32768\n"
"buffer_load_dwordx4 v12, s[32:35], s52 offen offset:0 lds\n"
"v_max3_f32 v245, v245, v48, v49\n"
"v_max3_f32 v246, v246, v52, v53\n"
"v_max3_f32 v247, v247, v56, v57\n"
"v_max3_f32 v248, v248, v60, v61\n"
"v_mfma_f32_32x32x16_bf16 a[16:31], v[192:195], v[192:195], 0\n"
"s_mov_b32 s50, 49152\n"
"v_max3_f32 v245, v245, v50, v51\n"
"v_max3_f32 v246, v246, v54, v55\n"
"v_max3_f32 v247, v247, v58, v59\n"
"v_max3_f32 v248, v248, v62, v63\n"
"v_mov_b32 v249, v245\n"
"v_mov_b32 v250, v246\n"
"v_mov_b32 v251, v247\n"
"v_mfma_f32_32x32x16_bf16 a[32:47], v[192:195], v[192:195], 0\n"
"s_mov_b32 s51, 32768\n"
"v_mov_b32 v252, v248\n"
"v_permlane32_swap_b32 v245, v249\n"
"v_permlane32_swap_b32 v246, v250\n"
"v_permlane32_swap_b32 v247, v251\n"
"v_permlane32_swap_b32 v248, v252\n"
"v_max_f32 v245, v245, v249\n"
"v_max_f32 v246, v246, v250\n"
"v_mfma_f32_32x32x16_bf16 a[48:63], v[192:195], v[192:195], 0\n"
"s_mov_b32 s69, s48\n"
"v_max_f32 v247, v247, v251\n"
"v_max_f32 v248, v248, v252\n"
"v_mov_b32 v249, v245\n"
"v_mov_b32 v250, v246\n"
"v_mov_b32 v251, v247\n"
"v_mov_b32 v252, v248\n"
"v_permlane16_swap_b32 v245, v249\n"
"v_mfma_f32_32x32x16_bf16 a[64:79], v[192:195], v[192:195], 0\n"
"s_add_u32 s67, s48, 16384\n"
"v_permlane16_swap_b32 v246, v250\n"
"v_permlane16_swap_b32 v247, v251\n"
"v_permlane16_swap_b32 v248, v252\n"
"v_max_f32 v245, v245, v249\n"
"v_max_f32 v246, v246, v250\n"
"v_max_f32 v247, v247, v251\n"
"v_max_f32 v248, v248, v252\n"
"v_mfma_f32_32x32x16_bf16 a[80:95], v[192:195], v[192:195], 0\n"
"s_add_u32 s68, s48, 32768\n"
"v_sub_f32 v224, 0, v245\n"
"v_sub_f32 v225, 0, v245\n"
"v_sub_f32 v226, 0, v245\n"
"v_sub_f32 v227, 0, v245\n"
"v_sub_f32 v228, 0, v246\n"
"v_sub_f32 v229, 0, v246\n"
"v_sub_f32 v230, 0, v246\n"
"v_mfma_f32_32x32x16_bf16 a[96:111], v[192:195], v[192:195], 0\n"
"s_mov_b32 s7, 2146435072\n"
"v_sub_f32 v231, 0, v246\n"
"v_sub_f32 v232, 0, v247\n"
"v_sub_f32 v233, 0, v247\n"
"v_sub_f32 v234, 0, v247\n"
"v_sub_f32 v235, 0, v247\n"
"v_sub_f32 v236, 0, v248\n"
"v_sub_f32 v237, 0, v248\n"
"v_mfma_f32_32x32x16_bf16 a[112:127], v[192:195], v[192:195], 0\n"
"s_add_u32 s6, s48, 98304\n"
"v_sub_f32 v238, 0, v248\n"
"v_sub_f32 v239, 0, v248\n"
"v_mfma_f32_32x32x16_bf16 a[224:239], v[192:195], v[192:195], 0\n"
"s_add_u32 s9, s57, 557056\n"
"s_nop 1\n"
"v_mfma_f32_16x16x32_bf16 v[92:95], a[144:147], v[44:47], v[236:239]\n"
"buffer_load_dwordx4 v13, s[32:35], s52 offen offset:1024 lds\n"
"v_mfma_f32_16x16x32_bf16 v[92:95], a[148:151], v[76:79], v[92:95]\n"
"ds_read_b128 a[160:163], v18 offset:8192\n"
"v_add_f32 v48, v224, v48\n"
"v_mfma_f32_16x16x32_bf16 v[92:95], a[152:155], v[108:111], v[92:95]\n"
"v_add_f32 v49, v224, v49\n"
"v_add_f32 v50, v224, v50\n"
"v_mfma_f32_16x16x32_bf16 v[92:95], a[156:159], v[140:143], v[92:95]\n"
"ds_read_b128 a[164:167], v19 offset:8192\n"
"v_add_f32 v51, v224, v51\n"
"v_mfma_f32_16x16x32_bf16 v[88:91], a[156:159], v[136:139], v[232:235]\n"
"v_exp_f32 v48, v48\n"
"v_mfma_f32_16x16x32_bf16 v[88:91], a[152:155], v[104:107], v[88:91]\n"
"ds_read_b128 a[168:171], v20 offset:8192\n"
"v_mfma_f32_16x16x32_bf16 v[88:91], a[148:151], v[72:75], v[88:91]\n"
"buffer_load_dwordx4 v14, s[32:35], s52 offen offset:2048 lds\n"
"v_mfma_f32_16x16x32_bf16 v[88:91], a[144:147], v[40:43], v[88:91]\n"
"ds_read_b128 a[172:175], v21 offset:8192\n"
"v_mfma_f32_16x16x32_bf16 v[84:87], a[144:147], v[36:39], v[228:231]\n"
"v_exp_f32 v49, v49\n"
"v_mfma_f32_16x16x32_bf16 v[84:87], a[148:151], v[68:71], v[84:87]\n"
"ds_read_b128 a[176:179], v18 offset:12288\n"
"v_mfma_f32_16x16x32_bf16 v[84:87], a[152:155], v[100:103], v[84:87]\n"
"v_exp_f32 v50, v50\n"
"v_mfma_f32_16x16x32_bf16 v[84:87], a[156:159], v[132:135], v[84:87]\n"
"ds_read_b128 a[180:183], v19 offset:12288\n"
"v_mfma_f32_16x16x32_bf16 v[80:83], a[156:159], v[128:131], v[224:227]\n"
".LPAD5:\n"
"s_cbranch_execz .LPAD8\n"
"buffer_load_dwordx4 v15, s[32:35], s52 offen offset:3072 lds\n"
"v_mfma_f32_16x16x32_bf16 v[80:83], a[152:155], v[96:99], v[80:83]\n"
"ds_read_b128 a[184:187], v20 offset:12288\n"
"v_mfma_f32_16x16x32_bf16 v[80:83], a[148:151], v[64:67], v[80:83]\n"
"v_exp_f32 v51, v51\n"
"v_mfma_f32_16x16x32_bf16 v[80:83], a[144:147], v[32:35], v[80:83]\n"
"s_waitcnt lgkmcnt(2)\n"
"ds_read_b128 a[188:191], v21 offset:12288\n"
"v_add_f32 v52, v228, v52\n"
"v_mfma_f32_16x16x32_bf16 v[112:115], a[160:163], v[32:35], v[224:227]\n"
"v_add_f32 v53, v228, v53\n"
"v_add_f32 v54, v228, v54\n"
"v_mfma_f32_16x16x32_bf16 v[112:115], a[164:167], v[64:67], v[112:115]\n"
"s_add_u32 m0, s48, 65536\n"
"v_add_u32 v18, 0x4000, v18\n"
"v_add_u32 v19, 0x4000, v19\n"
"v_mfma_f32_16x16x32_bf16 v[112:115], a[168:171], v[96:99], v[112:115]\n"
"s_mov_b32 s52, 16384\n"
"buffer_load_dwordx4 v16, s[36:39], s52 offen offset:0 lds\n"
"v_mfma_f32_16x16x32_bf16 v[112:115], a[172:175], v[128:131], v[112:115]\n"
"v_add_u32 v20, 0x4000, v20\n"
"v_add_u32 v21, 0x4000, v21\n"
"v_mfma_f32_16x16x32_bf16 v[116:119], a[172:175], v[132:135], v[228:231]\n"
"v_add_f32 v55, v228, v55\n"
"v_mfma_f32_16x16x32_bf16 v[116:119], a[168:171], v[100:103], v[116:119]\n"
"v_exp_f32 v52, v52\n"
"v_mfma_f32_16x16x32_bf16 v[116:119], a[164:167], v[68:71], v[116:119]\n"
"s_waitcnt vmcnt(5) lgkmcnt(0)\n"
"s_barrier\n"
"v_mfma_f32_16x16x32_bf16 v[116:119], a[160:163], v[36:39], v[116:119]\n"
"ds_read_b128 a[128:131], v18 offset:0\n"
"v_mfma_f32_16x16x32_bf16 v[120:123], a[160:163], v[40:43], v[232:235]\n"
"buffer_load_dwordx4 v17, s[36:39], s52 offen offset:1024 lds\n"
"v_mfma_f32_16x16x32_bf16 v[120:123], a[164:167], v[72:75], v[120:123]\n"
"ds_read_b128 a[132:135], v19 offset:0\n"
"v_mfma_f32_16x16x32_bf16 v[120:123], a[168:171], v[104:107], v[120:123]\n"
"v_exp_f32 v53, v53\n"
"v_mfma_f32_16x16x32_bf16 v[120:123], a[172:175], v[136:139], v[120:123]\n"
"ds_read_b128 a[136:139], v20 offset:0\n"
"v_mfma_f32_16x16x32_bf16 v[124:127], a[172:175], v[140:143], v[236:239]\n"
"v_exp_f32 v54, v54\n"
"v_mfma_f32_16x16x32_bf16 v[124:127], a[168:171], v[108:111], v[124:127]\n"
"ds_read_b128 a[140:143], v21 offset:0\n"
"v_mfma_f32_16x16x32_bf16 v[124:127], a[164:167], v[76:79], v[124:127]\n"
"buffer_load_dwordx4 v16, s[36:39], s52 offen offset:2048 lds\n"
"v_mfma_f32_16x16x32_bf16 v[124:127], a[160:163], v[44:47], v[124:127]\n"
"ds_read_b128 a[144:147], v18 offset:4096\n"
"v_mfma_f32_16x16x32_bf16 v[156:159], a[176:179], v[44:47], v[236:239]\n"
"v_exp_f32 v55, v55\n"
"v_mfma_f32_16x16x32_bf16 v[156:159], a[180:183], v[76:79], v[156:159]\n"
"ds_read_b128 a[148:151], v19 offset:4096\n"
"v_cvt_pk_bf16_f32 v160, v48, v49\n"
"v_mfma_f32_16x16x32_bf16 v[156:159], a[184:187], v[108:111], v[156:159]\n"
"v_cvt_pk_bf16_f32 v161, v50, v51\n"
"v_add_f32 v56, v232, v56\n"
"v_mfma_f32_16x16x32_bf16 v[156:159], a[188:191], v[140:143], v[156:159]\n"
"ds_read_b128 a[152:155], v20 offset:4096\n"
"v_add_f32 v57, v232, v57\n"
"v_mfma_f32_16x16x32_bf16 v[152:155], a[188:191], v[136:139], v[232:235]\n"
"buffer_load_dwordx4 v17, s[36:39], s52 offen offset:3072 lds\n"
"v_mfma_f32_16x16x32_bf16 v[152:155], a[184:187], v[104:107], v[152:155]\n"
"ds_read_b128 a[156:159], v21 offset:4096\n"
"v_add_f32 v58, v232, v58\n"
"v_mfma_f32_16x16x32_bf16 v[152:155], a[180:183], v[72:75], v[152:155]\n"
"v_add_f32 v59, v232, v59\n"
"v_mfma_f32_16x16x32_bf16 v[152:155], a[176:179], v[40:43], v[152:155]\n"
"v_exp_f32 v56, v56\n"
"v_mfma_f32_16x16x32_bf16 v[148:151], a[176:179], v[36:39], v[228:231]\n"
"v_exp_f32 v57, v57\n"
"v_mfma_f32_16x16x32_bf16 v[148:151], a[180:183], v[68:71], v[148:151]\n"
"v_exp_f32 v58, v58\n"
"v_mfma_f32_16x16x32_bf16 v[148:151], a[184:187], v[100:103], v[148:151]\n"
"v_exp_f32 v59, v59\n"
"v_mfma_f32_16x16x32_bf16 v[148:151], a[188:191], v[132:135], v[148:151]\n"
"v_cvt_pk_bf16_f32 v168, v52, v53\n"
"v_cvt_pk_bf16_f32 v169, v54, v55\n"
"v_mfma_f32_16x16x32_bf16 v[144:147], a[188:191], v[128:131], v[224:227]\n"
"v_add_f32 v60, v236, v60\n"
"v_add_f32 v61, v236, v61\n"
"v_mfma_f32_16x16x32_bf16 v[144:147], a[184:187], v[96:99], v[144:147]\n"
"v_add_f32 v62, v236, v62\n"
"v_add_f32 v63, v236, v63\n"
"v_mfma_f32_16x16x32_bf16 v[144:147], a[180:183], v[64:67], v[144:147]\n"
"v_exp_f32 v60, v60\n"
"v_mfma_f32_16x16x32_bf16 v[144:147], a[176:179], v[32:35], v[144:147]\n"
"v_exp_f32 v61, v61\n"
"v_permlane32_swap_b32 v208, v210\n"
"v_permlane32_swap_b32 v209, v211\n"
"buffer_load_dword v253, off, s[44:47], 0\n"
"buffer_store_dwordx4 v[208:211], v30, s[40:43], s57 offen sc1\n"
"v_exp_f32 v62, v62\n"
"v_exp_f32 v63, v63\n"
"v_cvt_pk_bf16_f32 v176, v56, v57\n"
"v_cvt_pk_bf16_f32 v177, v58, v59\n"
"v_exp_f32 v92, v92\n"
"v_exp_f32 v93, v93\n"
"v_exp_f32 v94, v94\n"
"v_exp_f32 v95, v95\n"
"v_permlane32_swap_b32 v216, v218\n"
"v_permlane32_swap_b32 v217, v219\n"
"buffer_load_dword v253, off, s[44:47], 0\n"
"buffer_store_dwordx4 v[216:219], v30, s[40:43], s9 offen sc1\n"
"v_cvt_pk_bf16_f32 v184, v60, v61\n"
"v_cvt_pk_bf16_f32 v185, v62, v63\n"
"v_exp_f32 v88, v88\n"
"v_exp_f32 v89, v89\n"
"v_exp_f32 v90, v90\n"
"v_exp_f32 v91, v91\n"
"v_cvt_pk_bf16_f32 v186, v92, v93\n"
"v_cvt_pk_bf16_f32 v187, v94, v95\n"
"v_exp_f32 v84, v84\n"
"v_exp_f32 v85, v85\n"
"v_exp_f32 v86, v86\n"
"v_exp_f32 v87, v87\n"
"v_cvt_pk_bf16_f32 v178, v88, v89\n"
"v_cvt_pk_bf16_f32 v179, v90, v91\n"
"v_exp_f32 v80, v80\n"
"v_exp_f32 v81, v81\n"
"v_exp_f32 v82, v82\n"
"v_exp_f32 v83, v83\n"
"v_cvt_pk_bf16_f32 v170, v84, v85\n"
"v_cvt_pk_bf16_f32 v171, v86, v87\n"
"v_exp_f32 v112, v112\n"
"v_exp_f32 v113, v113\n"
"v_exp_f32 v114, v114\n"
"v_exp_f32 v115, v115\n"
"v_cvt_pk_bf16_f32 v162, v80, v81\n"
"v_cvt_pk_bf16_f32 v163, v82, v83\n"
"v_exp_f32 v116, v116\n"
"v_exp_f32 v117, v117\n"
"v_exp_f32 v118, v118\n"
"v_exp_f32 v119, v119\n"
"v_cvt_pk_bf16_f32 v164, v112, v113\n"
"v_cvt_pk_bf16_f32 v165, v114, v115\n"
"v_exp_f32 v120, v120\n"
"v_exp_f32 v121, v121\n"
"v_exp_f32 v122, v122\n"
"v_exp_f32 v123, v123\n"
"v_cvt_pk_bf16_f32 v172, v116, v117\n"
"v_cvt_pk_bf16_f32 v173, v118, v119\n"
"v_exp_f32 v124, v124\n"
"v_exp_f32 v125, v125\n"
"v_exp_f32 v126, v126\n"
"v_exp_f32 v127, v127\n"
"v_cvt_pk_bf16_f32 v180, v120, v121\n"
"v_cvt_pk_bf16_f32 v181, v122, v123\n"
"v_exp_f32 v156, v156\n"
"v_exp_f32 v157, v157\n"
"v_exp_f32 v158, v158\n"
"v_exp_f32 v159, v159\n"
"v_cvt_pk_bf16_f32 v188, v124, v125\n"
"v_cvt_pk_bf16_f32 v189, v126, v127\n"
"v_exp_f32 v152, v152\n"
"v_exp_f32 v153, v153\n"
"v_exp_f32 v154, v154\n"
"v_exp_f32 v155, v155\n"
"v_cvt_pk_bf16_f32 v190, v156, v157\n"
"v_cvt_pk_bf16_f32 v191, v158, v159\n"
"v_exp_f32 v148, v148\n"
"v_exp_f32 v149, v149\n"
"v_exp_f32 v150, v150\n"
"v_exp_f32 v151, v151\n"
"v_cvt_pk_bf16_f32 v182, v152, v153\n"
"v_cvt_pk_bf16_f32 v183, v154, v155\n"
"v_cvt_pk_bf16_f32 v174, v148, v149\n"
"v_cvt_pk_bf16_f32 v175, v150, v151\n"
"s_waitcnt lgkmcnt(0)\n"
".LATT_X:\n"
"v_mfma_f32_16x16x32_bf16 v[48:51], a[128:131], v[32:35], v[224:227]\n"
"s_mov_b32 m0, s69\n"
"v_exp_f32 v144, v144\n"
"v_mfma_f32_16x16x32_bf16 v[48:51], a[132:135], v[64:67], v[48:51]\n"
"buffer_load_dwordx4 v12, s[32:35], s50 offen offset:0 lds\n"
"v_mfma_f32_16x16x32_bf16 v[48:51], a[136:139], v[96:99], v[48:51]\n"
"s_sub_u32 s70, s68, s67\n"
"v_exp_f32 v145, v145\n"
"v_mfma_f32_16x16x32_bf16 v[48:51], a[140:143], v[128:131], v[48:51]\n"
"s_sub_u32 s71, s67, s69\n"
"ds_read_b128 a[160:163], v18 offset:8192\n"
"v_cvt_pk_bf16_f32 v166, v144, v145\n"
"v_mfma_f32_16x16x32_bf16 v[52:55], a[140:143], v[132:135], v[228:231]\n"
"v_exp_f32 v146, v146\n"
"v_mfma_f32_16x16x32_bf16 v[52:55], a[136:139], v[100:103], v[52:55]\n"
"v_mfma_f32_16x16x32_bf16 v[52:55], a[132:135], v[68:71], v[52:55]\n"
"v_exp_f32 v147, v147\n"
"v_mfma_f32_16x16x32_bf16 v[52:55], a[128:131], v[36:39], v[52:55]\n"
"ds_read_b128 a[164:167], v19 offset:8192\n"
"v_cvt_pk_bf16_f32 v167, v146, v147\n"
"v_mfma_f32_16x16x32_bf16 v[56:59], a[128:131], v[40:43], v[232:235]\n"
"v_exp_f32 v48, v48\n"
"v_mfma_f32_16x16x32_bf16 v[56:59], a[132:135], v[72:75], v[56:59]\n"
"buffer_load_dwordx4 v13, s[32:35], s50 offen offset:1024 lds\n"
"v_mfma_f32_16x16x32_bf16 v[56:59], a[136:139], v[104:107], v[56:59]\n"
"v_exp_f32 v49, v49\n"
"v_mfma_f32_16x16x32_bf16 v[56:59], a[140:143], v[136:139], v[56:59]\n"
"ds_read_b128 a[168:171], v20 offset:8192\n"
"v_cvt_pk_bf16_f32 v192, v48, v49\n"
"v_mfma_f32_16x16x32_bf16 v[60:63], a[140:143], v[140:143], v[236:239]\n"
"v_exp_f32 v50, v50\n"
"v_mfma_f32_16x16x32_bf16 v[60:63], a[136:139], v[108:111], v[60:63]\n"
"v_mfma_f32_16x16x32_bf16 v[60:63], a[132:135], v[76:79], v[60:63]\n"
"v_exp_f32 v51, v51\n"
"v_mfma_f32_16x16x32_bf16 v[60:63], a[128:131], v[44:47], v[60:63]\n"
"s_waitcnt lgkmcnt(1)\n"
"ds_read_b128 a[172:175], v21 offset:8192\n"
"v_cvt_pk_bf16_f32 v193, v50, v51\n"
"v_mfma_f32_16x16x32_bf16 v[92:95], a[144:147], v[44:47], v[236:239]\n"
"v_exp_f32 v52, v52\n"
"v_mfma_f32_16x16x32_bf16 v[92:95], a[148:151], v[76:79], v[92:95]\n"
"buffer_load_dwordx4 v14, s[32:35], s50 offen offset:2048 lds\n"
"v_mfma_f32_16x16x32_bf16 v[92:95], a[152:155], v[108:111], v[92:95]\n"
"v_exp_f32 v53, v53\n"
"v_mfma_f32_16x16x32_bf16 v[92:95], a[156:159], v[140:143], v[92:95]\n"
"ds_read_b128 a[176:179], v18 offset:12288\n"
"v_cvt_pk_bf16_f32 v200, v52, v53\n"
"v_mfma_f32_16x16x32_bf16 v[88:91], a[156:159], v[136:139], v[232:235]\n"
"v_exp_f32 v54, v54\n"
"v_mfma_f32_16x16x32_bf16 v[88:91], a[152:155], v[104:107], v[88:91]\n"
"v_mfma_f32_16x16x32_bf16 v[88:91], a[148:151], v[72:75], v[88:91]\n"
"v_exp_f32 v55, v55\n"
"v_mfma_f32_16x16x32_bf16 v[88:91], a[144:147], v[40:43], v[88:91]\n"
"ds_read_b128 a[180:183], v19 offset:12288\n"
"v_cvt_pk_bf16_f32 v201, v54, v55\n"
"v_mfma_f32_16x16x32_bf16 v[84:87], a[144:147], v[36:39], v[228:231]\n"
"v_exp_f32 v56, v56\n"
"v_mfma_f32_16x16x32_bf16 v[84:87], a[148:151], v[68:71], v[84:87]\n"
"buffer_load_dwordx4 v15, s[32:35], s50 offen offset:3072 lds\n"
"v_mfma_f32_16x16x32_bf16 v[84:87], a[152:155], v[100:103], v[84:87]\n"
"s_add_u32 s50, s50, 16384\n"
"v_exp_f32 v57, v57\n"
"v_mfma_f32_16x16x32_bf16 v[84:87], a[156:159], v[132:135], v[84:87]\n"
"ds_read_b128 a[184:187], v20 offset:12288\n"
"v_cvt_pk_bf16_f32 v208, v56, v57\n"
"v_mfma_f32_16x16x32_bf16 v[80:83], a[156:159], v[128:131], v[224:227]\n"
"v_exp_f32 v58, v58\n"
"v_mfma_f32_16x16x32_bf16 v[80:83], a[152:155], v[96:99], v[80:83]\n"
"v_mfma_f32_16x16x32_bf16 v[80:83], a[148:151], v[64:67], v[80:83]\n"
"v_exp_f32 v59, v59\n"
"v_mfma_f32_16x16x32_bf16 v[80:83], a[144:147], v[32:35], v[80:83]\n"
"ds_read_b128 a[188:191], v21 offset:12288\n"
"v_cvt_pk_bf16_f32 v209, v58, v59\n"
"v_mfma_f32_16x16x32_bf16 v[112:115], a[160:163], v[32:35], v[224:227]\n"
"v_exp_f32 v60, v60\n"
"v_mfma_f32_16x16x32_bf16 v[112:115], a[164:167], v[64:67], v[112:115]\n"
"s_waitcnt lgkmcnt(1)\n"
"v_add_u32 v18, s70, v18\n"
"v_add_u32 v19, s70, v19\n"
"v_mfma_f32_16x16x32_bf16 v[112:115], a[168:171], v[96:99], v[112:115]\n"
"v_exp_f32 v61, v61\n"
"v_mfma_f32_16x16x32_bf16 v[112:115], a[172:175], v[128:131], v[112:115]\n"
"ds_read_b64_tr_b16 a[192:193], v22 offset:0\n"
"v_cvt_pk_bf16_f32 v216, v60, v61\n"
"v_mfma_f32_16x16x32_bf16 v[116:119], a[172:175], v[132:135], v[228:231]\n"
"s_add_u32 m0, s68, 49152\n"
"v_exp_f32 v62, v62\n"
"v_mfma_f32_16x16x32_bf16 v[116:119], a[168:171], v[100:103], v[116:119]\n"
"buffer_load_dwordx4 v16, s[36:39], s51 offen offset:0 lds\n"
"v_mfma_f32_16x16x32_bf16 v[116:119], a[164:167], v[68:71], v[116:119]\n"
"v_exp_f32 v63, v63\n"
"v_mfma_f32_16x16x32_bf16 v[116:119], a[160:163], v[36:39], v[116:119]\n"
"s_mov_b32 s10, s67\n"
"ds_read_b64_tr_b16 a[194:195], v22 offset:4096\n"
"v_cvt_pk_bf16_f32 v217, v62, v63\n"
"v_mfma_f32_16x16x32_bf16 v[120:123], a[160:163], v[40:43], v[232:235]\n"
"s_mov_b32 s67, s68\n"
"v_exp_f32 v92, v92\n"
"v_mfma_f32_16x16x32_bf16 v[120:123], a[164:167], v[72:75], v[120:123]\n"
"s_mov_b32 s68, s69\n"
"ds_read_b64_tr_b16 a[196:197], v22 offset:8192\n"
"v_add_u32 v20, s70, v20\n"
"v_mfma_f32_16x16x32_bf16 v[120:123], a[168:171], v[104:107], v[120:123]\n"
"s_mov_b32 s69, s10\n"
"v_exp_f32 v93, v93\n"
"v_mfma_f32_16x16x32_bf16 v[120:123], a[172:175], v[136:139], v[120:123]\n"
"ds_read_b64_tr_b16 a[198:199], v22 offset:12288\n"
"v_cvt_pk_bf16_f32 v218, v92, v93\n"
"v_mfma_f32_16x16x32_bf16 v[124:127], a[172:175], v[140:143], v[236:239]\n"
"v_exp_f32 v94, v94\n"
"v_mfma_f32_16x16x32_bf16 v[124:127], a[168:171], v[108:111], v[124:127]\n"
"buffer_load_dwordx4 v17, s[36:39], s51 offen offset:1024 lds\n"
"v_mfma_f32_16x16x32_bf16 v[124:127], a[164:167], v[76:79], v[124:127]\n"
"v_exp_f32 v95, v95\n"
"v_mfma_f32_16x16x32_bf16 v[124:127], a[160:163], v[44:47], v[124:127]\n"
"ds_read_b64_tr_b16 a[200:201], v23 offset:0\n"
"v_cvt_pk_bf16_f32 v219, v94, v95\n"
"v_mfma_f32_16x16x32_bf16 v[156:159], a[176:179], v[44:47], v[236:239]\n"
"v_exp_f32 v88, v88\n"
"v_mfma_f32_16x16x32_bf16 v[156:159], a[180:183], v[76:79], v[156:159]\n"
"ds_read_b64_tr_b16 a[202:203], v23 offset:4096\n"
"v_add_u32 v22, s71, v22\n"
"v_mfma_f32_16x16x32_bf16 v[156:159], a[184:187], v[108:111], v[156:159]\n"
"s_waitcnt lgkmcnt(2)\n"
"v_exp_f32 v89, v89\n"
"v_mfma_f32_16x16x32_bf16 v[156:159], a[188:191], v[140:143], v[156:159]\n"
"ds_read_b64_tr_b16 a[204:205], v23 offset:8192\n"
"v_cvt_pk_bf16_f32 v210, v88, v89\n"
"v_mfma_f32_16x16x32_bf16 v[152:155], a[188:191], v[136:139], v[232:235]\n"
"v_exp_f32 v90, v90\n"
"v_mfma_f32_16x16x32_bf16 v[152:155], a[184:187], v[104:107], v[152:155]\n"
"ds_read_b64_tr_b16 a[206:207], v23 offset:12288\n"
"v_add_u32 v23, s71, v23\n"
"v_mfma_f32_16x16x32_bf16 v[152:155], a[180:183], v[72:75], v[152:155]\n"
"v_exp_f32 v91, v91\n"
"v_mfma_f32_16x16x32_bf16 v[152:155], a[176:179], v[40:43], v[152:155]\n"
"ds_read_b64_tr_b16 a[208:209], v24 offset:0\n"
"v_cvt_pk_bf16_f32 v211, v90, v91\n"
"v_mfma_f32_16x16x32_bf16 v[148:151], a[176:179], v[36:39], v[228:231]\n"
"v_exp_f32 v84, v84\n"
"v_mfma_f32_16x16x32_bf16 v[148:151], a[180:183], v[68:71], v[148:151]\n"
"buffer_load_dwordx4 v16, s[36:39], s51 offen offset:2048 lds\n"
"v_mfma_f32_16x16x32_bf16 v[148:151], a[184:187], v[100:103], v[148:151]\n"
"v_exp_f32 v85, v85\n"
"v_mfma_f32_16x16x32_bf16 v[148:151], a[188:191], v[132:135], v[148:151]\n"
"ds_read_b64_tr_b16 a[210:211], v24 offset:4096\n"
"v_cvt_pk_bf16_f32 v202, v84, v85\n"
"v_mfma_f32_16x16x32_bf16 v[144:147], a[188:191], v[128:131], v[224:227]\n"
"v_exp_f32 v86, v86\n"
"v_mfma_f32_16x16x32_bf16 v[144:147], a[184:187], v[96:99], v[144:147]\n"
"ds_read_b64_tr_b16 a[212:213], v24 offset:8192\n"
"v_add_u32 v21, s70, v21\n"
"v_mfma_f32_16x16x32_bf16 v[144:147], a[180:183], v[64:67], v[144:147]\n"
"v_exp_f32 v87, v87\n"
"v_mfma_f32_16x16x32_bf16 v[144:147], a[176:179], v[32:35], v[144:147]\n"
"ds_read_b64_tr_b16 a[214:215], v24 offset:12288\n"
"v_cvt_pk_bf16_f32 v203, v86, v87\n"
"v_mfma_f32_16x16x32_bf16 a[0:3], a[192:195], v[160:163], a[0:3]\n"
"v_exp_f32 v80, v80\n"
"v_mfma_f32_16x16x32_bf16 a[0:3], a[196:199], v[164:167], a[0:3]\n"
"buffer_load_dwordx4 v17, s[36:39], s51 offen offset:3072 lds\n"
"v_mfma_f32_16x16x32_bf16 a[4:7], a[196:199], v[172:175], a[4:7]\n"
"s_add_u32 s51, s51, 16384\n"
"v_exp_f32 v81, v81\n"
"v_mfma_f32_16x16x32_bf16 a[4:7], a[192:195], v[168:171], a[4:7]\n"
"ds_read_b64_tr_b16 a[216:217], v25 offset:0\n"
"v_cvt_pk_bf16_f32 v194, v80, v81\n"
"v_mfma_f32_16x16x32_bf16 a[8:11], a[192:195], v[176:179], a[8:11]\n"
"v_exp_f32 v82, v82\n"
"v_mfma_f32_16x16x32_bf16 a[8:11], a[196:199], v[180:183], a[8:11]\n"
"ds_read_b64_tr_b16 a[218:219], v25 offset:4096\n"
"v_add_u32 v24, s71, v24\n"
"v_mfma_f32_16x16x32_bf16 a[12:15], a[196:199], v[188:191], a[12:15]\n"
"v_exp_f32 v83, v83\n"
"v_mfma_f32_16x16x32_bf16 a[12:15], a[192:195], v[184:187], a[12:15]\n"
"s_waitcnt lgkmcnt(2)\n"
"ds_read_b64_tr_b16 a[220:221], v25 offset:8192\n"
"v_cvt_pk_bf16_f32 v195, v82, v83\n"
"v_mfma_f32_16x16x32_bf16 a[28:31], a[200:203], v[184:187], a[28:31]\n"
"v_exp_f32 v112, v112\n"
"v_mfma_f32_16x16x32_bf16 a[28:31], a[204:207], v[188:191], a[28:31]\n"
"ds_read_b64_tr_b16 a[222:223], v25 offset:12288\n"
"v_add_u32 v25, s71, v25\n"
"v_mfma_f32_16x16x32_bf16 a[24:27], a[204:207], v[180:183], a[24:27]\n"
"v_exp_f32 v113, v113\n"
"v_mfma_f32_16x16x32_bf16 a[24:27], a[200:203], v[176:179], a[24:27]\n"
"ds_read_b64_tr_b16 a[192:193], v26 offset:0\n"
"v_cvt_pk_bf16_f32 v196, v112, v113\n"
"v_mfma_f32_16x16x32_bf16 a[20:23], a[200:203], v[168:171], a[20:23]\n"
"v_exp_f32 v114, v114\n"
"v_mfma_f32_16x16x32_bf16 a[20:23], a[204:207], v[172:175], a[20:23]\n"
"buffer_store_dwordx4 a[240:243], v1, s[40:43], s56 offen offset:0 sc1\n"
"v_mfma_f32_16x16x32_bf16 a[16:19], a[204:207], v[164:167], a[16:19]\n"
"v_exp_f32 v115, v115\n"
"v_mfma_f32_16x16x32_bf16 a[16:19], a[200:203], v[160:163], a[16:19]\n"
"ds_read_b64_tr_b16 a[194:195], v26 offset:4096\n"
"v_cvt_pk_bf16_f32 v197, v114, v115\n"
"v_mfma_f32_16x16x32_bf16 a[32:35], a[208:211], v[160:163], a[32:35]\n"
"v_exp_f32 v116, v116\n"
"v_mfma_f32_16x16x32_bf16 a[32:35], a[212:215], v[164:167], a[32:35]\n"
"ds_read_b64_tr_b16 a[196:197], v26 offset:8192\n"
"v_mfma_f32_16x16x32_bf16 a[36:39], a[212:215], v[172:175], a[36:39]\n"
"v_exp_f32 v117, v117\n"
"v_mfma_f32_16x16x32_bf16 a[36:39], a[208:211], v[168:171], a[36:39]\n"
"ds_read_b64_tr_b16 a[198:199], v26 offset:12288\n"
"v_cvt_pk_bf16_f32 v204, v116, v117\n"
"v_mfma_f32_16x16x32_bf16 a[40:43], a[208:211], v[176:179], a[40:43]\n"
"v_exp_f32 v118, v118\n"
"v_mfma_f32_16x16x32_bf16 a[40:43], a[212:215], v[180:183], a[40:43]\n"
"ds_read_b64_tr_b16 a[200:201], v27 offset:0\n"
"v_add_u32 v26, s71, v26\n"
"v_mfma_f32_16x16x32_bf16 a[44:47], a[212:215], v[188:191], a[44:47]\n"
"v_exp_f32 v119, v119\n"
"v_mfma_f32_16x16x32_bf16 a[44:47], a[208:211], v[184:187], a[44:47]\n"
"s_waitcnt lgkmcnt(2)\n"
"ds_read_b64_tr_b16 a[202:203], v27 offset:4096\n"
"v_cvt_pk_bf16_f32 v205, v118, v119\n"
"v_mfma_f32_16x16x32_bf16 a[60:63], a[216:219], v[184:187], a[60:63]\n"
"v_exp_f32 v120, v120\n"
"v_mfma_f32_16x16x32_bf16 a[60:63], a[220:223], v[188:191], a[60:63]\n"
"v_mfma_f32_16x16x32_bf16 a[56:59], a[220:223], v[180:183], a[56:59]\n"
"v_exp_f32 v121, v121\n"
"v_mfma_f32_16x16x32_bf16 a[56:59], a[216:219], v[176:179], a[56:59]\n"
"ds_read_b64_tr_b16 a[204:205], v27 offset:8192\n"
"v_cvt_pk_bf16_f32 v212, v120, v121\n"
"v_mfma_f32_16x16x32_bf16 a[52:55], a[216:219], v[168:171], a[52:55]\n"
"v_exp_f32 v122, v122\n"
"v_mfma_f32_16x16x32_bf16 a[52:55], a[220:223], v[172:175], a[52:55]\n"
"ds_read_b64_tr_b16 a[206:207], v27 offset:12288\n"
"v_add_u32 v27, s71, v27\n"
"v_mfma_f32_16x16x32_bf16 a[48:51], a[220:223], v[164:167], a[48:51]\n"
"v_exp_f32 v123, v123\n"
"v_mfma_f32_16x16x32_bf16 a[48:51], a[216:219], v[160:163], a[48:51]\n"
"ds_read_b64_tr_b16 a[208:209], v28 offset:0\n"
"v_cvt_pk_bf16_f32 v213, v122, v123\n"
"v_mfma_f32_16x16x32_bf16 a[64:67], a[192:195], v[160:163], a[64:67]\n"
"s_waitcnt lgkmcnt(3)\n"
"v_exp_f32 v124, v124\n"
"v_mfma_f32_16x16x32_bf16 a[64:67], a[196:199], v[164:167], a[64:67]\n"
"ds_read_b64_tr_b16 a[210:211], v28 offset:4096\n"
"v_mfma_f32_16x16x32_bf16 a[68:71], a[196:199], v[172:175], a[68:71]\n"
"v_exp_f32 v125, v125\n"
"v_mfma_f32_16x16x32_bf16 a[68:71], a[192:195], v[168:171], a[68:71]\n"
"ds_read_b64_tr_b16 a[212:213], v28 offset:8192\n"
"v_cvt_pk_bf16_f32 v220, v124, v125\n"
"v_mfma_f32_16x16x32_bf16 a[72:75], a[192:195], v[176:179], a[72:75]\n"
"v_exp_f32 v126, v126\n"
"v_mfma_f32_16x16x32_bf16 a[72:75], a[196:199], v[180:183], a[72:75]\n"
"buffer_store_dwordx4 a[244:247], v1, s[40:43], s56 offen offset:1024 sc1\n"
"v_mfma_f32_16x16x32_bf16 a[76:79], a[196:199], v[188:191], a[76:79]\n"
"v_exp_f32 v127, v127\n"
"v_mfma_f32_16x16x32_bf16 a[76:79], a[192:195], v[184:187], a[76:79]\n"
"ds_read_b64_tr_b16 a[214:215], v28 offset:12288\n"
"v_cvt_pk_bf16_f32 v221, v126, v127\n"
"v_mfma_f32_16x16x32_bf16 a[92:95], a[200:203], v[184:187], a[92:95]\n"
"s_waitcnt lgkmcnt(2)\n"
"v_exp_f32 v156, v156\n"
"v_mfma_f32_16x16x32_bf16 a[92:95], a[204:207], v[188:191], a[92:95]\n"
"ds_read_b64_tr_b16 a[216:217], v29 offset:0\n"
"v_add_u32 v28, s71, v28\n"
"v_mfma_f32_16x16x32_bf16 a[88:91], a[204:207], v[180:183], a[88:91]\n"
"v_exp_f32 v157, v157\n"
"v_mfma_f32_16x16x32_bf16 a[88:91], a[200:203], v[176:179], a[88:91]\n"
"ds_read_b64_tr_b16 a[218:219], v29 offset:4096\n"
"v_cvt_pk_bf16_f32 v222, v156, v157\n"
"v_mfma_f32_16x16x32_bf16 a[84:87], a[200:203], v[168:171], a[84:87]\n"
"v_exp_f32 v158, v158\n"
"v_mfma_f32_16x16x32_bf16 a[84:87], a[204:207], v[172:175], a[84:87]\n"
"ds_read_b64_tr_b16 a[220:221], v29 offset:8192\n"
"v_mfma_f32_16x16x32_bf16 a[80:83], a[204:207], v[164:167], a[80:83]\n"
"v_exp_f32 v159, v159\n"
"v_mfma_f32_16x16x32_bf16 a[80:83], a[200:203], v[160:163], a[80:83]\n"
"ds_read_b64_tr_b16 a[222:223], v29 offset:12288\n"
"v_cvt_pk_bf16_f32 v223, v158, v159\n"
"v_mfma_f32_16x16x32_bf16 a[96:99], a[208:211], v[160:163], a[96:99]\n"
"s_waitcnt lgkmcnt(3)\n"
"v_exp_f32 v152, v152\n"
"v_mfma_f32_16x16x32_bf16 a[96:99], a[212:215], v[164:167], a[96:99]\n"
"v_mfma_f32_16x16x32_bf16 a[100:103], a[212:215], v[172:175], a[100:103]\n"
"v_exp_f32 v153, v153\n"
"v_mfma_f32_16x16x32_bf16 a[100:103], a[208:211], v[168:171], a[100:103]\n"
"v_cvt_pk_bf16_f32 v214, v152, v153\n"
"v_add_u32 v29, s71, v29\n"
"v_mfma_f32_16x16x32_bf16 a[104:107], a[208:211], v[176:179], a[104:107]\n"
"v_exp_f32 v154, v154\n"
"v_mfma_f32_16x16x32_bf16 a[104:107], a[212:215], v[180:183], a[104:107]\n"
"s_waitcnt vmcnt(14) lgkmcnt(0)\n"
"s_barrier\n"
"v_mfma_f32_16x16x32_bf16 a[108:111], a[212:215], v[188:191], a[108:111]\n"
"v_exp_f32 v155, v155\n"
"v_mfma_f32_16x16x32_bf16 a[108:111], a[208:211], v[184:187], a[108:111]\n"
"ds_read_b128 a[128:131], v18 offset:0\n"
"v_cvt_pk_bf16_f32 v215, v154, v155\n"
"v_mfma_f32_16x16x32_bf16 a[124:127], a[216:219], v[184:187], a[124:127]\n"
"v_exp_f32 v148, v148\n"
"v_mfma_f32_16x16x32_bf16 a[124:127], a[220:223], v[188:191], a[124:127]\n"
"ds_read_b128 a[132:135], v19 offset:0\n"
"v_mfma_f32_16x16x32_bf16 a[120:123], a[220:223], v[180:183], a[120:123]\n"
"v_exp_f32 v149, v149\n"
"v_mfma_f32_16x16x32_bf16 a[120:123], a[216:219], v[176:179], a[120:123]\n"
"ds_read_b128 a[136:139], v20 offset:0\n"
"v_cvt_pk_bf16_f32 v206, v148, v149\n"
"v_mfma_f32_16x16x32_bf16 a[116:119], a[216:219], v[168:171], a[116:119]\n"
"v_exp_f32 v150, v150\n"
"v_mfma_f32_16x16x32_bf16 a[116:119], a[220:223], v[172:175], a[116:119]\n"
"ds_read_b128 a[140:143], v21 offset:0\n"
"v_mfma_f32_16x16x32_bf16 a[112:115], a[220:223], v[164:167], a[112:115]\n"
"v_exp_f32 v151, v151\n"
"v_mfma_f32_16x16x32_bf16 a[112:115], a[216:219], v[160:163], a[112:115]\n"
"ds_read_b128 a[144:147], v18 offset:4096\n"
"v_cvt_pk_bf16_f32 v207, v150, v151\n"
"v_smfmac_f32_16x16x64_bf16 a[224:227], v[240:243], v[160:167], v244\n"
"ds_read_b128 a[148:151], v19 offset:4096\n"
"v_smfmac_f32_16x16x64_bf16 a[228:231], v[240:243], v[168:175], v244\n"
"ds_read_b128 a[152:155], v20 offset:4096\n"
"v_smfmac_f32_16x16x64_bf16 a[232:235], v[240:243], v[176:183], v244\n"
"ds_read_b128 a[156:159], v21 offset:4096\n"
"v_smfmac_f32_16x16x64_bf16 a[236:239], v[240:243], v[184:191], v244\n"
"s_waitcnt lgkmcnt(4)\n"
"v_mfma_f32_16x16x32_bf16 v[48:51], a[128:131], v[32:35], v[224:227]\n"
"s_mov_b32 m0, s69\n"
"v_exp_f32 v144, v144\n"
"v_mfma_f32_16x16x32_bf16 v[48:51], a[132:135], v[64:67], v[48:51]\n"
"buffer_load_dwordx4 v12, s[32:35], s50 offen offset:0 lds\n"
"v_mfma_f32_16x16x32_bf16 v[48:51], a[136:139], v[96:99], v[48:51]\n"
"s_sub_u32 s70, s68, s67\n"
"v_exp_f32 v145, v145\n"
"v_mfma_f32_16x16x32_bf16 v[48:51], a[140:143], v[128:131], v[48:51]\n"
"s_sub_u32 s71, s67, s69\n"
"ds_read_b128 a[160:163], v18 offset:8192\n"
"v_cvt_pk_bf16_f32 v198, v144, v145\n"
"v_mfma_f32_16x16x32_bf16 v[52:55], a[140:143], v[132:135], v[228:231]\n"
"v_exp_f32 v146, v146\n"
"v_mfma_f32_16x16x32_bf16 v[52:55], a[136:139], v[100:103], v[52:55]\n"
"v_mfma_f32_16x16x32_bf16 v[52:55], a[132:135], v[68:71], v[52:55]\n"
"v_exp_f32 v147, v147\n"
"v_mfma_f32_16x16x32_bf16 v[52:55], a[128:131], v[36:39], v[52:55]\n"
"ds_read_b128 a[164:167], v19 offset:8192\n"
"v_cvt_pk_bf16_f32 v199, v146, v147\n"
"v_mfma_f32_16x16x32_bf16 v[56:59], a[128:131], v[40:43], v[232:235]\n"
"v_exp_f32 v48, v48\n"
"v_mfma_f32_16x16x32_bf16 v[56:59], a[132:135], v[72:75], v[56:59]\n"
"buffer_load_dwordx4 v13, s[32:35], s50 offen offset:1024 lds\n"
"v_mfma_f32_16x16x32_bf16 v[56:59], a[136:139], v[104:107], v[56:59]\n"
"v_exp_f32 v49, v49\n"
"v_mfma_f32_16x16x32_bf16 v[56:59], a[140:143], v[136:139], v[56:59]\n"
"ds_read_b128 a[168:171], v20 offset:8192\n"
"v_cvt_pk_bf16_f32 v160, v48, v49\n"
"v_mfma_f32_16x16x32_bf16 v[60:63], a[140:143], v[140:143], v[236:239]\n"
"v_exp_f32 v50, v50\n"
"v_mfma_f32_16x16x32_bf16 v[60:63], a[136:139], v[108:111], v[60:63]\n"
"v_mfma_f32_16x16x32_bf16 v[60:63], a[132:135], v[76:79], v[60:63]\n"
"v_exp_f32 v51, v51\n"
"v_mfma_f32_16x16x32_bf16 v[60:63], a[128:131], v[44:47], v[60:63]\n"
"s_waitcnt lgkmcnt(1)\n"
"ds_read_b128 a[172:175], v21 offset:8192\n"
"v_cvt_pk_bf16_f32 v161, v50, v51\n"
"v_mfma_f32_16x16x32_bf16 v[92:95], a[144:147], v[44:47], v[236:239]\n"
"v_exp_f32 v52, v52\n"
"v_mfma_f32_16x16x32_bf16 v[92:95], a[148:151], v[76:79], v[92:95]\n"
"buffer_load_dwordx4 v14, s[32:35], s50 offen offset:2048 lds\n"
"v_mfma_f32_16x16x32_bf16 v[92:95], a[152:155], v[108:111], v[92:95]\n"
"v_exp_f32 v53, v53\n"
"v_mfma_f32_16x16x32_bf16 v[92:95], a[156:159], v[140:143], v[92:95]\n"
"ds_read_b128 a[176:179], v18 offset:12288\n"
"v_cvt_pk_bf16_f32 v168, v52, v53\n"
"v_mfma_f32_16x16x32_bf16 v[88:91], a[156:159], v[136:139], v[232:235]\n"
"v_exp_f32 v54, v54\n"
"v_mfma_f32_16x16x32_bf16 v[88:91], a[152:155], v[104:107], v[88:91]\n"
"v_mfma_f32_16x16x32_bf16 v[88:91], a[148:151], v[72:75], v[88:91]\n"
"v_exp_f32 v55, v55\n"
"v_mfma_f32_16x16x32_bf16 v[88:91], a[144:147], v[40:43], v[88:91]\n"
"ds_read_b128 a[180:183], v19 offset:12288\n"
"v_cvt_pk_bf16_f32 v169, v54, v55\n"
"v_mfma_f32_16x16x32_bf16 v[84:87], a[144:147], v[36:39], v[228:231]\n"
"v_exp_f32 v56, v56\n"
"v_mfma_f32_16x16x32_bf16 v[84:87], a[148:151], v[68:71], v[84:87]\n"
"buffer_load_dwordx4 v15, s[32:35], s50 offen offset:3072 lds\n"
"v_mfma_f32_16x16x32_bf16 v[84:87], a[152:155], v[100:103], v[84:87]\n"
"s_add_u32 s50, s50, 16384\n"
"v_exp_f32 v57, v57\n"
"v_mfma_f32_16x16x32_bf16 v[84:87], a[156:159], v[132:135], v[84:87]\n"
"ds_read_b128 a[184:187], v20 offset:12288\n"
"v_cvt_pk_bf16_f32 v176, v56, v57\n"
"v_mfma_f32_16x16x32_bf16 v[80:83], a[156:159], v[128:131], v[224:227]\n"
"v_exp_f32 v58, v58\n"
"v_mfma_f32_16x16x32_bf16 v[80:83], a[152:155], v[96:99], v[80:83]\n"
"v_mfma_f32_16x16x32_bf16 v[80:83], a[148:151], v[64:67], v[80:83]\n"
"v_exp_f32 v59, v59\n"
"v_mfma_f32_16x16x32_bf16 v[80:83], a[144:147], v[32:35], v[80:83]\n"
"ds_read_b128 a[188:191], v21 offset:12288\n"
"v_cvt_pk_bf16_f32 v177, v58, v59\n"
"v_mfma_f32_16x16x32_bf16 v[112:115], a[160:163], v[32:35], v[224:227]\n"
"v_exp_f32 v60, v60\n"
"v_mfma_f32_16x16x32_bf16 v[112:115], a[164:167], v[64:67], v[112:115]\n"
"s_waitcnt lgkmcnt(1)\n"
"v_add_u32 v18, s70, v18\n"
"v_add_u32 v19, s70, v19\n"
"v_mfma_f32_16x16x32_bf16 v[112:115], a[168:171], v[96:99], v[112:115]\n"
"v_exp_f32 v61, v61\n"
"v_mfma_f32_16x16x32_bf16 v[112:115], a[172:175], v[128:131], v[112:115]\n"
"ds_read_b64_tr_b16 a[192:193], v22 offset:0\n"
"v_cvt_pk_bf16_f32 v184, v60, v61\n"
"v_mfma_f32_16x16x32_bf16 v[116:119], a[172:175], v[132:135], v[228:231]\n"
"s_add_u32 m0, s68, 49152\n"
"v_exp_f32 v62, v62\n"
"v_mfma_f32_16x16x32_bf16 v[116:119], a[168:171], v[100:103], v[116:119]\n"
"buffer_load_dwordx4 v16, s[36:39], s51 offen offset:0 lds\n"
"v_mfma_f32_16x16x32_bf16 v[116:119], a[164:167], v[68:71], v[116:119]\n"
"v_exp_f32 v63, v63\n"
"v_mfma_f32_16x16x32_bf16 v[116:119], a[160:163], v[36:39], v[116:119]\n"
"s_mov_b32 s10, s67\n"
"ds_read_b64_tr_b16 a[194:195], v22 offset:4096\n"
"v_cvt_pk_bf16_f32 v185, v62, v63\n"
"v_mfma_f32_16x16x32_bf16 v[120:123], a[160:163], v[40:43], v[232:235]\n"
"s_mov_b32 s67, s68\n"
"v_exp_f32 v92, v92\n"
"v_mfma_f32_16x16x32_bf16 v[120:123], a[164:167], v[72:75], v[120:123]\n"
"s_mov_b32 s68, s69\n"
"ds_read_b64_tr_b16 a[196:197], v22 offset:8192\n"
"v_add_u32 v20, s70, v20\n"
"v_mfma_f32_16x16x32_bf16 v[120:123], a[168:171], v[104:107], v[120:123]\n"
"s_mov_b32 s69, s10\n"
"v_exp_f32 v93, v93\n"
"v_mfma_f32_16x16x32_bf16 v[120:123], a[172:175], v[136:139], v[120:123]\n"
"ds_read_b64_tr_b16 a[198:199], v22 offset:12288\n"
"v_cvt_pk_bf16_f32 v186, v92, v93\n"
"v_mfma_f32_16x16x32_bf16 v[124:127], a[172:175], v[140:143], v[236:239]\n"
"v_exp_f32 v94, v94\n"
"v_mfma_f32_16x16x32_bf16 v[124:127], a[168:171], v[108:111], v[124:127]\n"
"buffer_load_dwordx4 v17, s[36:39], s51 offen offset:1024 lds\n"
"v_mfma_f32_16x16x32_bf16 v[124:127], a[164:167], v[76:79], v[124:127]\n"
"v_exp_f32 v95, v95\n"
"v_mfma_f32_16x16x32_bf16 v[124:127], a[160:163], v[44:47], v[124:127]\n"
"ds_read_b64_tr_b16 a[200:201], v23 offset:0\n"
"v_cvt_pk_bf16_f32 v187, v94, v95\n"
"v_mfma_f32_16x16x32_bf16 v[156:159], a[176:179], v[44:47], v[236:239]\n"
"v_exp_f32 v88, v88\n"
"v_mfma_f32_16x16x32_bf16 v[156:159], a[180:183], v[76:79], v[156:159]\n"
"ds_read_b64_tr_b16 a[202:203], v23 offset:4096\n"
"v_add_u32 v22, s71, v22\n"
"v_mfma_f32_16x16x32_bf16 v[156:159], a[184:187], v[108:111], v[156:159]\n"
"s_waitcnt lgkmcnt(2)\n"
"v_exp_f32 v89, v89\n"
"v_mfma_f32_16x16x32_bf16 v[156:159], a[188:191], v[140:143], v[156:159]\n"
"ds_read_b64_tr_b16 a[204:205], v23 offset:8192\n"
"v_cvt_pk_bf16_f32 v178, v88, v89\n"
"v_mfma_f32_16x16x32_bf16 v[152:155], a[188:191], v[136:139], v[232:235]\n"
"v_exp_f32 v90, v90\n"
"v_mfma_f32_16x16x32_bf16 v[152:155], a[184:187], v[104:107], v[152:155]\n"
"ds_read_b64_tr_b16 a[206:207], v23 offset:12288\n"
"v_add_u32 v23, s71, v23\n"
"v_mfma_f32_16x16x32_bf16 v[152:155], a[180:183], v[72:75], v[152:155]\n"
".LPAD6:\n"
"s_cbranch_execz .LPAD9\n"
"v_exp_f32 v91, v91\n"
"v_mfma_f32_16x16x32_bf16 v[152:155], a[176:179], v[40:43], v[152:155]\n"
"ds_read_b64_tr_b16 a[208:209], v24 offset:0\n"
"v_cvt_pk_bf16_f32 v179, v90, v91\n"
"v_mfma_f32_16x16x32_bf16 v[148:151], a[176:179], v[36:39], v[228:231]\n"
"v_exp_f32 v84, v84\n"
"v_mfma_f32_16x16x32_bf16 v[148:151], a[180:183], v[68:71], v[148:151]\n"
"buffer_load_dwordx4 v16, s[36:39], s51 offen offset:2048 lds\n"
"v_mfma_f32_16x16x32_bf16 v[148:151], a[184:187], v[100:103], v[148:151]\n"
"v_exp_f32 v85, v85\n"
"v_mfma_f32_16x16x32_bf16 v[148:151], a[188:191], v[132:135], v[148:151]\n"
"ds_read_b64_tr_b16 a[210:211], v24 offset:4096\n"
"v_cvt_pk_bf16_f32 v170, v84, v85\n"
"v_mfma_f32_16x16x32_bf16 v[144:147], a[188:191], v[128:131], v[224:227]\n"
"v_exp_f32 v86, v86\n"
"v_mfma_f32_16x16x32_bf16 v[144:147], a[184:187], v[96:99], v[144:147]\n"
"ds_read_b64_tr_b16 a[212:213], v24 offset:8192\n"
"v_add_u32 v21, s70, v21\n"
"v_mfma_f32_16x16x32_bf16 v[144:147], a[180:183], v[64:67], v[144:147]\n"
"v_exp_f32 v87, v87\n"
"v_mfma_f32_16x16x32_bf16 v[144:147], a[176:179], v[32:35], v[144:147]\n"
"ds_read_b64_tr_b16 a[214:215], v24 offset:12288\n"
"v_cvt_pk_bf16_f32 v171, v86, v87\n"
"v_mfma_f32_16x16x32_bf16 a[0:3], a[192:195], v[192:195], a[0:3]\n"
"v_exp_f32 v80, v80\n"
"v_mfma_f32_16x16x32_bf16 a[0:3], a[196:199], v[196:199], a[0:3]\n"
"buffer_load_dwordx4 v17, s[36:39], s51 offen offset:3072 lds\n"
"v_mfma_f32_16x16x32_bf16 a[4:7], a[196:199], v[204:207], a[4:7]\n"
"s_add_u32 s51, s51, 16384\n"
"v_exp_f32 v81, v81\n"
"v_mfma_f32_16x16x32_bf16 a[4:7], a[192:195], v[200:203], a[4:7]\n"
"ds_read_b64_tr_b16 a[216:217], v25 offset:0\n"
"v_cvt_pk_bf16_f32 v162, v80, v81\n"
"v_mfma_f32_16x16x32_bf16 a[8:11], a[192:195], v[208:211], a[8:11]\n"
"v_exp_f32 v82, v82\n"
"v_mfma_f32_16x16x32_bf16 a[8:11], a[196:199], v[212:215], a[8:11]\n"
"ds_read_b64_tr_b16 a[218:219], v25 offset:4096\n"
"v_add_u32 v24, s71, v24\n"
"v_mfma_f32_16x16x32_bf16 a[12:15], a[196:199], v[220:223], a[12:15]\n"
"v_exp_f32 v83, v83\n"
"v_mfma_f32_16x16x32_bf16 a[12:15], a[192:195], v[216:219], a[12:15]\n"
"s_waitcnt lgkmcnt(2)\n"
"ds_read_b64_tr_b16 a[220:221], v25 offset:8192\n"
"v_cvt_pk_bf16_f32 v163, v82, v83\n"
"v_mfma_f32_16x16x32_bf16 a[28:31], a[200:203], v[216:219], a[28:31]\n"
"v_exp_f32 v112, v112\n"
"v_mfma_f32_16x16x32_bf16 a[28:31], a[204:207], v[220:223], a[28:31]\n"
"ds_read_b64_tr_b16 a[222:223], v25 offset:12288\n"
"v_add_u32 v25, s71, v25\n"
"v_mfma_f32_16x16x32_bf16 a[24:27], a[204:207], v[212:215], a[24:27]\n"
"v_exp_f32 v113, v113\n"
"v_mfma_f32_16x16x32_bf16 a[24:27], a[200:203], v[208:211], a[24:27]\n"
"ds_read_b64_tr_b16 a[192:193], v26 offset:0\n"
"v_cvt_pk_bf16_f32 v164, v112, v113\n"
"v_mfma_f32_16x16x32_bf16 a[20:23], a[200:203], v[200:203], a[20:23]\n"
"v_exp_f32 v114, v114\n"
"v_mfma_f32_16x16x32_bf16 a[20:23], a[204:207], v[204:207], a[20:23]\n"
"buffer_store_dwordx4 a[248:251], v1, s[40:43], s49 offen offset:0 sc1\n"
"v_mfma_f32_16x16x32_bf16 a[16:19], a[204:207], v[196:199], a[16:19]\n"
"v_exp_f32 v115, v115\n"
"v_mfma_f32_16x16x32_bf16 a[16:19], a[200:203], v[192:195], a[16:19]\n"
"ds_read_b64_tr_b16 a[194:195], v26 offset:4096\n"
"v_cvt_pk_bf16_f32 v165, v114, v115\n"
"v_mfma_f32_16x16x32_bf16 a[32:35], a[208:211], v[192:195], a[32:35]\n"
"v_exp_f32 v116, v116\n"
"v_mfma_f32_16x16x32_bf16 a[32:35], a[212:215], v[196:199], a[32:35]\n"
"ds_read_b64_tr_b16 a[196:197], v26 offset:8192\n"
"v_mfma_f32_16x16x32_bf16 a[36:39], a[212:215], v[204:207], a[36:39]\n"
"v_exp_f32 v117, v117\n"
"v_mfma_f32_16x16x32_bf16 a[36:39], a[208:211], v[200:203], a[36:39]\n"
"ds_read_b64_tr_b16 a[198:199], v26 offset:12288\n"
"v_cvt_pk_bf16_f32 v172, v116, v117\n"
"v_mfma_f32_16x16x32_bf16 a[40:43], a[208:211], v[208:211], a[40:43]\n"
"v_exp_f32 v118, v118\n"
"v_mfma_f32_16x16x32_bf16 a[40:43], a[212:215], v[212:215], a[40:43]\n"
"ds_read_b64_tr_b16 a[200:201], v27 offset:0\n"
"v_add_u32 v26, s71, v26\n"
"v_mfma_f32_16x16x32_bf16 a[44:47], a[212:215], v[220:223], a[44:47]\n"
"v_exp_f32 v119, v119\n"
"v_mfma_f32_16x16x32_bf16 a[44:47], a[208:211], v[216:219], a[44:47]\n"
"s_waitcnt lgkmcnt(2)\n"
"ds_read_b64_tr_b16 a[202:203], v27 offset:4096\n"
"v_cvt_pk_bf16_f32 v173, v118, v119\n"
"v_mfma_f32_16x16x32_bf16 a[60:63], a[216:219], v[216:219], a[60:63]\n"
"v_exp_f32 v120, v120\n"
"v_mfma_f32_16x16x32_bf16 a[60:63], a[220:223], v[220:223], a[60:63]\n"
"v_mfma_f32_16x16x32_bf16 a[56:59], a[220:223], v[212:215], a[56:59]\n"
"v_exp_f32 v121, v121\n"
"v_mfma_f32_16x16x32_bf16 a[56:59], a[216:219], v[208:211], a[56:59]\n"
"ds_read_b64_tr_b16 a[204:205], v27 offset:8192\n"
"v_cvt_pk_bf16_f32 v180, v120, v121\n"
"v_mfma_f32_16x16x32_bf16 a[52:55], a[216:219], v[200:203], a[52:55]\n"
"v_exp_f32 v122, v122\n"
"v_mfma_f32_16x16x32_bf16 a[52:55], a[220:223], v[204:207], a[52:55]\n"
"ds_read_b64_tr_b16 a[206:207], v27 offset:12288\n"
"v_add_u32 v27, s71, v27\n"
"v_mfma_f32_16x16x32_bf16 a[48:51], a[220:223], v[196:199], a[48:51]\n"
"v_exp_f32 v123, v123\n"
"v_mfma_f32_16x16x32_bf16 a[48:51], a[216:219], v[192:195], a[48:51]\n"
"ds_read_b64_tr_b16 a[208:209], v28 offset:0\n"
"v_cvt_pk_bf16_f32 v181, v122, v123\n"
"v_mfma_f32_16x16x32_bf16 a[64:67], a[192:195], v[192:195], a[64:67]\n"
"s_waitcnt lgkmcnt(3)\n"
"v_exp_f32 v124, v124\n"
"v_mfma_f32_16x16x32_bf16 a[64:67], a[196:199], v[196:199], a[64:67]\n"
"ds_read_b64_tr_b16 a[210:211], v28 offset:4096\n"
"v_mfma_f32_16x16x32_bf16 a[68:71], a[196:199], v[204:207], a[68:71]\n"
"v_exp_f32 v125, v125\n"
"v_mfma_f32_16x16x32_bf16 a[68:71], a[192:195], v[200:203], a[68:71]\n"
"ds_read_b64_tr_b16 a[212:213], v28 offset:8192\n"
"v_cvt_pk_bf16_f32 v188, v124, v125\n"
"v_mfma_f32_16x16x32_bf16 a[72:75], a[192:195], v[208:211], a[72:75]\n"
"v_exp_f32 v126, v126\n"
"v_mfma_f32_16x16x32_bf16 a[72:75], a[196:199], v[212:215], a[72:75]\n"
"buffer_store_dwordx4 a[252:255], v1, s[40:43], s49 offen offset:1024 sc1\n"
"v_mfma_f32_16x16x32_bf16 a[76:79], a[196:199], v[220:223], a[76:79]\n"
"v_exp_f32 v127, v127\n"
"v_mfma_f32_16x16x32_bf16 a[76:79], a[192:195], v[216:219], a[76:79]\n"
"ds_read_b64_tr_b16 a[214:215], v28 offset:12288\n"
"v_cvt_pk_bf16_f32 v189, v126, v127\n"
"v_mfma_f32_16x16x32_bf16 a[92:95], a[200:203], v[216:219], a[92:95]\n"
"s_waitcnt lgkmcnt(2)\n"
"v_exp_f32 v156, v156\n"
"v_mfma_f32_16x16x32_bf16 a[92:95], a[204:207], v[220:223], a[92:95]\n"
"ds_read_b64_tr_b16 a[216:217], v29 offset:0\n"
"v_add_u32 v28, s71, v28\n"
"v_mfma_f32_16x16x32_bf16 a[88:91], a[204:207], v[212:215], a[88:91]\n"
"v_exp_f32 v157, v157\n"
"v_mfma_f32_16x16x32_bf16 a[88:91], a[200:203], v[208:211], a[88:91]\n"
"ds_read_b64_tr_b16 a[218:219], v29 offset:4096\n"
"v_cvt_pk_bf16_f32 v190, v156, v157\n"
"v_mfma_f32_16x16x32_bf16 a[84:87], a[200:203], v[200:203], a[84:87]\n"
"v_exp_f32 v158, v158\n"
"v_mfma_f32_16x16x32_bf16 a[84:87], a[204:207], v[204:207], a[84:87]\n"
"ds_read_b64_tr_b16 a[220:221], v29 offset:8192\n"
"v_mfma_f32_16x16x32_bf16 a[80:83], a[204:207], v[196:199], a[80:83]\n"
"v_exp_f32 v159, v159\n"
"v_mfma_f32_16x16x32_bf16 a[80:83], a[200:203], v[192:195], a[80:83]\n"
"ds_read_b64_tr_b16 a[222:223], v29 offset:12288\n"
"v_cvt_pk_bf16_f32 v191, v158, v159\n"
"v_mfma_f32_16x16x32_bf16 a[96:99], a[208:211], v[192:195], a[96:99]\n"
"s_waitcnt lgkmcnt(3)\n"
"v_exp_f32 v152, v152\n"
"v_mfma_f32_16x16x32_bf16 a[96:99], a[212:215], v[196:199], a[96:99]\n"
"v_mfma_f32_16x16x32_bf16 a[100:103], a[212:215], v[204:207], a[100:103]\n"
"v_exp_f32 v153, v153\n"
"v_mfma_f32_16x16x32_bf16 a[100:103], a[208:211], v[200:203], a[100:103]\n"
"v_cvt_pk_bf16_f32 v182, v152, v153\n"
"v_add_u32 v29, s71, v29\n"
"v_mfma_f32_16x16x32_bf16 a[104:107], a[208:211], v[208:211], a[104:107]\n"
"v_exp_f32 v154, v154\n"
"v_mfma_f32_16x16x32_bf16 a[104:107], a[212:215], v[212:215], a[104:107]\n"
"s_waitcnt vmcnt(12) lgkmcnt(0)\n"
"s_barrier\n"
"v_mfma_f32_16x16x32_bf16 a[108:111], a[212:215], v[220:223], a[108:111]\n"
"v_exp_f32 v155, v155\n"
"v_mfma_f32_16x16x32_bf16 a[108:111], a[208:211], v[216:219], a[108:111]\n"
"ds_read_b128 a[128:131], v18 offset:0\n"
"v_cvt_pk_bf16_f32 v183, v154, v155\n"
"v_mfma_f32_16x16x32_bf16 a[124:127], a[216:219], v[216:219], a[124:127]\n"
"v_exp_f32 v148, v148\n"
"v_mfma_f32_16x16x32_bf16 a[124:127], a[220:223], v[220:223], a[124:127]\n"
"ds_read_b128 a[132:135], v19 offset:0\n"
"v_mfma_f32_16x16x32_bf16 a[120:123], a[220:223], v[212:215], a[120:123]\n"
"v_exp_f32 v149, v149\n"
"v_mfma_f32_16x16x32_bf16 a[120:123], a[216:219], v[208:211], a[120:123]\n"
"ds_read_b128 a[136:139], v20 offset:0\n"
"v_cvt_pk_bf16_f32 v174, v148, v149\n"
"v_mfma_f32_16x16x32_bf16 a[116:119], a[216:219], v[200:203], a[116:119]\n"
"v_exp_f32 v150, v150\n"
"v_mfma_f32_16x16x32_bf16 a[116:119], a[220:223], v[204:207], a[116:119]\n"
"ds_read_b128 a[140:143], v21 offset:0\n"
"v_mfma_f32_16x16x32_bf16 a[112:115], a[220:223], v[196:199], a[112:115]\n"
"v_exp_f32 v151, v151\n"
"v_mfma_f32_16x16x32_bf16 a[112:115], a[216:219], v[192:195], a[112:115]\n"
"ds_read_b128 a[144:147], v18 offset:4096\n"
"v_cvt_pk_bf16_f32 v175, v150, v151\n"
"v_smfmac_f32_16x16x64_bf16 a[224:227], v[240:243], v[192:199], v244\n"
"ds_read_b128 a[148:151], v19 offset:4096\n"
"v_smfmac_f32_16x16x64_bf16 a[228:231], v[240:243], v[200:207], v244\n"
"ds_read_b128 a[152:155], v20 offset:4096\n"
"v_smfmac_f32_16x16x64_bf16 a[232:235], v[240:243], v[208:215], v244\n"
"ds_read_b128 a[156:159], v21 offset:4096\n"
"v_smfmac_f32_16x16x64_bf16 a[236:239], v[240:243], v[216:223], v244\n"
"s_waitcnt lgkmcnt(4)\n"
"v_mfma_f32_16x16x32_bf16 v[48:51], a[128:131], v[32:35], v[224:227]\n"
"s_mov_b32 m0, s69\n"
"v_exp_f32 v144, v144\n"
"v_mfma_f32_16x16x32_bf16 v[48:51], a[132:135], v[64:67], v[48:51]\n"
"buffer_load_dwordx4 v12, s[32:35], s50 offen offset:0 lds\n"
"v_mfma_f32_16x16x32_bf16 v[48:51], a[136:139], v[96:99], v[48:51]\n"
"s_sub_u32 s70, s68, s67\n"
"v_exp_f32 v145, v145\n"
"v_mfma_f32_16x16x32_bf16 v[48:51], a[140:143], v[128:131], v[48:51]\n"
"s_sub_u32 s71, s67, s69\n"
"ds_read_b128 a[160:163], v18 offset:8192\n"
"v_cvt_pk_bf16_f32 v166, v144, v145\n"
"v_mfma_f32_16x16x32_bf16 v[52:55], a[140:143], v[132:135], v[228:231]\n"
"v_exp_f32 v146, v146\n"
"v_mfma_f32_16x16x32_bf16 v[52:55], a[136:139], v[100:103], v[52:55]\n"
"ds_read_b64_tr_b16 a[240:241], v4 offset:0\n"
"ds_read_b64_tr_b16 a[242:243], v4 offset:2048\n"
"v_mfma_f32_16x16x32_bf16 v[52:55], a[132:135], v[68:71], v[52:55]\n"
"v_exp_f32 v147, v147\n"
"v_mfma_f32_16x16x32_bf16 v[52:55], a[128:131], v[36:39], v[52:55]\n"
"ds_read_b128 a[164:167], v19 offset:8192\n"
"v_cvt_pk_bf16_f32 v167, v146, v147\n"
"v_mfma_f32_16x16x32_bf16 v[56:59], a[128:131], v[40:43], v[232:235]\n"
"v_exp_f32 v48, v48\n"
"v_mfma_f32_16x16x32_bf16 v[56:59], a[132:135], v[72:75], v[56:59]\n"
"buffer_load_dwordx4 v13, s[32:35], s50 offen offset:1024 lds\n"
"v_mfma_f32_16x16x32_bf16 v[56:59], a[136:139], v[104:107], v[56:59]\n"
"s_add_u32 s56, s56, 2048\n"
"v_exp_f32 v49, v49\n"
"v_mfma_f32_16x16x32_bf16 v[56:59], a[140:143], v[136:139], v[56:59]\n"
"ds_read_b128 a[168:171], v20 offset:8192\n"
"v_cvt_pk_bf16_f32 v192, v48, v49\n"
"v_mfma_f32_16x16x32_bf16 v[60:63], a[140:143], v[140:143], v[236:239]\n"
"s_add_u32 s49, s49, 2048\n"
"v_exp_f32 v50, v50\n"
"v_mfma_f32_16x16x32_bf16 v[60:63], a[136:139], v[108:111], v[60:63]\n"
"ds_read_b64_tr_b16 a[244:245], v5 offset:0\n"
"ds_read_b64_tr_b16 a[246:247], v5 offset:2048\n"
"v_mfma_f32_16x16x32_bf16 v[60:63], a[132:135], v[76:79], v[60:63]\n"
"v_exp_f32 v51, v51\n"
"v_mfma_f32_16x16x32_bf16 v[60:63], a[128:131], v[44:47], v[60:63]\n"
"s_waitcnt lgkmcnt(3)\n"
"ds_read_b128 a[172:175], v21 offset:8192\n"
"v_cvt_pk_bf16_f32 v193, v50, v51\n"
"v_mfma_f32_16x16x32_bf16 v[92:95], a[144:147], v[44:47], v[236:239]\n"
"v_exp_f32 v52, v52\n"
"v_mfma_f32_16x16x32_bf16 v[92:95], a[148:151], v[76:79], v[92:95]\n"
"buffer_load_dwordx4 v14, s[32:35], s50 offen offset:2048 lds\n"
"v_mfma_f32_16x16x32_bf16 v[92:95], a[152:155], v[108:111], v[92:95]\n"
"v_exp_f32 v53, v53\n"
"v_mfma_f32_16x16x32_bf16 v[92:95], a[156:159], v[140:143], v[92:95]\n"
"ds_read_b128 a[176:179], v18 offset:12288\n"
"v_cvt_pk_bf16_f32 v200, v52, v53\n"
"v_mfma_f32_16x16x32_bf16 v[88:91], a[156:159], v[136:139], v[232:235]\n"
"v_exp_f32 v54, v54\n"
"v_mfma_f32_16x16x32_bf16 v[88:91], a[152:155], v[104:107], v[88:91]\n"
"ds_read_b64_tr_b16 a[248:249], v6 offset:0\n"
"ds_read_b64_tr_b16 a[250:251], v6 offset:2048\n"
"v_mfma_f32_16x16x32_bf16 v[88:91], a[148:151], v[72:75], v[88:91]\n"
"v_exp_f32 v55, v55\n"
"v_mfma_f32_16x16x32_bf16 v[88:91], a[144:147], v[40:43], v[88:91]\n"
"ds_read_b128 a[180:183], v19 offset:12288\n"
"v_cvt_pk_bf16_f32 v201, v54, v55\n"
"v_mfma_f32_16x16x32_bf16 v[84:87], a[144:147], v[36:39], v[228:231]\n"
"v_exp_f32 v56, v56\n"
"v_mfma_f32_16x16x32_bf16 v[84:87], a[148:151], v[68:71], v[84:87]\n"
"buffer_load_dwordx4 v15, s[32:35], s50 offen offset:3072 lds\n"
"v_mfma_f32_16x16x32_bf16 v[84:87], a[152:155], v[100:103], v[84:87]\n"
"s_add_u32 s50, s50, 16384\n"
"v_exp_f32 v57, v57\n"
"v_mfma_f32_16x16x32_bf16 v[84:87], a[156:159], v[132:135], v[84:87]\n"
"ds_read_b128 a[184:187], v20 offset:12288\n"
"v_cvt_pk_bf16_f32 v208, v56, v57\n"
"v_mfma_f32_16x16x32_bf16 v[80:83], a[156:159], v[128:131], v[224:227]\n"
"v_exp_f32 v58, v58\n"
"v_mfma_f32_16x16x32_bf16 v[80:83], a[152:155], v[96:99], v[80:83]\n"
"ds_read_b64_tr_b16 a[252:253], v7 offset:0\n"
"ds_read_b64_tr_b16 a[254:255], v7 offset:2048\n"
"v_mfma_f32_16x16x32_bf16 v[80:83], a[148:151], v[64:67], v[80:83]\n"
"v_exp_f32 v59, v59\n"
"v_mfma_f32_16x16x32_bf16 v[80:83], a[144:147], v[32:35], v[80:83]\n"
"ds_read_b128 a[188:191], v21 offset:12288\n"
"v_cvt_pk_bf16_f32 v209, v58, v59\n"
"v_mfma_f32_16x16x32_bf16 v[112:115], a[160:163], v[32:35], v[224:227]\n"
"v_exp_f32 v60, v60\n"
"v_mfma_f32_16x16x32_bf16 v[112:115], a[164:167], v[64:67], v[112:115]\n"
"s_waitcnt lgkmcnt(3)\n"
"v_add_u32 v18, s70, v18\n"
"v_add_u32 v19, s70, v19\n"
"v_mfma_f32_16x16x32_bf16 v[112:115], a[168:171], v[96:99], v[112:115]\n"
"v_exp_f32 v61, v61\n"
"v_mfma_f32_16x16x32_bf16 v[112:115], a[172:175], v[128:131], v[112:115]\n"
"ds_read_b64_tr_b16 a[192:193], v22 offset:0\n"
"v_cvt_pk_bf16_f32 v216, v60, v61\n"
"v_mfma_f32_16x16x32_bf16 v[116:119], a[172:175], v[132:135], v[228:231]\n"
"s_add_u32 m0, s68, 49152\n"
"v_exp_f32 v62, v62\n"
"v_mfma_f32_16x16x32_bf16 v[116:119], a[168:171], v[100:103], v[116:119]\n"
"buffer_load_dwordx4 v16, s[36:39], s51 offen offset:0 lds\n"
"v_mfma_f32_16x16x32_bf16 v[116:119], a[164:167], v[68:71], v[116:119]\n"
"v_exp_f32 v63, v63\n"
"v_mfma_f32_16x16x32_bf16 v[116:119], a[160:163], v[36:39], v[116:119]\n"
"s_mov_b32 s10, s67\n"
"ds_read_b64_tr_b16 a[194:195], v22 offset:4096\n"
"v_cvt_pk_bf16_f32 v217, v62, v63\n"
"v_mfma_f32_16x16x32_bf16 v[120:123], a[160:163], v[40:43], v[232:235]\n"
"s_mov_b32 s67, s68\n"
"v_exp_f32 v92, v92\n"
"v_mfma_f32_16x16x32_bf16 v[120:123], a[164:167], v[72:75], v[120:123]\n"
"s_mov_b32 s68, s69\n"
"ds_read_b64_tr_b16 a[196:197], v22 offset:8192\n"
"v_add_u32 v20, s70, v20\n"
"v_mfma_f32_16x16x32_bf16 v[120:123], a[168:171], v[104:107], v[120:123]\n"
"s_mov_b32 s69, s10\n"
"v_exp_f32 v93, v93\n"
"v_mfma_f32_16x16x32_bf16 v[120:123], a[172:175], v[136:139], v[120:123]\n"
"ds_read_b64_tr_b16 a[198:199], v22 offset:12288\n"
"v_cvt_pk_bf16_f32 v218, v92, v93\n"
"v_mfma_f32_16x16x32_bf16 v[124:127], a[172:175], v[140:143], v[236:239]\n"
"v_exp_f32 v94, v94\n"
"v_mfma_f32_16x16x32_bf16 v[124:127], a[168:171], v[108:111], v[124:127]\n"
"buffer_load_dwordx4 v17, s[36:39], s51 offen offset:1024 lds\n"
"v_mfma_f32_16x16x32_bf16 v[124:127], a[164:167], v[76:79], v[124:127]\n"
"v_exp_f32 v95, v95\n"
"v_mfma_f32_16x16x32_bf16 v[124:127], a[160:163], v[44:47], v[124:127]\n"
"ds_read_b64_tr_b16 a[200:201], v23 offset:0\n"
"v_cvt_pk_bf16_f32 v219, v94, v95\n"
"v_mfma_f32_16x16x32_bf16 v[156:159], a[176:179], v[44:47], v[236:239]\n"
"v_exp_f32 v88, v88\n"
"v_mfma_f32_16x16x32_bf16 v[156:159], a[180:183], v[76:79], v[156:159]\n"
"ds_read_b64_tr_b16 a[202:203], v23 offset:4096\n"
"v_add_u32 v22, s71, v22\n"
"v_mfma_f32_16x16x32_bf16 v[156:159], a[184:187], v[108:111], v[156:159]\n"
"s_waitcnt lgkmcnt(2)\n"
"v_exp_f32 v89, v89\n"
"v_mfma_f32_16x16x32_bf16 v[156:159], a[188:191], v[140:143], v[156:159]\n"
"ds_read_b64_tr_b16 a[204:205], v23 offset:8192\n"
"v_cvt_pk_bf16_f32 v210, v88, v89\n"
"v_mfma_f32_16x16x32_bf16 v[152:155], a[188:191], v[136:139], v[232:235]\n"
"v_exp_f32 v90, v90\n"
"v_mfma_f32_16x16x32_bf16 v[152:155], a[184:187], v[104:107], v[152:155]\n"
"ds_read_b64_tr_b16 a[206:207], v23 offset:12288\n"
"v_add_u32 v23, s71, v23\n"
"v_mfma_f32_16x16x32_bf16 v[152:155], a[180:183], v[72:75], v[152:155]\n"
"v_exp_f32 v91, v91\n"
"v_mfma_f32_16x16x32_bf16 v[152:155], a[176:179], v[40:43], v[152:155]\n"
"ds_read_b64_tr_b16 a[208:209], v24 offset:0\n"
"v_cvt_pk_bf16_f32 v211, v90, v91\n"
"v_mfma_f32_16x16x32_bf16 v[148:151], a[176:179], v[36:39], v[228:231]\n"
"v_exp_f32 v84, v84\n"
"v_mfma_f32_16x16x32_bf16 v[148:151], a[180:183], v[68:71], v[148:151]\n"
"buffer_load_dwordx4 v16, s[36:39], s51 offen offset:2048 lds\n"
"v_mfma_f32_16x16x32_bf16 v[148:151], a[184:187], v[100:103], v[148:151]\n"
"v_exp_f32 v85, v85\n"
"v_mfma_f32_16x16x32_bf16 v[148:151], a[188:191], v[132:135], v[148:151]\n"
"ds_read_b64_tr_b16 a[210:211], v24 offset:4096\n"
"v_cvt_pk_bf16_f32 v202, v84, v85\n"
"v_mfma_f32_16x16x32_bf16 v[144:147], a[188:191], v[128:131], v[224:227]\n"
"v_exp_f32 v86, v86\n"
"v_mfma_f32_16x16x32_bf16 v[144:147], a[184:187], v[96:99], v[144:147]\n"
"ds_read_b64_tr_b16 a[212:213], v24 offset:8192\n"
"v_add_u32 v21, s70, v21\n"
"v_mfma_f32_16x16x32_bf16 v[144:147], a[180:183], v[64:67], v[144:147]\n"
"v_exp_f32 v87, v87\n"
"v_mfma_f32_16x16x32_bf16 v[144:147], a[176:179], v[32:35], v[144:147]\n"
"ds_read_b64_tr_b16 a[214:215], v24 offset:12288\n"
"v_cvt_pk_bf16_f32 v203, v86, v87\n"
"v_mfma_f32_16x16x32_bf16 a[0:3], a[192:195], v[160:163], a[0:3]\n"
"v_exp_f32 v80, v80\n"
"v_mfma_f32_16x16x32_bf16 a[0:3], a[196:199], v[164:167], a[0:3]\n"
"buffer_load_dwordx4 v17, s[36:39], s51 offen offset:3072 lds\n"
"v_mfma_f32_16x16x32_bf16 a[4:7], a[196:199], v[172:175], a[4:7]\n"
"s_add_u32 s51, s51, 16384\n"
"v_exp_f32 v81, v81\n"
"v_mfma_f32_16x16x32_bf16 a[4:7], a[192:195], v[168:171], a[4:7]\n"
"ds_read_b64_tr_b16 a[216:217], v25 offset:0\n"
"v_cvt_pk_bf16_f32 v194, v80, v81\n"
"v_mfma_f32_16x16x32_bf16 a[8:11], a[192:195], v[176:179], a[8:11]\n"
"v_exp_f32 v82, v82\n"
"v_mfma_f32_16x16x32_bf16 a[8:11], a[196:199], v[180:183], a[8:11]\n"
"ds_read_b64_tr_b16 a[218:219], v25 offset:4096\n"
"v_add_u32 v24, s71, v24\n"
"v_mfma_f32_16x16x32_bf16 a[12:15], a[196:199], v[188:191], a[12:15]\n"
"v_exp_f32 v83, v83\n"
"v_mfma_f32_16x16x32_bf16 a[12:15], a[192:195], v[184:187], a[12:15]\n"
"s_waitcnt lgkmcnt(2)\n"
"ds_read_b64_tr_b16 a[220:221], v25 offset:8192\n"
"v_cvt_pk_bf16_f32 v195, v82, v83\n"
"v_mfma_f32_16x16x32_bf16 a[28:31], a[200:203], v[184:187], a[28:31]\n"
"v_exp_f32 v112, v112\n"
"v_mfma_f32_16x16x32_bf16 a[28:31], a[204:207], v[188:191], a[28:31]\n"
"ds_read_b64_tr_b16 a[222:223], v25 offset:12288\n"
"v_add_u32 v25, s71, v25\n"
"v_mfma_f32_16x16x32_bf16 a[24:27], a[204:207], v[180:183], a[24:27]\n"
"v_exp_f32 v113, v113\n"
"v_mfma_f32_16x16x32_bf16 a[24:27], a[200:203], v[176:179], a[24:27]\n"
"ds_read_b64_tr_b16 a[192:193], v26 offset:0\n"
"v_cvt_pk_bf16_f32 v196, v112, v113\n"
"v_mfma_f32_16x16x32_bf16 a[20:23], a[200:203], v[168:171], a[20:23]\n"
"v_exp_f32 v114, v114\n"
"v_mfma_f32_16x16x32_bf16 a[20:23], a[204:207], v[172:175], a[20:23]\n"
"buffer_store_dwordx4 a[240:243], v1, s[12:15], s84 offen sc1\n"
"v_mfma_f32_16x16x32_bf16 a[16:19], a[204:207], v[164:167], a[16:19]\n"
"s_add_u32 s84, s84, s85\n"
"v_exp_f32 v115, v115\n"
"v_mfma_f32_16x16x32_bf16 a[16:19], a[200:203], v[160:163], a[16:19]\n"
"ds_read_b64_tr_b16 a[194:195], v26 offset:4096\n"
"v_cvt_pk_bf16_f32 v197, v114, v115\n"
"v_mfma_f32_16x16x32_bf16 a[32:35], a[208:211], v[160:163], a[32:35]\n"
"v_exp_f32 v116, v116\n"
"v_mfma_f32_16x16x32_bf16 a[32:35], a[212:215], v[164:167], a[32:35]\n"
"ds_read_b64_tr_b16 a[196:197], v26 offset:8192\n"
"v_mfma_f32_16x16x32_bf16 a[36:39], a[212:215], v[172:175], a[36:39]\n"
"v_exp_f32 v117, v117\n"
"v_mfma_f32_16x16x32_bf16 a[36:39], a[208:211], v[168:171], a[36:39]\n"
"ds_read_b64_tr_b16 a[198:199], v26 offset:12288\n"
"v_cvt_pk_bf16_f32 v204, v116, v117\n"
"v_mfma_f32_16x16x32_bf16 a[40:43], a[208:211], v[176:179], a[40:43]\n"
"v_exp_f32 v118, v118\n"
"v_mfma_f32_16x16x32_bf16 a[40:43], a[212:215], v[180:183], a[40:43]\n"
"ds_read_b64_tr_b16 a[200:201], v27 offset:0\n"
"v_add_u32 v26, s71, v26\n"
"v_mfma_f32_16x16x32_bf16 a[44:47], a[212:215], v[188:191], a[44:47]\n"
"v_exp_f32 v119, v119\n"
"v_mfma_f32_16x16x32_bf16 a[44:47], a[208:211], v[184:187], a[44:47]\n"
"s_waitcnt lgkmcnt(2)\n"
"ds_read_b64_tr_b16 a[202:203], v27 offset:4096\n"
"v_cvt_pk_bf16_f32 v205, v118, v119\n"
"v_mfma_f32_16x16x32_bf16 a[60:63], a[216:219], v[184:187], a[60:63]\n"
"v_exp_f32 v120, v120\n"
"v_mfma_f32_16x16x32_bf16 a[60:63], a[220:223], v[188:191], a[60:63]\n"
"buffer_store_dwordx4 a[244:247], v1, s[12:15], s84 offen sc1\n"
"v_mfma_f32_16x16x32_bf16 a[56:59], a[220:223], v[180:183], a[56:59]\n"
"s_add_u32 s84, s84, s85\n"
"v_exp_f32 v121, v121\n"
"v_mfma_f32_16x16x32_bf16 a[56:59], a[216:219], v[176:179], a[56:59]\n"
"ds_read_b64_tr_b16 a[204:205], v27 offset:8192\n"
"v_cvt_pk_bf16_f32 v212, v120, v121\n"
"v_mfma_f32_16x16x32_bf16 a[52:55], a[216:219], v[168:171], a[52:55]\n"
"v_exp_f32 v122, v122\n"
"v_mfma_f32_16x16x32_bf16 a[52:55], a[220:223], v[172:175], a[52:55]\n"
"ds_read_b64_tr_b16 a[206:207], v27 offset:12288\n"
"v_add_u32 v27, s71, v27\n"
"v_mfma_f32_16x16x32_bf16 a[48:51], a[220:223], v[164:167], a[48:51]\n"
"v_exp_f32 v123, v123\n"
"v_mfma_f32_16x16x32_bf16 a[48:51], a[216:219], v[160:163], a[48:51]\n"
"ds_read_b64_tr_b16 a[208:209], v28 offset:0\n"
"v_cvt_pk_bf16_f32 v213, v122, v123\n"
"v_mfma_f32_16x16x32_bf16 a[64:67], a[192:195], v[160:163], a[64:67]\n"
"s_waitcnt lgkmcnt(3)\n"
"v_exp_f32 v124, v124\n"
"v_mfma_f32_16x16x32_bf16 a[64:67], a[196:199], v[164:167], a[64:67]\n"
"ds_read_b64_tr_b16 a[210:211], v28 offset:4096\n"
"v_mfma_f32_16x16x32_bf16 a[68:71], a[196:199], v[172:175], a[68:71]\n"
"v_exp_f32 v125, v125\n"
"v_mfma_f32_16x16x32_bf16 a[68:71], a[192:195], v[168:171], a[68:71]\n"
"ds_read_b64_tr_b16 a[212:213], v28 offset:8192\n"
"v_cvt_pk_bf16_f32 v220, v124, v125\n"
"v_mfma_f32_16x16x32_bf16 a[72:75], a[192:195], v[176:179], a[72:75]\n"
"v_exp_f32 v126, v126\n"
"v_mfma_f32_16x16x32_bf16 a[72:75], a[196:199], v[180:183], a[72:75]\n"
"buffer_store_dwordx4 a[248:251], v1, s[12:15], s84 offen sc1\n"
"v_mfma_f32_16x16x32_bf16 a[76:79], a[196:199], v[188:191], a[76:79]\n"
"s_add_u32 s84, s84, s85\n"
"v_exp_f32 v127, v127\n"
"v_mfma_f32_16x16x32_bf16 a[76:79], a[192:195], v[184:187], a[76:79]\n"
"ds_read_b64_tr_b16 a[214:215], v28 offset:12288\n"
"v_cvt_pk_bf16_f32 v221, v126, v127\n"
"v_mfma_f32_16x16x32_bf16 a[92:95], a[200:203], v[184:187], a[92:95]\n"
"s_waitcnt lgkmcnt(2)\n"
"v_exp_f32 v156, v156\n"
"v_mfma_f32_16x16x32_bf16 a[92:95], a[204:207], v[188:191], a[92:95]\n"
"ds_read_b64_tr_b16 a[216:217], v29 offset:0\n"
"v_add_u32 v28, s71, v28\n"
"v_mfma_f32_16x16x32_bf16 a[88:91], a[204:207], v[180:183], a[88:91]\n"
"v_exp_f32 v157, v157\n"
"v_mfma_f32_16x16x32_bf16 a[88:91], a[200:203], v[176:179], a[88:91]\n"
"ds_read_b64_tr_b16 a[218:219], v29 offset:4096\n"
"v_cvt_pk_bf16_f32 v222, v156, v157\n"
"v_mfma_f32_16x16x32_bf16 a[84:87], a[200:203], v[168:171], a[84:87]\n"
"v_exp_f32 v158, v158\n"
"v_mfma_f32_16x16x32_bf16 a[84:87], a[204:207], v[172:175], a[84:87]\n"
"ds_read_b64_tr_b16 a[220:221], v29 offset:8192\n"
"v_mfma_f32_16x16x32_bf16 a[80:83], a[204:207], v[164:167], a[80:83]\n"
"v_exp_f32 v159, v159\n"
"v_mfma_f32_16x16x32_bf16 a[80:83], a[200:203], v[160:163], a[80:83]\n"
"ds_read_b64_tr_b16 a[222:223], v29 offset:12288\n"
"v_cvt_pk_bf16_f32 v223, v158, v159\n"
"v_mfma_f32_16x16x32_bf16 a[96:99], a[208:211], v[160:163], a[96:99]\n"
"s_waitcnt lgkmcnt(3)\n"
"v_exp_f32 v152, v152\n"
"v_mfma_f32_16x16x32_bf16 a[96:99], a[212:215], v[164:167], a[96:99]\n"
"buffer_store_dwordx4 a[252:255], v1, s[12:15], s84 offen sc1\n"
"v_mfma_f32_16x16x32_bf16 a[100:103], a[212:215], v[172:175], a[100:103]\n"
"v_exp_f32 v153, v153\n"
"v_mfma_f32_16x16x32_bf16 a[100:103], a[208:211], v[168:171], a[100:103]\n"
"v_cvt_pk_bf16_f32 v214, v152, v153\n"
"v_add_u32 v29, s71, v29\n"
"v_mfma_f32_16x16x32_bf16 a[104:107], a[208:211], v[176:179], a[104:107]\n"
"v_exp_f32 v154, v154\n"
"v_mfma_f32_16x16x32_bf16 a[104:107], a[212:215], v[180:183], a[104:107]\n"
"s_waitcnt vmcnt(14) lgkmcnt(0)\n"
"s_barrier\n"
"v_mfma_f32_16x16x32_bf16 a[108:111], a[212:215], v[188:191], a[108:111]\n"
"v_exp_f32 v155, v155\n"
"v_mfma_f32_16x16x32_bf16 a[108:111], a[208:211], v[184:187], a[108:111]\n"
"ds_read_b128 a[128:131], v18 offset:0\n"
"v_cvt_pk_bf16_f32 v215, v154, v155\n"
"v_mfma_f32_16x16x32_bf16 a[124:127], a[216:219], v[184:187], a[124:127]\n"
"v_exp_f32 v148, v148\n"
"v_mfma_f32_16x16x32_bf16 a[124:127], a[220:223], v[188:191], a[124:127]\n"
"ds_read_b128 a[132:135], v19 offset:0\n"
"v_mfma_f32_16x16x32_bf16 a[120:123], a[220:223], v[180:183], a[120:123]\n"
"v_exp_f32 v149, v149\n"
"v_mfma_f32_16x16x32_bf16 a[120:123], a[216:219], v[176:179], a[120:123]\n"
"ds_read_b128 a[136:139], v20 offset:0\n"
"v_cvt_pk_bf16_f32 v206, v148, v149\n"
"v_mfma_f32_16x16x32_bf16 a[116:119], a[216:219], v[168:171], a[116:119]\n"
"v_exp_f32 v150, v150\n"
"v_mfma_f32_16x16x32_bf16 a[116:119], a[220:223], v[172:175], a[116:119]\n"
"ds_read_b128 a[140:143], v21 offset:0\n"
"v_mfma_f32_16x16x32_bf16 a[112:115], a[220:223], v[164:167], a[112:115]\n"
"v_exp_f32 v151, v151\n"
"v_mfma_f32_16x16x32_bf16 a[112:115], a[216:219], v[160:163], a[112:115]\n"
"ds_read_b128 a[144:147], v18 offset:4096\n"
"v_cvt_pk_bf16_f32 v207, v150, v151\n"
"v_smfmac_f32_16x16x64_bf16 a[224:227], v[240:243], v[160:167], v244\n"
"ds_read_b128 a[148:151], v19 offset:4096\n"
"v_smfmac_f32_16x16x64_bf16 a[228:231], v[240:243], v[168:175], v244\n"
"s_cmp_ge_u32 s50, 294912\n"
"ds_read_b128 a[152:155], v20 offset:4096\n"
"v_smfmac_f32_16x16x64_bf16 a[232:235], v[240:243], v[176:183], v244\n"
"ds_read_b128 a[156:159], v21 offset:4096\n"
"v_smfmac_f32_16x16x64_bf16 a[236:239], v[240:243], v[184:191], v244\n"
"s_waitcnt lgkmcnt(4)\n"
"s_cbranch_scc1 .LATT_XY\n"
"v_mfma_f32_16x16x32_bf16 v[48:51], a[128:131], v[32:35], v[224:227]\n"
"s_mov_b32 m0, s69\n"
"v_exp_f32 v144, v144\n"
"v_mfma_f32_16x16x32_bf16 v[48:51], a[132:135], v[64:67], v[48:51]\n"
"buffer_load_dwordx4 v12, s[32:35], s50 offen offset:0 lds\n"
"v_mfma_f32_16x16x32_bf16 v[48:51], a[136:139], v[96:99], v[48:51]\n"
"s_sub_u32 s70, s68, s67\n"
"v_exp_f32 v145, v145\n"
"v_mfma_f32_16x16x32_bf16 v[48:51], a[140:143], v[128:131], v[48:51]\n"
"s_sub_u32 s71, s67, s69\n"
"ds_read_b128 a[160:163], v18 offset:8192\n"
"v_cvt_pk_bf16_f32 v198, v144, v145\n"
"v_mfma_f32_16x16x32_bf16 v[52:55], a[140:143], v[132:135], v[228:231]\n"
"s_mul_i32 s52, s8, 7282\n"
"v_exp_f32 v146, v146\n"
"v_mfma_f32_16x16x32_bf16 v[52:55], a[136:139], v[100:103], v[52:55]\n"
"s_lshr_b32 s52, s52, 16\n"
"ds_read_b128 a[240:243], v3 offset:4096\n"
"v_mfma_f32_16x16x32_bf16 v[52:55], a[132:135], v[68:71], v[52:55]\n"
"s_mul_i32 s54, s52, 9\n"
"v_exp_f32 v147, v147\n"
"v_mfma_f32_16x16x32_bf16 v[52:55], a[128:131], v[36:39], v[52:55]\n"
"s_sub_u32 s53, s8, s54\n"
"ds_read_b128 a[164:167], v19 offset:8192\n"
"v_cvt_pk_bf16_f32 v199, v146, v147\n"
"v_mfma_f32_16x16x32_bf16 v[56:59], a[128:131], v[40:43], v[232:235]\n"
"s_mul_i32 s54, s58, 36\n"
"v_exp_f32 v48, v48\n"
"v_mfma_f32_16x16x32_bf16 v[56:59], a[132:135], v[72:75], v[56:59]\n"
"s_lshl_b32 s55, s58, 5\n"
"buffer_load_dwordx4 v13, s[32:35], s50 offen offset:1024 lds\n"
"v_mfma_f32_16x16x32_bf16 v[56:59], a[136:139], v[104:107], v[56:59]\n"
"s_sub_u32 s55, s55, 36\n"
"v_exp_f32 v49, v49\n"
"v_mfma_f32_16x16x32_bf16 v[56:59], a[140:143], v[136:139], v[56:59]\n"
"s_cmp_lt_u32 s52, 36\n"
"ds_read_b128 a[168:171], v20 offset:8192\n"
"v_cvt_pk_bf16_f32 v160, v48, v49\n"
"v_mfma_f32_16x16x32_bf16 v[60:63], a[140:143], v[140:143], v[236:239]\n"
"s_cselect_b32 s54, s54, s55\n"
"v_exp_f32 v50, v50\n"
"v_mfma_f32_16x16x32_bf16 v[60:63], a[136:139], v[108:111], v[60:63]\n"
"s_cselect_b64 s[44:45], s[76:77], s[72:73]\n"
"ds_read_b128 a[244:247], v3 offset:5120\n"
"v_mfma_f32_16x16x32_bf16 v[60:63], a[132:135], v[76:79], v[60:63]\n"
"s_add_u32 s54, s54, s52\n"
"v_exp_f32 v51, v51\n"
"v_mfma_f32_16x16x32_bf16 v[60:63], a[128:131], v[44:47], v[60:63]\n"
"s_waitcnt lgkmcnt(2)\n"
"ds_read_b128 a[172:175], v21 offset:8192\n"
"v_cvt_pk_bf16_f32 v161, v50, v51\n"
"v_mfma_f32_16x16x32_bf16 v[92:95], a[144:147], v[44:47], v[236:239]\n"
"s_mul_i32 s54, s54, 36864\n"
"v_exp_f32 v52, v52\n"
"v_mfma_f32_16x16x32_bf16 v[92:95], a[148:151], v[76:79], v[92:95]\n"
"s_lshl_b32 s55, s53, 7\n"
"buffer_load_dwordx4 v14, s[32:35], s50 offen offset:2048 lds\n"
"v_mfma_f32_16x16x32_bf16 v[92:95], a[152:155], v[108:111], v[92:95]\n"
"s_add_u32 s54, s54, s55\n"
"v_exp_f32 v53, v53\n"
"v_mfma_f32_16x16x32_bf16 v[92:95], a[156:159], v[140:143], v[92:95]\n"
"s_add_u32 s44, s44, s54\n"
"ds_read_b128 a[176:179], v18 offset:12288\n"
"v_cvt_pk_bf16_f32 v168, v52, v53\n"
"v_mfma_f32_16x16x32_bf16 v[88:91], a[156:159], v[136:139], v[232:235]\n"
"s_addc_u32 s45, s45, 0\n"
"v_exp_f32 v54, v54\n"
"v_mfma_f32_16x16x32_bf16 v[88:91], a[152:155], v[104:107], v[88:91]\n"
"s_cmp_lt_u32 s8, 612\n"
"ds_read_b128 a[248:251], v3 offset:6144\n"
"v_mfma_f32_16x16x32_bf16 v[88:91], a[148:151], v[72:75], v[88:91]\n"
"s_cselect_b32 s46, 35840, 0\n"
"v_exp_f32 v55, v55\n"
"v_mfma_f32_16x16x32_bf16 v[88:91], a[144:147], v[40:43], v[88:91]\n"
"s_cselect_b32 s14, 2506752, 0\n"
"ds_read_b128 a[180:183], v19 offset:12288\n"
"v_cvt_pk_bf16_f32 v169, v54, v55\n"
"v_mfma_f32_16x16x32_bf16 v[84:87], a[144:147], v[36:39], v[228:231]\n"
"s_lshr_b32 s54, s53, 1\n"
"v_exp_f32 v56, v56\n"
"v_mfma_f32_16x16x32_bf16 v[84:87], a[148:151], v[68:71], v[84:87]\n"
"s_mul_i32 s54, s54, 626688\n"
"buffer_load_dwordx4 v15, s[32:35], s50 offen offset:3072 lds\n"
"v_mfma_f32_16x16x32_bf16 v[84:87], a[152:155], v[100:103], v[84:87]\n"
"s_add_u32 s50, s50, 16384\n"
"v_exp_f32 v57, v57\n"
"v_mfma_f32_16x16x32_bf16 v[84:87], a[156:159], v[132:135], v[84:87]\n"
".LPAD7:\n"
"s_cbranch_execz .LPAD10\n"
"s_and_b32 s55, s53, 1\n"
"ds_read_b128 a[184:187], v20 offset:12288\n"
"v_cvt_pk_bf16_f32 v176, v56, v57\n"
"v_mfma_f32_16x16x32_bf16 v[80:83], a[156:159], v[128:131], v[224:227]\n"
"s_lshl_b32 s55, s55, 12\n"
"v_exp_f32 v58, v58\n"
"v_mfma_f32_16x16x32_bf16 v[80:83], a[152:155], v[96:99], v[80:83]\n"
"s_add_u32 s54, s54, s55\n"
"ds_read_b128 a[252:255], v3 offset:7168\n"
"v_add_u32 v3, 4096, v3\n"
"v_mfma_f32_16x16x32_bf16 v[80:83], a[148:151], v[64:67], v[80:83]\n"
"s_cmp_lt_u32 s53, 8\n"
"v_exp_f32 v59, v59\n"
"v_mfma_f32_16x16x32_bf16 v[80:83], a[144:147], v[32:35], v[80:83]\n"
"s_cselect_b32 s54, s54, 8192\n"
"ds_read_b128 a[188:191], v21 offset:12288\n"
"v_cvt_pk_bf16_f32 v177, v58, v59\n"
"v_mfma_f32_16x16x32_bf16 v[112:115], a[160:163], v[32:35], v[224:227]\n"
"s_cselect_b32 s85, 1, 612\n"
"v_exp_f32 v60, v60\n"
"v_mfma_f32_16x16x32_bf16 v[112:115], a[164:167], v[64:67], v[112:115]\n"
"s_waitcnt lgkmcnt(2)\n"
"v_add_u32 v18, s70, v18\n"
"v_add_u32 v19, s70, v19\n"
"v_mfma_f32_16x16x32_bf16 v[112:115], a[168:171], v[96:99], v[112:115]\n"
"s_lshl_b32 s85, s85, 10\n"
"v_exp_f32 v61, v61\n"
"v_mfma_f32_16x16x32_bf16 v[112:115], a[172:175], v[128:131], v[112:115]\n"
"s_mul_i32 s52, s52, 9216\n"
"ds_read_b64_tr_b16 a[192:193], v22 offset:0\n"
"v_cvt_pk_bf16_f32 v184, v60, v61\n"
"v_mfma_f32_16x16x32_bf16 v[116:119], a[172:175], v[132:135], v[228:231]\n"
"s_add_u32 m0, s68, 49152\n"
"v_exp_f32 v62, v62\n"
"v_mfma_f32_16x16x32_bf16 v[116:119], a[168:171], v[100:103], v[116:119]\n"
"s_add_u32 s84, s52, s54\n"
"buffer_load_dwordx4 v16, s[36:39], s51 offen offset:0 lds\n"
"v_mfma_f32_16x16x32_bf16 v[116:119], a[164:167], v[68:71], v[116:119]\n"
"s_add_u32 s8, s8, 256\n"
"v_exp_f32 v63, v63\n"
"v_mfma_f32_16x16x32_bf16 v[116:119], a[160:163], v[36:39], v[116:119]\n"
"s_mov_b32 s10, s67\n"
"ds_read_b64_tr_b16 a[194:195], v22 offset:4096\n"
"v_cvt_pk_bf16_f32 v185, v62, v63\n"
"v_mfma_f32_16x16x32_bf16 v[120:123], a[160:163], v[40:43], v[232:235]\n"
"s_mov_b32 s67, s68\n"
"v_exp_f32 v92, v92\n"
"v_mfma_f32_16x16x32_bf16 v[120:123], a[164:167], v[72:75], v[120:123]\n"
"s_mov_b32 s68, s69\n"
"ds_read_b64_tr_b16 a[196:197], v22 offset:8192\n"
"v_add_u32 v20, s70, v20\n"
"v_mfma_f32_16x16x32_bf16 v[120:123], a[168:171], v[104:107], v[120:123]\n"
"s_mov_b32 s69, s10\n"
"v_exp_f32 v93, v93\n"
"v_mfma_f32_16x16x32_bf16 v[120:123], a[172:175], v[136:139], v[120:123]\n"
"ds_read_b64_tr_b16 a[198:199], v22 offset:12288\n"
"v_cvt_pk_bf16_f32 v186, v92, v93\n"
"v_mfma_f32_16x16x32_bf16 v[124:127], a[172:175], v[140:143], v[236:239]\n"
"v_exp_f32 v94, v94\n"
"v_mfma_f32_16x16x32_bf16 v[124:127], a[168:171], v[108:111], v[124:127]\n"
"buffer_load_dwordx4 v17, s[36:39], s51 offen offset:1024 lds\n"
"v_mfma_f32_16x16x32_bf16 v[124:127], a[164:167], v[76:79], v[124:127]\n"
"v_exp_f32 v95, v95\n"
"v_mfma_f32_16x16x32_bf16 v[124:127], a[160:163], v[44:47], v[124:127]\n"
"ds_read_b64_tr_b16 a[200:201], v23 offset:0\n"
"v_cvt_pk_bf16_f32 v187, v94, v95\n"
"v_mfma_f32_16x16x32_bf16 v[156:159], a[176:179], v[44:47], v[236:239]\n"
"v_exp_f32 v88, v88\n"
"v_mfma_f32_16x16x32_bf16 v[156:159], a[180:183], v[76:79], v[156:159]\n"
"ds_read_b64_tr_b16 a[202:203], v23 offset:4096\n"
"v_add_u32 v22, s71, v22\n"
"v_mfma_f32_16x16x32_bf16 v[156:159], a[184:187], v[108:111], v[156:159]\n"
"s_waitcnt lgkmcnt(2)\n"
"v_exp_f32 v89, v89\n"
"v_mfma_f32_16x16x32_bf16 v[156:159], a[188:191], v[140:143], v[156:159]\n"
"ds_read_b64_tr_b16 a[204:205], v23 offset:8192\n"
"v_cvt_pk_bf16_f32 v178, v88, v89\n"
"v_mfma_f32_16x16x32_bf16 v[152:155], a[188:191], v[136:139], v[232:235]\n"
"v_exp_f32 v90, v90\n"
"v_mfma_f32_16x16x32_bf16 v[152:155], a[184:187], v[104:107], v[152:155]\n"
"ds_read_b64_tr_b16 a[206:207], v23 offset:12288\n"
"v_add_u32 v23, s71, v23\n"
"v_mfma_f32_16x16x32_bf16 v[152:155], a[180:183], v[72:75], v[152:155]\n"
"v_exp_f32 v91, v91\n"
"v_mfma_f32_16x16x32_bf16 v[152:155], a[176:179], v[40:43], v[152:155]\n"
"ds_read_b64_tr_b16 a[208:209], v24 offset:0\n"
"v_cvt_pk_bf16_f32 v179, v90, v91\n"
"v_mfma_f32_16x16x32_bf16 v[148:151], a[176:179], v[36:39], v[228:231]\n"
"v_exp_f32 v84, v84\n"
"v_mfma_f32_16x16x32_bf16 v[148:151], a[180:183], v[68:71], v[148:151]\n"
"buffer_load_dwordx4 v16, s[36:39], s51 offen offset:2048 lds\n"
"v_mfma_f32_16x16x32_bf16 v[148:151], a[184:187], v[100:103], v[148:151]\n"
"v_exp_f32 v85, v85\n"
"v_mfma_f32_16x16x32_bf16 v[148:151], a[188:191], v[132:135], v[148:151]\n"
"ds_read_b64_tr_b16 a[210:211], v24 offset:4096\n"
"v_cvt_pk_bf16_f32 v170, v84, v85\n"
"v_mfma_f32_16x16x32_bf16 v[144:147], a[188:191], v[128:131], v[224:227]\n"
"v_exp_f32 v86, v86\n"
"v_mfma_f32_16x16x32_bf16 v[144:147], a[184:187], v[96:99], v[144:147]\n"
"ds_read_b64_tr_b16 a[212:213], v24 offset:8192\n"
"v_add_u32 v21, s70, v21\n"
"v_mfma_f32_16x16x32_bf16 v[144:147], a[180:183], v[64:67], v[144:147]\n"
"v_exp_f32 v87, v87\n"
"v_mfma_f32_16x16x32_bf16 v[144:147], a[176:179], v[32:35], v[144:147]\n"
"ds_read_b64_tr_b16 a[214:215], v24 offset:12288\n"
"v_cvt_pk_bf16_f32 v171, v86, v87\n"
"v_mfma_f32_16x16x32_bf16 a[0:3], a[192:195], v[192:195], a[0:3]\n"
"v_exp_f32 v80, v80\n"
"v_mfma_f32_16x16x32_bf16 a[0:3], a[196:199], v[196:199], a[0:3]\n"
"buffer_load_dwordx4 v17, s[36:39], s51 offen offset:3072 lds\n"
"v_mfma_f32_16x16x32_bf16 a[4:7], a[196:199], v[204:207], a[4:7]\n"
"s_add_u32 s51, s51, 16384\n"
"v_exp_f32 v81, v81\n"
"v_mfma_f32_16x16x32_bf16 a[4:7], a[192:195], v[200:203], a[4:7]\n"
"ds_read_b64_tr_b16 a[216:217], v25 offset:0\n"
"v_cvt_pk_bf16_f32 v162, v80, v81\n"
"v_mfma_f32_16x16x32_bf16 a[8:11], a[192:195], v[208:211], a[8:11]\n"
"v_exp_f32 v82, v82\n"
"v_mfma_f32_16x16x32_bf16 a[8:11], a[196:199], v[212:215], a[8:11]\n"
"ds_read_b64_tr_b16 a[218:219], v25 offset:4096\n"
"v_add_u32 v24, s71, v24\n"
"v_mfma_f32_16x16x32_bf16 a[12:15], a[196:199], v[220:223], a[12:15]\n"
"v_exp_f32 v83, v83\n"
"v_mfma_f32_16x16x32_bf16 a[12:15], a[192:195], v[216:219], a[12:15]\n"
"s_waitcnt lgkmcnt(2)\n"
"ds_read_b64_tr_b16 a[220:221], v25 offset:8192\n"
"v_cvt_pk_bf16_f32 v163, v82, v83\n"
"v_mfma_f32_16x16x32_bf16 a[28:31], a[200:203], v[216:219], a[28:31]\n"
"v_exp_f32 v112, v112\n"
"v_mfma_f32_16x16x32_bf16 a[28:31], a[204:207], v[220:223], a[28:31]\n"
"ds_read_b64_tr_b16 a[222:223], v25 offset:12288\n"
"v_add_u32 v25, s71, v25\n"
"v_mfma_f32_16x16x32_bf16 a[24:27], a[204:207], v[212:215], a[24:27]\n"
"v_exp_f32 v113, v113\n"
"v_mfma_f32_16x16x32_bf16 a[24:27], a[200:203], v[208:211], a[24:27]\n"
"ds_read_b64_tr_b16 a[192:193], v26 offset:0\n"
"v_cvt_pk_bf16_f32 v164, v112, v113\n"
"v_mfma_f32_16x16x32_bf16 a[20:23], a[200:203], v[200:203], a[20:23]\n"
"s_add_u32 m0, s74, 0\n"
"v_exp_f32 v114, v114\n"
"v_mfma_f32_16x16x32_bf16 a[20:23], a[204:207], v[204:207], a[20:23]\n"
"s_mov_b32 s9, 0\n"
"buffer_load_dwordx4 v2, s[44:47], s9 offen lds\n"
"v_mfma_f32_16x16x32_bf16 a[16:19], a[204:207], v[196:199], a[16:19]\n"
"v_exp_f32 v115, v115\n"
"v_mfma_f32_16x16x32_bf16 a[16:19], a[200:203], v[192:195], a[16:19]\n"
"ds_read_b64_tr_b16 a[194:195], v26 offset:4096\n"
"v_cvt_pk_bf16_f32 v165, v114, v115\n"
"v_mfma_f32_16x16x32_bf16 a[32:35], a[208:211], v[192:195], a[32:35]\n"
"v_exp_f32 v116, v116\n"
"v_mfma_f32_16x16x32_bf16 a[32:35], a[212:215], v[196:199], a[32:35]\n"
"ds_read_b64_tr_b16 a[196:197], v26 offset:8192\n"
"v_mfma_f32_16x16x32_bf16 a[36:39], a[212:215], v[204:207], a[36:39]\n"
"v_exp_f32 v117, v117\n"
"v_mfma_f32_16x16x32_bf16 a[36:39], a[208:211], v[200:203], a[36:39]\n"
"ds_read_b64_tr_b16 a[198:199], v26 offset:12288\n"
"v_cvt_pk_bf16_f32 v172, v116, v117\n"
"v_mfma_f32_16x16x32_bf16 a[40:43], a[208:211], v[208:211], a[40:43]\n"
"v_exp_f32 v118, v118\n"
"v_mfma_f32_16x16x32_bf16 a[40:43], a[212:215], v[212:215], a[40:43]\n"
"ds_read_b64_tr_b16 a[200:201], v27 offset:0\n"
"v_add_u32 v26, s71, v26\n"
"v_mfma_f32_16x16x32_bf16 a[44:47], a[212:215], v[220:223], a[44:47]\n"
"v_exp_f32 v119, v119\n"
"v_mfma_f32_16x16x32_bf16 a[44:47], a[208:211], v[216:219], a[44:47]\n"
"s_waitcnt lgkmcnt(2)\n"
"ds_read_b64_tr_b16 a[202:203], v27 offset:4096\n"
"v_cvt_pk_bf16_f32 v173, v118, v119\n"
"v_mfma_f32_16x16x32_bf16 a[60:63], a[216:219], v[216:219], a[60:63]\n"
"s_add_u32 m0, s74, 1024\n"
"v_exp_f32 v120, v120\n"
"v_mfma_f32_16x16x32_bf16 a[60:63], a[220:223], v[220:223], a[60:63]\n"
"s_mov_b32 s9, 9216\n"
"buffer_load_dwordx4 v2, s[44:47], s9 offen lds\n"
"v_mfma_f32_16x16x32_bf16 a[56:59], a[220:223], v[212:215], a[56:59]\n"
"v_exp_f32 v121, v121\n"
"v_mfma_f32_16x16x32_bf16 a[56:59], a[216:219], v[208:211], a[56:59]\n"
"ds_read_b64_tr_b16 a[204:205], v27 offset:8192\n"
"v_cvt_pk_bf16_f32 v180, v120, v121\n"
"v_mfma_f32_16x16x32_bf16 a[52:55], a[216:219], v[200:203], a[52:55]\n"
"v_exp_f32 v122, v122\n"
"v_mfma_f32_16x16x32_bf16 a[52:55], a[220:223], v[204:207], a[52:55]\n"
"ds_read_b64_tr_b16 a[206:207], v27 offset:12288\n"
"v_add_u32 v27, s71, v27\n"
"v_mfma_f32_16x16x32_bf16 a[48:51], a[220:223], v[196:199], a[48:51]\n"
"v_exp_f32 v123, v123\n"
"v_mfma_f32_16x16x32_bf16 a[48:51], a[216:219], v[192:195], a[48:51]\n"
"ds_read_b64_tr_b16 a[208:209], v28 offset:0\n"
"v_cvt_pk_bf16_f32 v181, v122, v123\n"
"v_mfma_f32_16x16x32_bf16 a[64:67], a[192:195], v[192:195], a[64:67]\n"
"s_waitcnt lgkmcnt(3)\n"
"v_exp_f32 v124, v124\n"
"v_mfma_f32_16x16x32_bf16 a[64:67], a[196:199], v[196:199], a[64:67]\n"
"ds_read_b64_tr_b16 a[210:211], v28 offset:4096\n"
"v_mfma_f32_16x16x32_bf16 a[68:71], a[196:199], v[204:207], a[68:71]\n"
"v_exp_f32 v125, v125\n"
"v_mfma_f32_16x16x32_bf16 a[68:71], a[192:195], v[200:203], a[68:71]\n"
"ds_read_b64_tr_b16 a[212:213], v28 offset:8192\n"
"v_cvt_pk_bf16_f32 v188, v124, v125\n"
"v_mfma_f32_16x16x32_bf16 a[72:75], a[192:195], v[208:211], a[72:75]\n"
"s_add_u32 m0, s74, 2048\n"
"v_exp_f32 v126, v126\n"
"v_mfma_f32_16x16x32_bf16 a[72:75], a[196:199], v[212:215], a[72:75]\n"
"s_mov_b32 s9, 18432\n"
"buffer_load_dwordx4 v2, s[44:47], s9 offen lds\n"
"v_mfma_f32_16x16x32_bf16 a[76:79], a[196:199], v[220:223], a[76:79]\n"
"v_exp_f32 v127, v127\n"
"v_mfma_f32_16x16x32_bf16 a[76:79], a[192:195], v[216:219], a[76:79]\n"
"ds_read_b64_tr_b16 a[214:215], v28 offset:12288\n"
"v_cvt_pk_bf16_f32 v189, v126, v127\n"
"v_mfma_f32_16x16x32_bf16 a[92:95], a[200:203], v[216:219], a[92:95]\n"
"s_waitcnt lgkmcnt(2)\n"
"v_exp_f32 v156, v156\n"
"v_mfma_f32_16x16x32_bf16 a[92:95], a[204:207], v[220:223], a[92:95]\n"
"ds_read_b64_tr_b16 a[216:217], v29 offset:0\n"
"v_add_u32 v28, s71, v28\n"
"v_mfma_f32_16x16x32_bf16 a[88:91], a[204:207], v[212:215], a[88:91]\n"
"v_exp_f32 v157, v157\n"
"v_mfma_f32_16x16x32_bf16 a[88:91], a[200:203], v[208:211], a[88:91]\n"
"ds_read_b64_tr_b16 a[218:219], v29 offset:4096\n"
"v_cvt_pk_bf16_f32 v190, v156, v157\n"
"v_mfma_f32_16x16x32_bf16 a[84:87], a[200:203], v[200:203], a[84:87]\n"
"v_exp_f32 v158, v158\n"
"v_mfma_f32_16x16x32_bf16 a[84:87], a[204:207], v[204:207], a[84:87]\n"
"ds_read_b64_tr_b16 a[220:221], v29 offset:8192\n"
"v_mfma_f32_16x16x32_bf16 a[80:83], a[204:207], v[196:199], a[80:83]\n"
"v_exp_f32 v159, v159\n"
"v_mfma_f32_16x16x32_bf16 a[80:83], a[200:203], v[192:195], a[80:83]\n"
"ds_read_b64_tr_b16 a[222:223], v29 offset:12288\n"
"v_cvt_pk_bf16_f32 v191, v158, v159\n"
"v_mfma_f32_16x16x32_bf16 a[96:99], a[208:211], v[192:195], a[96:99]\n"
"s_waitcnt lgkmcnt(3)\n"
"s_add_u32 m0, s74, 3072\n"
"v_exp_f32 v152, v152\n"
"v_mfma_f32_16x16x32_bf16 a[96:99], a[212:215], v[196:199], a[96:99]\n"
"s_mov_b32 s9, 27648\n"
"buffer_load_dwordx4 v2, s[44:47], s9 offen lds\n"
"v_mfma_f32_16x16x32_bf16 a[100:103], a[212:215], v[204:207], a[100:103]\n"
"v_exp_f32 v153, v153\n"
"v_mfma_f32_16x16x32_bf16 a[100:103], a[208:211], v[200:203], a[100:103]\n"
"v_cvt_pk_bf16_f32 v182, v152, v153\n"
"v_add_u32 v29, s71, v29\n"
"v_mfma_f32_16x16x32_bf16 a[104:107], a[208:211], v[208:211], a[104:107]\n"
"v_exp_f32 v154, v154\n"
"v_mfma_f32_16x16x32_bf16 a[104:107], a[212:215], v[212:215], a[104:107]\n"
"s_waitcnt vmcnt(16) lgkmcnt(0)\n"
"s_barrier\n"
"v_mfma_f32_16x16x32_bf16 a[108:111], a[212:215], v[220:223], a[108:111]\n"
"v_exp_f32 v155, v155\n"
"v_mfma_f32_16x16x32_bf16 a[108:111], a[208:211], v[216:219], a[108:111]\n"
"ds_read_b128 a[128:131], v18 offset:0\n"
"v_cvt_pk_bf16_f32 v183, v154, v155\n"
"v_mfma_f32_16x16x32_bf16 a[124:127], a[216:219], v[216:219], a[124:127]\n"
"v_exp_f32 v148, v148\n"
"v_mfma_f32_16x16x32_bf16 a[124:127], a[220:223], v[220:223], a[124:127]\n"
"ds_read_b128 a[132:135], v19 offset:0\n"
"v_mfma_f32_16x16x32_bf16 a[120:123], a[220:223], v[212:215], a[120:123]\n"
"v_exp_f32 v149, v149\n"
"v_mfma_f32_16x16x32_bf16 a[120:123], a[216:219], v[208:211], a[120:123]\n"
"ds_read_b128 a[136:139], v20 offset:0\n"
"v_cvt_pk_bf16_f32 v174, v148, v149\n"
"v_mfma_f32_16x16x32_bf16 a[116:119], a[216:219], v[200:203], a[116:119]\n"
"v_exp_f32 v150, v150\n"
"v_mfma_f32_16x16x32_bf16 a[116:119], a[220:223], v[204:207], a[116:119]\n"
"ds_read_b128 a[140:143], v21 offset:0\n"
"v_mfma_f32_16x16x32_bf16 a[112:115], a[220:223], v[196:199], a[112:115]\n"
"v_exp_f32 v151, v151\n"
"v_mfma_f32_16x16x32_bf16 a[112:115], a[216:219], v[192:195], a[112:115]\n"
"ds_read_b128 a[144:147], v18 offset:4096\n"
"v_cvt_pk_bf16_f32 v175, v150, v151\n"
"v_smfmac_f32_16x16x64_bf16 a[224:227], v[240:243], v[192:199], v244\n"
"ds_read_b128 a[148:151], v19 offset:4096\n"
"v_smfmac_f32_16x16x64_bf16 a[228:231], v[240:243], v[200:207], v244\n"
"ds_read_b128 a[152:155], v20 offset:4096\n"
"v_smfmac_f32_16x16x64_bf16 a[232:235], v[240:243], v[208:215], v244\n"
"ds_read_b128 a[156:159], v21 offset:4096\n"
"v_smfmac_f32_16x16x64_bf16 a[236:239], v[240:243], v[216:223], v244\n"
"s_waitcnt lgkmcnt(4)\n"
"s_branch .LATT_X\n"
".LATT_HOOK:\n"
"s_mov_b32 s11, 262144\n"
".LPE_L:\n"
"v_cmp_gt_u32 vcc, 8, v253\n"
"s_nop 0\n"
"s_cmp_eq_u64 vcc, 0\n"
"s_cbranch_scc1 .LPE_X\n"
"s_sub_u32 s11, s11, 1\n"
"s_cbranch_scc1 .LPE_X\n"
"s_sleep 2\n"
"buffer_load_dword v253, v254, s[24:27], 0 offen sc1\n"
"s_waitcnt vmcnt(0)\n"
"s_branch .LPE_L\n"
".LPE_X:\n"
"s_add_u32 s7, s48, 8192\n"
"s_add_u32 s6, s48, 98304\n"
"s_lshl_b32 s54, s59, 2\n"
"s_add_u32 s54, s54, s61\n"
"s_getpc_b64 s[12:13]\n"
".LPF_PC1:\n"
"s_sub_u32 s12, s12, .LPF_PC1-.LK_START\n"
"s_subb_u32 s13, s13, 0\n"
"s_mov_b32 s14, .LK_END-.LK_START\n"
"s_mov_b32 s15, 0x20000\n"
"s_lshl_b32 s10, s54, 2\n"
"s_add_u32 s10, s10, s83\n"
"s_lshl_b32 s10, s10, 10\n"
"buffer_load_dword v248, v1, s[12:15], s10 offen\n"
"s_lshr_b32 s10, s10, 3\n"
"v_add_u32 v30, s84, v1\n"
"v_xor_b32 v3, s58, v0\n"
"v_lshlrev_b32 v3, 4, v3\n"
"v_add_u32 v2, 90112, v1\n"
"s_mul_i32 s74, s82, 0\n"
"s_add_u32 s74, s74, 73728\n"
"s_branch .LATT_Y\n"
"s_nop 0\n"
"s_nop 0\n"
"s_nop 0\n"
"s_nop 0\n"
"s_nop 0\n"
"s_nop 0\n"
"s_nop 0\n"
"s_nop 0\n"
"s_nop 0\n"
"s_nop 0\n"
"s_nop 0\n"
"s_nop 0\n"
"s_nop 0\n"
"s_nop 0\n"
"s_nop 0\n"
".LATT_XY:\n"
".LPAD8:\n"
"s_cbranch_execz .LPAD11\n"
".LATT_Y:\n"
"v_mfma_f32_16x16x32_bf16 v[48:51], a[128:131], v[32:35], v[224:227]\n"
"s_mov_b32 m0, s69\n"
"v_exp_f32 v144, v144\n"
"v_mfma_f32_16x16x32_bf16 v[48:51], a[132:135], v[64:67], v[48:51]\n"
"buffer_load_dwordx4 v12, s[32:35], s50 offen offset:0 lds\n"
"v_mfma_f32_16x16x32_bf16 v[48:51], a[136:139], v[96:99], v[48:51]\n"
"s_sub_u32 s70, s68, s67\n"
"v_exp_f32 v145, v145\n"
"v_mfma_f32_16x16x32_bf16 v[48:51], a[140:143], v[128:131], v[48:51]\n"
"s_sub_u32 s71, s67, s69\n"
"ds_read_b128 a[160:163], v18 offset:8192\n"
"v_cvt_pk_bf16_f32 v198, v144, v145\n"
"v_mfma_f32_16x16x32_bf16 v[52:55], a[140:143], v[132:135], v[228:231]\n"
"v_exp_f32 v146, v146\n"
"v_mfma_f32_16x16x32_bf16 v[52:55], a[136:139], v[100:103], v[52:55]\n"
"buffer_load_dword v253, v254, s[24:27], 0 offen sc1\n"
"v_mfma_f32_16x16x32_bf16 v[52:55], a[132:135], v[68:71], v[52:55]\n"
"v_exp_f32 v147, v147\n"
"v_mfma_f32_16x16x32_bf16 v[52:55], a[128:131], v[36:39], v[52:55]\n"
"ds_read_b128 a[164:167], v19 offset:8192\n"
"v_cvt_pk_bf16_f32 v199, v146, v147\n"
"v_mfma_f32_16x16x32_bf16 v[56:59], a[128:131], v[40:43], v[232:235]\n"
"v_exp_f32 v48, v48\n"
"v_mfma_f32_16x16x32_bf16 v[56:59], a[132:135], v[72:75], v[56:59]\n"
"buffer_load_dwordx4 v13, s[32:35], s50 offen offset:1024 lds\n"
"v_mfma_f32_16x16x32_bf16 v[56:59], a[136:139], v[104:107], v[56:59]\n"
"v_exp_f32 v49, v49\n"
"v_mfma_f32_16x16x32_bf16 v[56:59], a[140:143], v[136:139], v[56:59]\n"
"ds_read_b128 a[168:171], v20 offset:8192\n"
"v_cvt_pk_bf16_f32 v160, v48, v49\n"
"v_mfma_f32_16x16x32_bf16 v[60:63], a[140:143], v[140:143], v[236:239]\n"
"v_exp_f32 v50, v50\n"
"v_mfma_f32_16x16x32_bf16 v[60:63], a[136:139], v[108:111], v[60:63]\n"
"v_mfma_f32_16x16x32_bf16 v[60:63], a[132:135], v[76:79], v[60:63]\n"
"v_exp_f32 v51, v51\n"
"v_mfma_f32_16x16x32_bf16 v[60:63], a[128:131], v[44:47], v[60:63]\n"
"s_waitcnt lgkmcnt(1)\n"
"ds_read_b128 a[172:175], v21 offset:8192\n"
"v_cvt_pk_bf16_f32 v161, v50, v51\n"
"v_mfma_f32_16x16x32_bf16 v[92:95], a[144:147], v[44:47], v[236:239]\n"
"v_exp_f32 v52, v52\n"
"v_mfma_f32_16x16x32_bf16 v[92:95], a[148:151], v[76:79], v[92:95]\n"
"buffer_load_dwordx4 v14, s[32:35], s50 offen offset:2048 lds\n"
"v_mfma_f32_16x16x32_bf16 v[92:95], a[152:155], v[108:111], v[92:95]\n"
"v_exp_f32 v53, v53\n"
"v_mfma_f32_16x16x32_bf16 v[92:95], a[156:159], v[140:143], v[92:95]\n"
"ds_read_b128 a[176:179], v18 offset:12288\n"
"v_cvt_pk_bf16_f32 v168, v52, v53\n"
"v_mfma_f32_16x16x32_bf16 v[88:91], a[156:159], v[136:139], v[232:235]\n"
"v_exp_f32 v54, v54\n"
"v_mfma_f32_16x16x32_bf16 v[88:91], a[152:155], v[104:107], v[88:91]\n"
"v_mfma_f32_16x16x32_bf16 v[88:91], a[148:151], v[72:75], v[88:91]\n"
"v_exp_f32 v55, v55\n"
"v_mfma_f32_16x16x32_bf16 v[88:91], a[144:147], v[40:43], v[88:91]\n"
"ds_read_b128 a[180:183], v19 offset:12288\n"
"v_cvt_pk_bf16_f32 v169, v54, v55\n"
"v_mfma_f32_16x16x32_bf16 v[84:87], a[144:147], v[36:39], v[228:231]\n"
"v_exp_f32 v56, v56\n"
"v_mfma_f32_16x16x32_bf16 v[84:87], a[148:151], v[68:71], v[84:87]\n"
"buffer_load_dwordx4 v15, s[32:35], s50 offen offset:3072 lds\n"
"v_mfma_f32_16x16x32_bf16 v[84:87], a[152:155], v[100:103], v[84:87]\n"
"s_add_u32 s50, s50, 16384\n"
"v_exp_f32 v57, v57\n"
"v_mfma_f32_16x16x32_bf16 v[84:87], a[156:159], v[132:135], v[84:87]\n"
"ds_read_b128 a[184:187], v20 offset:12288\n"
"v_cvt_pk_bf16_f32 v176, v56, v57\n"
"v_mfma_f32_16x16x32_bf16 v[80:83], a[156:159], v[128:131], v[224:227]\n"
"v_exp_f32 v58, v58\n"
"v_mfma_f32_16x16x32_bf16 v[80:83], a[152:155], v[96:99], v[80:83]\n"
"v_mfma_f32_16x16x32_bf16 v[80:83], a[148:151], v[64:67], v[80:83]\n"
"v_exp_f32 v59, v59\n"
"v_mfma_f32_16x16x32_bf16 v[80:83], a[144:147], v[32:35], v[80:83]\n"
"ds_read_b128 a[188:191], v21 offset:12288\n"
"v_cvt_pk_bf16_f32 v177, v58, v59\n"
"v_mfma_f32_16x16x32_bf16 v[112:115], a[160:163], v[32:35], v[224:227]\n"
"v_exp_f32 v60, v60\n"
"v_mfma_f32_16x16x32_bf16 v[112:115], a[164:167], v[64:67], v[112:115]\n"
"s_waitcnt lgkmcnt(1)\n"
"v_add_u32 v18, s70, v18\n"
"v_add_u32 v19, s70, v19\n"
"v_mfma_f32_16x16x32_bf16 v[112:115], a[168:171], v[96:99], v[112:115]\n"
"v_exp_f32 v61, v61\n"
"v_mfma_f32_16x16x32_bf16 v[112:115], a[172:175], v[128:131], v[112:115]\n"
"ds_read_b64_tr_b16 a[192:193], v22 offset:0\n"
"v_cvt_pk_bf16_f32 v184, v60, v61\n"
"v_mfma_f32_16x16x32_bf16 v[116:119], a[172:175], v[132:135], v[228:231]\n"
"s_add_u32 m0, s68, 49152\n"
"v_exp_f32 v62, v62\n"
"v_mfma_f32_16x16x32_bf16 v[116:119], a[168:171], v[100:103], v[116:119]\n"
"buffer_load_dwordx4 v16, s[36:39], s51 offen offset:0 lds\n"
"v_mfma_f32_16x16x32_bf16 v[116:119], a[164:167], v[68:71], v[116:119]\n"
"v_exp_f32 v63, v63\n"
"v_mfma_f32_16x16x32_bf16 v[116:119], a[160:163], v[36:39], v[116:119]\n"
"s_mov_b32 s10, s67\n"
"ds_read_b64_tr_b16 a[194:195], v22 offset:4096\n"
"v_cvt_pk_bf16_f32 v185, v62, v63\n"
"v_mfma_f32_16x16x32_bf16 v[120:123], a[160:163], v[40:43], v[232:235]\n"
"s_mov_b32 s67, s68\n"
"v_exp_f32 v92, v92\n"
"v_mfma_f32_16x16x32_bf16 v[120:123], a[164:167], v[72:75], v[120:123]\n"
"s_mov_b32 s68, s69\n"
"ds_read_b64_tr_b16 a[196:197], v22 offset:8192\n"
"v_add_u32 v20, s70, v20\n"
"v_mfma_f32_16x16x32_bf16 v[120:123], a[168:171], v[104:107], v[120:123]\n"
"s_mov_b32 s69, s10\n"
"v_exp_f32 v93, v93\n"
"v_mfma_f32_16x16x32_bf16 v[120:123], a[172:175], v[136:139], v[120:123]\n"
"ds_read_b64_tr_b16 a[198:199], v22 offset:12288\n"
"v_cvt_pk_bf16_f32 v186, v92, v93\n"
"v_mfma_f32_16x16x32_bf16 v[124:127], a[172:175], v[140:143], v[236:239]\n"
"v_exp_f32 v94, v94\n"
"v_mfma_f32_16x16x32_bf16 v[124:127], a[168:171], v[108:111], v[124:127]\n"
"buffer_load_dwordx4 v17, s[36:39], s51 offen offset:1024 lds\n"
"v_mfma_f32_16x16x32_bf16 v[124:127], a[164:167], v[76:79], v[124:127]\n"
"v_exp_f32 v95, v95\n"
"v_mfma_f32_16x16x32_bf16 v[124:127], a[160:163], v[44:47], v[124:127]\n"
"ds_read_b64_tr_b16 a[200:201], v23 offset:0\n"
"v_cvt_pk_bf16_f32 v187, v94, v95\n"
"v_mfma_f32_16x16x32_bf16 v[156:159], a[176:179], v[44:47], v[236:239]\n"
"v_exp_f32 v88, v88\n"
"v_mfma_f32_16x16x32_bf16 v[156:159], a[180:183], v[76:79], v[156:159]\n"
"ds_read_b64_tr_b16 a[202:203], v23 offset:4096\n"
"v_add_u32 v22, s71, v22\n"
"v_mfma_f32_16x16x32_bf16 v[156:159], a[184:187], v[108:111], v[156:159]\n"
"s_waitcnt lgkmcnt(2)\n"
"v_exp_f32 v89, v89\n"
"v_mfma_f32_16x16x32_bf16 v[156:159], a[188:191], v[140:143], v[156:159]\n"
"ds_read_b64_tr_b16 a[204:205], v23 offset:8192\n"
"v_cvt_pk_bf16_f32 v178, v88, v89\n"
"v_mfma_f32_16x16x32_bf16 v[152:155], a[188:191], v[136:139], v[232:235]\n"
"v_exp_f32 v90, v90\n"
"v_mfma_f32_16x16x32_bf16 v[152:155], a[184:187], v[104:107], v[152:155]\n"
"ds_read_b64_tr_b16 a[206:207], v23 offset:12288\n"
"v_add_u32 v23, s71, v23\n"
"v_mfma_f32_16x16x32_bf16 v[152:155], a[180:183], v[72:75], v[152:155]\n"
"v_exp_f32 v91, v91\n"
"v_mfma_f32_16x16x32_bf16 v[152:155], a[176:179], v[40:43], v[152:155]\n"
"ds_read_b64_tr_b16 a[208:209], v24 offset:0\n"
"v_cvt_pk_bf16_f32 v179, v90, v91\n"
"v_mfma_f32_16x16x32_bf16 v[148:151], a[176:179], v[36:39], v[228:231]\n"
"v_exp_f32 v84, v84\n"
"v_mfma_f32_16x16x32_bf16 v[148:151], a[180:183], v[68:71], v[148:151]\n"
"buffer_load_dwordx4 v16, s[36:39], s51 offen offset:2048 lds\n"
"v_mfma_f32_16x16x32_bf16 v[148:151], a[184:187], v[100:103], v[148:151]\n"
"v_exp_f32 v85, v85\n"
"v_mfma_f32_16x16x32_bf16 v[148:151], a[188:191], v[132:135], v[148:151]\n"
"ds_read_b64_tr_b16 a[210:211], v24 offset:4096\n"
"v_cvt_pk_bf16_f32 v170, v84, v85\n"
"v_mfma_f32_16x16x32_bf16 v[144:147], a[188:191], v[128:131], v[224:227]\n"
"v_exp_f32 v86, v86\n"
"v_mfma_f32_16x16x32_bf16 v[144:147], a[184:187], v[96:99], v[144:147]\n"
"ds_read_b64_tr_b16 a[212:213], v24 offset:8192\n"
"v_add_u32 v21, s70, v21\n"
"v_mfma_f32_16x16x32_bf16 v[144:147], a[180:183], v[64:67], v[144:147]\n"
"v_exp_f32 v87, v87\n"
"v_mfma_f32_16x16x32_bf16 v[144:147], a[176:179], v[32:35], v[144:147]\n"
"ds_read_b64_tr_b16 a[214:215], v24 offset:12288\n"
"v_cvt_pk_bf16_f32 v171, v86, v87\n"
"v_mfma_f32_16x16x32_bf16 a[0:3], a[192:195], v[192:195], a[0:3]\n"
"v_exp_f32 v80, v80\n"
"v_mfma_f32_16x16x32_bf16 a[0:3], a[196:199], v[196:199], a[0:3]\n"
"buffer_load_dwordx4 v17, s[36:39], s51 offen offset:3072 lds\n"
"v_mfma_f32_16x16x32_bf16 a[4:7], a[196:199], v[204:207], a[4:7]\n"
"s_add_u32 s51, s51, 16384\n"
"v_exp_f32 v81, v81\n"
"v_mfma_f32_16x16x32_bf16 a[4:7], a[192:195], v[200:203], a[4:7]\n"
"ds_read_b64_tr_b16 a[216:217], v25 offset:0\n"
"v_cvt_pk_bf16_f32 v162, v80, v81\n"
"v_mfma_f32_16x16x32_bf16 a[8:11], a[192:195], v[208:211], a[8:11]\n"
"v_exp_f32 v82, v82\n"
"v_mfma_f32_16x16x32_bf16 a[8:11], a[196:199], v[212:215], a[8:11]\n"
"ds_read_b64_tr_b16 a[218:219], v25 offset:4096\n"
"v_add_u32 v24, s71, v24\n"
"v_mfma_f32_16x16x32_bf16 a[12:15], a[196:199], v[220:223], a[12:15]\n"
"v_exp_f32 v83, v83\n"
"v_mfma_f32_16x16x32_bf16 a[12:15], a[192:195], v[216:219], a[12:15]\n"
"s_waitcnt lgkmcnt(2)\n"
"ds_read_b64_tr_b16 a[220:221], v25 offset:8192\n"
"v_cvt_pk_bf16_f32 v163, v82, v83\n"
"v_mfma_f32_16x16x32_bf16 a[28:31], a[200:203], v[216:219], a[28:31]\n"
"v_exp_f32 v112, v112\n"
"v_mfma_f32_16x16x32_bf16 a[28:31], a[204:207], v[220:223], a[28:31]\n"
"ds_read_b64_tr_b16 a[222:223], v25 offset:12288\n"
"v_add_u32 v25, s71, v25\n"
"v_mfma_f32_16x16x32_bf16 a[24:27], a[204:207], v[212:215], a[24:27]\n"
"s_cmp_eq_u32 s50, 344064\n"
"v_exp_f32 v113, v113\n"
"v_mfma_f32_16x16x32_bf16 a[24:27], a[200:203], v[208:211], a[24:27]\n"
"s_cselect_b32 s9, s75, 2146435072\n"
"ds_read_b64_tr_b16 a[192:193], v26 offset:0\n"
"v_cvt_pk_bf16_f32 v164, v112, v113\n"
"v_mfma_f32_16x16x32_bf16 a[20:23], a[200:203], v[200:203], a[20:23]\n"
"v_exp_f32 v114, v114\n"
"v_mfma_f32_16x16x32_bf16 a[20:23], a[204:207], v[204:207], a[20:23]\n"
"buffer_atomic_add v31, v255, s[24:27], s9 offen sc1\n"
"v_mfma_f32_16x16x32_bf16 a[16:19], a[204:207], v[196:199], a[16:19]\n"
"v_exp_f32 v115, v115\n"
"v_mfma_f32_16x16x32_bf16 a[16:19], a[200:203], v[192:195], a[16:19]\n"
"ds_read_b64_tr_b16 a[194:195], v26 offset:4096\n"
"v_cvt_pk_bf16_f32 v165, v114, v115\n"
"v_mfma_f32_16x16x32_bf16 a[32:35], a[208:211], v[192:195], a[32:35]\n"
"v_exp_f32 v116, v116\n"
"v_mfma_f32_16x16x32_bf16 a[32:35], a[212:215], v[196:199], a[32:35]\n"
"s_mul_i32 s84, s83, 9216\n"
"ds_read_b64_tr_b16 a[196:197], v26 offset:8192\n"
"v_mfma_f32_16x16x32_bf16 a[36:39], a[212:215], v[204:207], a[36:39]\n"
"s_lshl_b32 s85, s83, 11\n"
"v_exp_f32 v117, v117\n"
"v_mfma_f32_16x16x32_bf16 a[36:39], a[208:211], v[200:203], a[36:39]\n"
"s_lshl_b32 s54, s59, 3\n"
"ds_read_b64_tr_b16 a[198:199], v26 offset:12288\n"
"v_cvt_pk_bf16_f32 v172, v116, v117\n"
"v_mfma_f32_16x16x32_bf16 a[40:43], a[208:211], v[208:211], a[40:43]\n"
"s_and_b32 s55, s62, 1\n"
"v_exp_f32 v118, v118\n"
"v_mfma_f32_16x16x32_bf16 a[40:43], a[212:215], v[212:215], a[40:43]\n"
"s_lshl_b32 s55, s55, 2\n"
"ds_read_b64_tr_b16 a[200:201], v27 offset:0\n"
"v_add_u32 v26, s71, v26\n"
"v_mfma_f32_16x16x32_bf16 a[44:47], a[212:215], v[220:223], a[44:47]\n"
"s_add_u32 s54, s54, s55\n"
"v_exp_f32 v119, v119\n"
"v_mfma_f32_16x16x32_bf16 a[44:47], a[208:211], v[216:219], a[44:47]\n"
"s_waitcnt lgkmcnt(2)\n"
"ds_read_b64_tr_b16 a[202:203], v27 offset:4096\n"
"v_cvt_pk_bf16_f32 v173, v118, v119\n"
"v_mfma_f32_16x16x32_bf16 a[60:63], a[216:219], v[216:219], a[60:63]\n"
"s_mov_b32 m0, s6\n"
"v_exp_f32 v120, v120\n"
"v_mfma_f32_16x16x32_bf16 a[60:63], a[220:223], v[220:223], a[60:63]\n"
"s_add_u32 s56, s54, s83\n"
"buffer_load_dwordx4 v1, s[28:31], s7 offen offset:0 sc1 lds\n"
"v_mfma_f32_16x16x32_bf16 a[56:59], a[220:223], v[212:215], a[56:59]\n"
"s_lshl_b32 s54, s56, 2\n"
"v_exp_f32 v121, v121\n"
"v_mfma_f32_16x16x32_bf16 a[56:59], a[216:219], v[208:211], a[56:59]\n"
"s_add_u32 s54, s54, s58\n"
"ds_read_b64_tr_b16 a[204:205], v27 offset:8192\n"
"v_cvt_pk_bf16_f32 v180, v120, v121\n"
"v_mfma_f32_16x16x32_bf16 a[52:55], a[216:219], v[200:203], a[52:55]\n"
"s_mul_i32 s54, s54, 139264\n"
"v_exp_f32 v122, v122\n"
"v_mfma_f32_16x16x32_bf16 a[52:55], a[220:223], v[204:207], a[52:55]\n"
"s_add_u32 s54, s54, 4194304\n"
"ds_read_b64_tr_b16 a[206:207], v27 offset:12288\n"
"v_add_u32 v27, s71, v27\n"
"v_mfma_f32_16x16x32_bf16 a[48:51], a[220:223], v[196:199], a[48:51]\n"
"s_add_u32 s44, s80, s54\n"
"v_exp_f32 v123, v123\n"
"v_mfma_f32_16x16x32_bf16 a[48:51], a[216:219], v[192:195], a[48:51]\n"
"s_addc_u32 s45, s81, 0\n"
"ds_read_b64_tr_b16 a[208:209], v28 offset:0\n"
"v_cvt_pk_bf16_f32 v181, v122, v123\n"
"v_mfma_f32_16x16x32_bf16 a[64:67], a[192:195], v[192:195], a[64:67]\n"
"s_waitcnt lgkmcnt(3)\n"
"v_exp_f32 v124, v124\n"
"v_mfma_f32_16x16x32_bf16 a[64:67], a[196:199], v[196:199], a[64:67]\n"
"s_mov_b32 s46, 139264\n"
"ds_read_b64_tr_b16 a[210:211], v28 offset:4096\n"
"v_mfma_f32_16x16x32_bf16 a[68:71], a[196:199], v[204:207], a[68:71]\n"
"s_mov_b32 s47, 0x20000\n"
"v_exp_f32 v125, v125\n"
"v_mfma_f32_16x16x32_bf16 a[68:71], a[192:195], v[200:203], a[68:71]\n"
"s_and_b32 s57, s63, 7\n"
"ds_read_b64_tr_b16 a[212:213], v28 offset:8192\n"
"v_cvt_pk_bf16_f32 v188, v124, v125\n"
"v_mfma_f32_16x16x32_bf16 a[72:75], a[192:195], v[208:211], a[72:75]\n"
"s_lshl_b32 s57, s57, 13\n"
"v_exp_f32 v126, v126\n"
"v_mfma_f32_16x16x32_bf16 a[72:75], a[196:199], v[212:215], a[72:75]\n"
"s_add_u32 s57, s57, 73728\n"
"v_mfma_f32_16x16x32_bf16 a[76:79], a[196:199], v[220:223], a[76:79]\n"
"v_exp_f32 v127, v127\n"
"v_mfma_f32_16x16x32_bf16 a[76:79], a[192:195], v[216:219], a[76:79]\n"
"ds_read_b64_tr_b16 a[214:215], v28 offset:12288\n"
"v_cvt_pk_bf16_f32 v189, v126, v127\n"
"v_mfma_f32_16x16x32_bf16 a[92:95], a[200:203], v[216:219], a[92:95]\n"
"s_waitcnt lgkmcnt(2)\n"
"v_exp_f32 v156, v156\n"
"v_mfma_f32_16x16x32_bf16 a[92:95], a[204:207], v[220:223], a[92:95]\n"
"ds_read_b64_tr_b16 a[216:217], v29 offset:0\n"
"v_add_u32 v28, s71, v28\n"
"v_mfma_f32_16x16x32_bf16 a[88:91], a[204:207], v[212:215], a[88:91]\n"
"v_exp_f32 v157, v157\n"
"v_mfma_f32_16x16x32_bf16 a[88:91], a[200:203], v[208:211], a[88:91]\n"
".LPAD9:\n"
"s_cbranch_execz .LPAD12\n"
"ds_read_b64_tr_b16 a[218:219], v29 offset:4096\n"
"v_cvt_pk_bf16_f32 v190, v156, v157\n"
"v_mfma_f32_16x16x32_bf16 a[84:87], a[200:203], v[200:203], a[84:87]\n"
"v_exp_f32 v158, v158\n"
"v_mfma_f32_16x16x32_bf16 a[84:87], a[204:207], v[204:207], a[84:87]\n"
"ds_read_b64_tr_b16 a[220:221], v29 offset:8192\n"
"v_mfma_f32_16x16x32_bf16 a[80:83], a[204:207], v[196:199], a[80:83]\n"
"v_exp_f32 v159, v159\n"
"v_mfma_f32_16x16x32_bf16 a[80:83], a[200:203], v[192:195], a[80:83]\n"
"ds_read_b64_tr_b16 a[222:223], v29 offset:12288\n"
"v_cvt_pk_bf16_f32 v191, v158, v159\n"
"v_mfma_f32_16x16x32_bf16 a[96:99], a[208:211], v[192:195], a[96:99]\n"
"s_waitcnt lgkmcnt(3)\n"
"v_exp_f32 v152, v152\n"
"v_mfma_f32_16x16x32_bf16 a[96:99], a[212:215], v[196:199], a[96:99]\n"
"buffer_load_dwordx4 v1, s[28:31], s7 offen offset:1024 sc1 lds\n"
"v_mfma_f32_16x16x32_bf16 a[100:103], a[212:215], v[204:207], a[100:103]\n"
"v_exp_f32 v153, v153\n"
"v_mfma_f32_16x16x32_bf16 a[100:103], a[208:211], v[200:203], a[100:103]\n"
"v_cvt_pk_bf16_f32 v182, v152, v153\n"
"v_add_u32 v29, s71, v29\n"
"v_mfma_f32_16x16x32_bf16 a[104:107], a[208:211], v[208:211], a[104:107]\n"
"v_exp_f32 v154, v154\n"
"v_mfma_f32_16x16x32_bf16 a[104:107], a[212:215], v[212:215], a[104:107]\n"
"s_waitcnt vmcnt(16) lgkmcnt(0)\n"
"s_barrier\n"
"v_mfma_f32_16x16x32_bf16 a[108:111], a[212:215], v[220:223], a[108:111]\n"
"v_exp_f32 v155, v155\n"
"v_mfma_f32_16x16x32_bf16 a[108:111], a[208:211], v[216:219], a[108:111]\n"
"ds_read_b128 a[128:131], v18 offset:0\n"
"v_cvt_pk_bf16_f32 v183, v154, v155\n"
"v_mfma_f32_16x16x32_bf16 a[124:127], a[216:219], v[216:219], a[124:127]\n"
"v_exp_f32 v148, v148\n"
"v_mfma_f32_16x16x32_bf16 a[124:127], a[220:223], v[220:223], a[124:127]\n"
"ds_read_b128 a[132:135], v19 offset:0\n"
"v_mfma_f32_16x16x32_bf16 a[120:123], a[220:223], v[212:215], a[120:123]\n"
"v_exp_f32 v149, v149\n"
"v_mfma_f32_16x16x32_bf16 a[120:123], a[216:219], v[208:211], a[120:123]\n"
"ds_read_b128 a[136:139], v20 offset:0\n"
"v_cvt_pk_bf16_f32 v174, v148, v149\n"
"v_mfma_f32_16x16x32_bf16 a[116:119], a[216:219], v[200:203], a[116:119]\n"
"v_exp_f32 v150, v150\n"
"v_mfma_f32_16x16x32_bf16 a[116:119], a[220:223], v[204:207], a[116:119]\n"
"ds_read_b128 a[140:143], v21 offset:0\n"
"v_mfma_f32_16x16x32_bf16 a[112:115], a[220:223], v[196:199], a[112:115]\n"
"v_exp_f32 v151, v151\n"
"v_mfma_f32_16x16x32_bf16 a[112:115], a[216:219], v[192:195], a[112:115]\n"
"ds_read_b128 a[144:147], v18 offset:4096\n"
"v_cvt_pk_bf16_f32 v175, v150, v151\n"
"v_smfmac_f32_16x16x64_bf16 a[224:227], v[240:243], v[192:199], v244\n"
"ds_read_b128 a[148:151], v19 offset:4096\n"
"v_smfmac_f32_16x16x64_bf16 a[228:231], v[240:243], v[200:207], v244\n"
"ds_read_b128 a[152:155], v20 offset:4096\n"
"v_smfmac_f32_16x16x64_bf16 a[232:235], v[240:243], v[208:215], v244\n"
"ds_read_b128 a[156:159], v21 offset:4096\n"
"v_smfmac_f32_16x16x64_bf16 a[236:239], v[240:243], v[216:223], v244\n"
"s_waitcnt lgkmcnt(4)\n"
"v_mfma_f32_16x16x32_bf16 v[48:51], a[128:131], v[32:35], v[224:227]\n"
"s_mov_b32 m0, s69\n"
"v_exp_f32 v144, v144\n"
"v_mfma_f32_16x16x32_bf16 v[48:51], a[132:135], v[64:67], v[48:51]\n"
"buffer_load_dwordx4 v12, s[32:35], s50 offen offset:0 lds\n"
"v_mfma_f32_16x16x32_bf16 v[48:51], a[136:139], v[96:99], v[48:51]\n"
"s_sub_u32 s70, s68, s67\n"
"v_exp_f32 v145, v145\n"
"v_mfma_f32_16x16x32_bf16 v[48:51], a[140:143], v[128:131], v[48:51]\n"
"s_sub_u32 s71, s67, s69\n"
"ds_read_b128 a[160:163], v18 offset:8192\n"
"v_cvt_pk_bf16_f32 v166, v144, v145\n"
"v_mfma_f32_16x16x32_bf16 v[52:55], a[140:143], v[132:135], v[228:231]\n"
"v_exp_f32 v146, v146\n"
"v_mfma_f32_16x16x32_bf16 v[52:55], a[136:139], v[100:103], v[52:55]\n"
"v_mfma_f32_16x16x32_bf16 v[52:55], a[132:135], v[68:71], v[52:55]\n"
"v_exp_f32 v147, v147\n"
"v_mfma_f32_16x16x32_bf16 v[52:55], a[128:131], v[36:39], v[52:55]\n"
"ds_read_b128 a[164:167], v19 offset:8192\n"
"v_cvt_pk_bf16_f32 v167, v146, v147\n"
"v_mfma_f32_16x16x32_bf16 v[56:59], a[128:131], v[40:43], v[232:235]\n"
"v_exp_f32 v48, v48\n"
"v_mfma_f32_16x16x32_bf16 v[56:59], a[132:135], v[72:75], v[56:59]\n"
"buffer_load_dwordx4 v13, s[32:35], s50 offen offset:1024 lds\n"
"v_mfma_f32_16x16x32_bf16 v[56:59], a[136:139], v[104:107], v[56:59]\n"
"v_exp_f32 v49, v49\n"
"v_mfma_f32_16x16x32_bf16 v[56:59], a[140:143], v[136:139], v[56:59]\n"
"ds_read_b128 a[168:171], v20 offset:8192\n"
"v_cvt_pk_bf16_f32 v192, v48, v49\n"
"v_mfma_f32_16x16x32_bf16 v[60:63], a[140:143], v[140:143], v[236:239]\n"
"v_exp_f32 v50, v50\n"
"v_mfma_f32_16x16x32_bf16 v[60:63], a[136:139], v[108:111], v[60:63]\n"
"v_mfma_f32_16x16x32_bf16 v[60:63], a[132:135], v[76:79], v[60:63]\n"
"v_exp_f32 v51, v51\n"
"v_mfma_f32_16x16x32_bf16 v[60:63], a[128:131], v[44:47], v[60:63]\n"
"s_waitcnt lgkmcnt(1)\n"
"ds_read_b128 a[172:175], v21 offset:8192\n"
"v_cvt_pk_bf16_f32 v193, v50, v51\n"
"v_mfma_f32_16x16x32_bf16 v[92:95], a[144:147], v[44:47], v[236:239]\n"
"v_exp_f32 v52, v52\n"
"v_mfma_f32_16x16x32_bf16 v[92:95], a[148:151], v[76:79], v[92:95]\n"
"buffer_load_dwordx4 v14, s[32:35], s50 offen offset:2048 lds\n"
"v_mfma_f32_16x16x32_bf16 v[92:95], a[152:155], v[108:111], v[92:95]\n"
"v_exp_f32 v53, v53\n"
"v_mfma_f32_16x16x32_bf16 v[92:95], a[156:159], v[140:143], v[92:95]\n"
"ds_read_b128 a[176:179], v18 offset:12288\n"
"v_cvt_pk_bf16_f32 v200, v52, v53\n"
"v_mfma_f32_16x16x32_bf16 v[88:91], a[156:159], v[136:139], v[232:235]\n"
"v_exp_f32 v54, v54\n"
"v_mfma_f32_16x16x32_bf16 v[88:91], a[152:155], v[104:107], v[88:91]\n"
"v_mfma_f32_16x16x32_bf16 v[88:91], a[148:151], v[72:75], v[88:91]\n"
"v_exp_f32 v55, v55\n"
"v_mfma_f32_16x16x32_bf16 v[88:91], a[144:147], v[40:43], v[88:91]\n"
"ds_read_b128 a[180:183], v19 offset:12288\n"
"v_cvt_pk_bf16_f32 v201, v54, v55\n"
"v_mfma_f32_16x16x32_bf16 v[84:87], a[144:147], v[36:39], v[228:231]\n"
"v_exp_f32 v56, v56\n"
"v_mfma_f32_16x16x32_bf16 v[84:87], a[148:151], v[68:71], v[84:87]\n"
"buffer_load_dwordx4 v15, s[32:35], s50 offen offset:3072 lds\n"
"v_mfma_f32_16x16x32_bf16 v[84:87], a[152:155], v[100:103], v[84:87]\n"
"s_add_u32 s50, s50, 16384\n"
"v_exp_f32 v57, v57\n"
"v_mfma_f32_16x16x32_bf16 v[84:87], a[156:159], v[132:135], v[84:87]\n"
"ds_read_b128 a[184:187], v20 offset:12288\n"
"v_cvt_pk_bf16_f32 v208, v56, v57\n"
"v_mfma_f32_16x16x32_bf16 v[80:83], a[156:159], v[128:131], v[224:227]\n"
"v_exp_f32 v58, v58\n"
"v_mfma_f32_16x16x32_bf16 v[80:83], a[152:155], v[96:99], v[80:83]\n"
"v_mfma_f32_16x16x32_bf16 v[80:83], a[148:151], v[64:67], v[80:83]\n"
"v_exp_f32 v59, v59\n"
"v_mfma_f32_16x16x32_bf16 v[80:83], a[144:147], v[32:35], v[80:83]\n"
"ds_read_b128 a[188:191], v21 offset:12288\n"
"v_cvt_pk_bf16_f32 v209, v58, v59\n"
"v_mfma_f32_16x16x32_bf16 v[112:115], a[160:163], v[32:35], v[224:227]\n"
"v_exp_f32 v60, v60\n"
"v_mfma_f32_16x16x32_bf16 v[112:115], a[164:167], v[64:67], v[112:115]\n"
"s_waitcnt lgkmcnt(1)\n"
"v_add_u32 v18, s70, v18\n"
"v_add_u32 v19, s70, v19\n"
"v_mfma_f32_16x16x32_bf16 v[112:115], a[168:171], v[96:99], v[112:115]\n"
"v_exp_f32 v61, v61\n"
"v_mfma_f32_16x16x32_bf16 v[112:115], a[172:175], v[128:131], v[112:115]\n"
"ds_read_b64_tr_b16 a[192:193], v22 offset:0\n"
"v_cvt_pk_bf16_f32 v216, v60, v61\n"
"v_mfma_f32_16x16x32_bf16 v[116:119], a[172:175], v[132:135], v[228:231]\n"
"s_add_u32 m0, s68, 49152\n"
"v_exp_f32 v62, v62\n"
"v_mfma_f32_16x16x32_bf16 v[116:119], a[168:171], v[100:103], v[116:119]\n"
"buffer_load_dwordx4 v16, s[36:39], s51 offen offset:0 lds\n"
"v_mfma_f32_16x16x32_bf16 v[116:119], a[164:167], v[68:71], v[116:119]\n"
"v_exp_f32 v63, v63\n"
"v_mfma_f32_16x16x32_bf16 v[116:119], a[160:163], v[36:39], v[116:119]\n"
"s_mov_b32 s10, s67\n"
"ds_read_b64_tr_b16 a[194:195], v22 offset:4096\n"
"v_cvt_pk_bf16_f32 v217, v62, v63\n"
"v_mfma_f32_16x16x32_bf16 v[120:123], a[160:163], v[40:43], v[232:235]\n"
"s_mov_b32 s67, s68\n"
"v_exp_f32 v92, v92\n"
"v_mfma_f32_16x16x32_bf16 v[120:123], a[164:167], v[72:75], v[120:123]\n"
"s_mov_b32 s68, s69\n"
"ds_read_b64_tr_b16 a[196:197], v22 offset:8192\n"
"v_add_u32 v20, s70, v20\n"
"v_mfma_f32_16x16x32_bf16 v[120:123], a[168:171], v[104:107], v[120:123]\n"
"s_mov_b32 s69, s10\n"
"v_exp_f32 v93, v93\n"
"v_mfma_f32_16x16x32_bf16 v[120:123], a[172:175], v[136:139], v[120:123]\n"
"ds_read_b64_tr_b16 a[198:199], v22 offset:12288\n"
"v_cvt_pk_bf16_f32 v218, v92, v93\n"
"v_mfma_f32_16x16x32_bf16 v[124:127], a[172:175], v[140:143], v[236:239]\n"
"v_exp_f32 v94, v94\n"
"v_mfma_f32_16x16x32_bf16 v[124:127], a[168:171], v[108:111], v[124:127]\n"
"buffer_load_dwordx4 v17, s[36:39], s51 offen offset:1024 lds\n"
"v_mfma_f32_16x16x32_bf16 v[124:127], a[164:167], v[76:79], v[124:127]\n"
"v_exp_f32 v95, v95\n"
"v_mfma_f32_16x16x32_bf16 v[124:127], a[160:163], v[44:47], v[124:127]\n"
"ds_read_b64_tr_b16 a[200:201], v23 offset:0\n"
"v_cvt_pk_bf16_f32 v219, v94, v95\n"
"v_mfma_f32_16x16x32_bf16 v[156:159], a[176:179], v[44:47], v[236:239]\n"
"v_exp_f32 v88, v88\n"
"v_mfma_f32_16x16x32_bf16 v[156:159], a[180:183], v[76:79], v[156:159]\n"
"ds_read_b64_tr_b16 a[202:203], v23 offset:4096\n"
"v_add_u32 v22, s71, v22\n"
"v_mfma_f32_16x16x32_bf16 v[156:159], a[184:187], v[108:111], v[156:159]\n"
"s_waitcnt lgkmcnt(2)\n"
"v_exp_f32 v89, v89\n"
"v_mfma_f32_16x16x32_bf16 v[156:159], a[188:191], v[140:143], v[156:159]\n"
"ds_read_b64_tr_b16 a[204:205], v23 offset:8192\n"
"v_cvt_pk_bf16_f32 v210, v88, v89\n"
"v_mfma_f32_16x16x32_bf16 v[152:155], a[188:191], v[136:139], v[232:235]\n"
"v_exp_f32 v90, v90\n"
"v_mfma_f32_16x16x32_bf16 v[152:155], a[184:187], v[104:107], v[152:155]\n"
"ds_read_b64_tr_b16 a[206:207], v23 offset:12288\n"
"v_add_u32 v23, s71, v23\n"
"v_mfma_f32_16x16x32_bf16 v[152:155], a[180:183], v[72:75], v[152:155]\n"
"v_exp_f32 v91, v91\n"
"v_mfma_f32_16x16x32_bf16 v[152:155], a[176:179], v[40:43], v[152:155]\n"
"ds_read_b64_tr_b16 a[208:209], v24 offset:0\n"
"v_cvt_pk_bf16_f32 v211, v90, v91\n"
"v_mfma_f32_16x16x32_bf16 v[148:151], a[176:179], v[36:39], v[228:231]\n"
"v_exp_f32 v84, v84\n"
"v_mfma_f32_16x16x32_bf16 v[148:151], a[180:183], v[68:71], v[148:151]\n"
"buffer_load_dwordx4 v16, s[36:39], s51 offen offset:2048 lds\n"
"v_mfma_f32_16x16x32_bf16 v[148:151], a[184:187], v[100:103], v[148:151]\n"
"v_exp_f32 v85, v85\n"
"v_mfma_f32_16x16x32_bf16 v[148:151], a[188:191], v[132:135], v[148:151]\n"
"ds_read_b64_tr_b16 a[210:211], v24 offset:4096\n"
"v_cvt_pk_bf16_f32 v202, v84, v85\n"
"v_mfma_f32_16x16x32_bf16 v[144:147], a[188:191], v[128:131], v[224:227]\n"
"v_exp_f32 v86, v86\n"
"v_mfma_f32_16x16x32_bf16 v[144:147], a[184:187], v[96:99], v[144:147]\n"
"ds_read_b64_tr_b16 a[212:213], v24 offset:8192\n"
"v_add_u32 v21, s70, v21\n"
"v_mfma_f32_16x16x32_bf16 v[144:147], a[180:183], v[64:67], v[144:147]\n"
"v_exp_f32 v87, v87\n"
"v_mfma_f32_16x16x32_bf16 v[144:147], a[176:179], v[32:35], v[144:147]\n"
"ds_read_b64_tr_b16 a[214:215], v24 offset:12288\n"
"v_cvt_pk_bf16_f32 v203, v86, v87\n"
"v_mfma_f32_16x16x32_bf16 a[0:3], a[192:195], v[160:163], a[0:3]\n"
"v_exp_f32 v80, v80\n"
"v_mfma_f32_16x16x32_bf16 a[0:3], a[196:199], v[164:167], a[0:3]\n"
"buffer_load_dwordx4 v17, s[36:39], s51 offen offset:3072 lds\n"
"v_mfma_f32_16x16x32_bf16 a[4:7], a[196:199], v[172:175], a[4:7]\n"
"s_add_u32 s51, s51, 16384\n"
"v_exp_f32 v81, v81\n"
"v_mfma_f32_16x16x32_bf16 a[4:7], a[192:195], v[168:171], a[4:7]\n"
"ds_read_b64_tr_b16 a[216:217], v25 offset:0\n"
"v_cvt_pk_bf16_f32 v194, v80, v81\n"
"v_mfma_f32_16x16x32_bf16 a[8:11], a[192:195], v[176:179], a[8:11]\n"
"v_exp_f32 v82, v82\n"
"v_mfma_f32_16x16x32_bf16 a[8:11], a[196:199], v[180:183], a[8:11]\n"
"ds_read_b64_tr_b16 a[218:219], v25 offset:4096\n"
"v_add_u32 v24, s71, v24\n"
"v_mfma_f32_16x16x32_bf16 a[12:15], a[196:199], v[188:191], a[12:15]\n"
"v_exp_f32 v83, v83\n"
"v_mfma_f32_16x16x32_bf16 a[12:15], a[192:195], v[184:187], a[12:15]\n"
"s_waitcnt lgkmcnt(2)\n"
"ds_read_b64_tr_b16 a[220:221], v25 offset:8192\n"
"v_cvt_pk_bf16_f32 v195, v82, v83\n"
"v_mfma_f32_16x16x32_bf16 a[28:31], a[200:203], v[184:187], a[28:31]\n"
"v_exp_f32 v112, v112\n"
"v_mfma_f32_16x16x32_bf16 a[28:31], a[204:207], v[188:191], a[28:31]\n"
"ds_read_b64_tr_b16 a[222:223], v25 offset:12288\n"
"v_add_u32 v25, s71, v25\n"
"v_mfma_f32_16x16x32_bf16 a[24:27], a[204:207], v[180:183], a[24:27]\n"
"v_exp_f32 v113, v113\n"
"v_mfma_f32_16x16x32_bf16 a[24:27], a[200:203], v[176:179], a[24:27]\n"
"ds_read_b64_tr_b16 a[192:193], v26 offset:0\n"
"v_cvt_pk_bf16_f32 v196, v112, v113\n"
"v_mfma_f32_16x16x32_bf16 a[20:23], a[200:203], v[168:171], a[20:23]\n"
"v_exp_f32 v114, v114\n"
"v_mfma_f32_16x16x32_bf16 a[20:23], a[204:207], v[172:175], a[20:23]\n"
"buffer_store_dwordx4 v[8:11], v1, s[20:23], s66 offen sc1\n"
"v_mfma_f32_16x16x32_bf16 a[16:19], a[204:207], v[164:167], a[16:19]\n"
"v_exp_f32 v115, v115\n"
"v_mfma_f32_16x16x32_bf16 a[16:19], a[200:203], v[160:163], a[16:19]\n"
"ds_read_b64_tr_b16 a[194:195], v26 offset:4096\n"
"v_cvt_pk_bf16_f32 v197, v114, v115\n"
"v_mfma_f32_16x16x32_bf16 a[32:35], a[208:211], v[160:163], a[32:35]\n"
"v_exp_f32 v116, v116\n"
"v_mfma_f32_16x16x32_bf16 a[32:35], a[212:215], v[164:167], a[32:35]\n"
"ds_read_b64_tr_b16 a[196:197], v26 offset:8192\n"
"v_mfma_f32_16x16x32_bf16 a[36:39], a[212:215], v[172:175], a[36:39]\n"
"v_exp_f32 v117, v117\n"
"v_mfma_f32_16x16x32_bf16 a[36:39], a[208:211], v[168:171], a[36:39]\n"
"ds_read_b64_tr_b16 a[198:199], v26 offset:12288\n"
"v_cvt_pk_bf16_f32 v204, v116, v117\n"
"v_mfma_f32_16x16x32_bf16 a[40:43], a[208:211], v[176:179], a[40:43]\n"
"v_exp_f32 v118, v118\n"
"v_mfma_f32_16x16x32_bf16 a[40:43], a[212:215], v[180:183], a[40:43]\n"
"ds_read_b64_tr_b16 a[200:201], v27 offset:0\n"
"v_add_u32 v26, s71, v26\n"
"v_mfma_f32_16x16x32_bf16 a[44:47], a[212:215], v[188:191], a[44:47]\n"
"v_exp_f32 v119, v119\n"
"v_mfma_f32_16x16x32_bf16 a[44:47], a[208:211], v[184:187], a[44:47]\n"
"s_waitcnt lgkmcnt(2)\n"
"ds_read_b64_tr_b16 a[202:203], v27 offset:4096\n"
"v_cvt_pk_bf16_f32 v205, v118, v119\n"
"v_mfma_f32_16x16x32_bf16 a[60:63], a[216:219], v[184:187], a[60:63]\n"
"s_mov_b32 m0, s6\n"
"v_exp_f32 v120, v120\n"
"v_mfma_f32_16x16x32_bf16 a[60:63], a[220:223], v[188:191], a[60:63]\n"
"buffer_load_dwordx4 v1, s[28:31], s7 offen offset:2048 sc1 lds\n"
"v_mfma_f32_16x16x32_bf16 a[56:59], a[220:223], v[180:183], a[56:59]\n"
"v_exp_f32 v121, v121\n"
"v_mfma_f32_16x16x32_bf16 a[56:59], a[216:219], v[176:179], a[56:59]\n"
"ds_read_b64_tr_b16 a[204:205], v27 offset:8192\n"
"v_cvt_pk_bf16_f32 v212, v120, v121\n"
"v_mfma_f32_16x16x32_bf16 a[52:55], a[216:219], v[168:171], a[52:55]\n"
"v_exp_f32 v122, v122\n"
"v_mfma_f32_16x16x32_bf16 a[52:55], a[220:223], v[172:175], a[52:55]\n"
"ds_read_b64_tr_b16 a[206:207], v27 offset:12288\n"
"v_add_u32 v27, s71, v27\n"
"v_mfma_f32_16x16x32_bf16 a[48:51], a[220:223], v[164:167], a[48:51]\n"
"v_exp_f32 v123, v123\n"
"v_mfma_f32_16x16x32_bf16 a[48:51], a[216:219], v[160:163], a[48:51]\n"
"ds_read_b64_tr_b16 a[208:209], v28 offset:0\n"
"v_cvt_pk_bf16_f32 v213, v122, v123\n"
"v_mfma_f32_16x16x32_bf16 a[64:67], a[192:195], v[160:163], a[64:67]\n"
"s_waitcnt lgkmcnt(3)\n"
"v_exp_f32 v124, v124\n"
"v_mfma_f32_16x16x32_bf16 a[64:67], a[196:199], v[164:167], a[64:67]\n"
"ds_read_b64_tr_b16 a[210:211], v28 offset:4096\n"
"v_mfma_f32_16x16x32_bf16 a[68:71], a[196:199], v[172:175], a[68:71]\n"
"v_exp_f32 v125, v125\n"
"v_mfma_f32_16x16x32_bf16 a[68:71], a[192:195], v[168:171], a[68:71]\n"
"ds_read_b64_tr_b16 a[212:213], v28 offset:8192\n"
"v_cvt_pk_bf16_f32 v220, v124, v125\n"
"v_mfma_f32_16x16x32_bf16 a[72:75], a[192:195], v[176:179], a[72:75]\n"
"v_exp_f32 v126, v126\n"
"v_mfma_f32_16x16x32_bf16 a[72:75], a[196:199], v[180:183], a[72:75]\n"
"buffer_store_dwordx4 v[8:11], v1, s[20:23], s66 offen offset:1024 sc1\n"
"v_mfma_f32_16x16x32_bf16 a[76:79], a[196:199], v[188:191], a[76:79]\n"
"s_add_u32 s66, s66, 2048\n"
"v_exp_f32 v127, v127\n"
"v_mfma_f32_16x16x32_bf16 a[76:79], a[192:195], v[184:187], a[76:79]\n"
"ds_read_b64_tr_b16 a[214:215], v28 offset:12288\n"
"v_cvt_pk_bf16_f32 v221, v126, v127\n"
"v_mfma_f32_16x16x32_bf16 a[92:95], a[200:203], v[184:187], a[92:95]\n"
"s_waitcnt lgkmcnt(2)\n"
"v_exp_f32 v156, v156\n"
"v_mfma_f32_16x16x32_bf16 a[92:95], a[204:207], v[188:191], a[92:95]\n"
"ds_read_b64_tr_b16 a[216:217], v29 offset:0\n"
"v_add_u32 v28, s71, v28\n"
"v_mfma_f32_16x16x32_bf16 a[88:91], a[204:207], v[180:183], a[88:91]\n"
"v_exp_f32 v157, v157\n"
"v_mfma_f32_16x16x32_bf16 a[88:91], a[200:203], v[176:179], a[88:91]\n"
"ds_read_b64_tr_b16 a[218:219], v29 offset:4096\n"
"v_cvt_pk_bf16_f32 v222, v156, v157\n"
"v_mfma_f32_16x16x32_bf16 a[84:87], a[200:203], v[168:171], a[84:87]\n"
"v_exp_f32 v158, v158\n"
"v_mfma_f32_16x16x32_bf16 a[84:87], a[204:207], v[172:175], a[84:87]\n"
"ds_read_b64_tr_b16 a[220:221], v29 offset:8192\n"
"v_mfma_f32_16x16x32_bf16 a[80:83], a[204:207], v[164:167], a[80:83]\n"
"v_exp_f32 v159, v159\n"
"v_mfma_f32_16x16x32_bf16 a[80:83], a[200:203], v[160:163], a[80:83]\n"
"ds_read_b64_tr_b16 a[222:223], v29 offset:12288\n"
"v_cvt_pk_bf16_f32 v223, v158, v159\n"
"v_mfma_f32_16x16x32_bf16 a[96:99], a[208:211], v[160:163], a[96:99]\n"
"s_waitcnt lgkmcnt(3)\n"
"v_exp_f32 v152, v152\n"
"v_mfma_f32_16x16x32_bf16 a[96:99], a[212:215], v[164:167], a[96:99]\n"
"buffer_load_dwordx4 v1, s[28:31], s7 offen offset:3072 sc1 lds\n"
"v_mfma_f32_16x16x32_bf16 a[100:103], a[212:215], v[172:175], a[100:103]\n"
"s_add_u32 s7, s7, 16384\n"
"v_exp_f32 v153, v153\n"
"v_mfma_f32_16x16x32_bf16 a[100:103], a[208:211], v[168:171], a[100:103]\n"
"v_cvt_pk_bf16_f32 v214, v152, v153\n"
"v_add_u32 v29, s71, v29\n"
"v_mfma_f32_16x16x32_bf16 a[104:107], a[208:211], v[176:179], a[104:107]\n"
"s_add_u32 s6, s6, 16384\n"
"v_exp_f32 v154, v154\n"
"v_mfma_f32_16x16x32_bf16 a[104:107], a[212:215], v[180:183], a[104:107]\n"
"s_waitcnt vmcnt(15) lgkmcnt(0)\n"
"s_barrier\n"
"v_mfma_f32_16x16x32_bf16 a[108:111], a[212:215], v[188:191], a[108:111]\n"
"v_exp_f32 v155, v155\n"
"v_mfma_f32_16x16x32_bf16 a[108:111], a[208:211], v[184:187], a[108:111]\n"
"ds_read_b128 a[128:131], v18 offset:0\n"
"v_cvt_pk_bf16_f32 v215, v154, v155\n"
"v_mfma_f32_16x16x32_bf16 a[124:127], a[216:219], v[184:187], a[124:127]\n"
"v_exp_f32 v148, v148\n"
"v_mfma_f32_16x16x32_bf16 a[124:127], a[220:223], v[188:191], a[124:127]\n"
"ds_read_b128 a[132:135], v19 offset:0\n"
"v_mfma_f32_16x16x32_bf16 a[120:123], a[220:223], v[180:183], a[120:123]\n"
"v_exp_f32 v149, v149\n"
"v_mfma_f32_16x16x32_bf16 a[120:123], a[216:219], v[176:179], a[120:123]\n"
"ds_read_b128 a[136:139], v20 offset:0\n"
"v_cvt_pk_bf16_f32 v206, v148, v149\n"
"v_mfma_f32_16x16x32_bf16 a[116:119], a[216:219], v[168:171], a[116:119]\n"
"v_exp_f32 v150, v150\n"
"v_mfma_f32_16x16x32_bf16 a[116:119], a[220:223], v[172:175], a[116:119]\n"
"ds_read_b128 a[140:143], v21 offset:0\n"
"v_mfma_f32_16x16x32_bf16 a[112:115], a[220:223], v[164:167], a[112:115]\n"
"v_exp_f32 v151, v151\n"
"v_mfma_f32_16x16x32_bf16 a[112:115], a[216:219], v[160:163], a[112:115]\n"
"ds_read_b128 a[144:147], v18 offset:4096\n"
"v_cvt_pk_bf16_f32 v207, v150, v151\n"
"v_smfmac_f32_16x16x64_bf16 a[224:227], v[240:243], v[160:167], v244\n"
"s_lshr_b32 s10, s50, 15\n"
"ds_read_b128 a[148:151], v19 offset:4096\n"
"v_smfmac_f32_16x16x64_bf16 a[228:231], v[240:243], v[168:175], v244\n"
"s_bitcmp1_b32 0xdc00, s10\n"
"ds_read_b128 a[152:155], v20 offset:4096\n"
"v_smfmac_f32_16x16x64_bf16 a[232:235], v[240:243], v[176:183], v244\n"
"ds_read_b128 a[156:159], v21 offset:4096\n"
"v_smfmac_f32_16x16x64_bf16 a[236:239], v[240:243], v[184:191], v244\n"
"s_waitcnt lgkmcnt(4)\n"
"s_cbranch_scc1 .LATT_Y\n"
"s_cmp_lg_u32 s10, 16\n"
"s_cbranch_scc1 .LATT_HOOK\n"
"v_mfma_f32_16x16x32_bf16 v[48:51], a[128:131], v[32:35], v[224:227]\n"
"s_add_u32 m0, s48, 65536\n"
"v_exp_f32 v144, v144\n"
"v_mfma_f32_16x16x32_bf16 v[48:51], a[132:135], v[64:67], v[48:51]\n"
"s_mov_b32 s52, 507904\n"
"buffer_load_dwordx4 v16, s[36:39], s52 offen offset:0 lds\n"
"v_mfma_f32_16x16x32_bf16 v[48:51], a[136:139], v[96:99], v[48:51]\n"
"v_exp_f32 v145, v145\n"
"v_mfma_f32_16x16x32_bf16 v[48:51], a[140:143], v[128:131], v[48:51]\n"
"ds_read_b128 a[160:163], v18 offset:8192\n"
"v_cvt_pk_bf16_f32 v198, v144, v145\n"
"v_mfma_f32_16x16x32_bf16 v[52:55], a[140:143], v[132:135], v[228:231]\n"
"v_exp_f32 v146, v146\n"
"v_mfma_f32_16x16x32_bf16 v[52:55], a[136:139], v[100:103], v[52:55]\n"
"v_mfma_f32_16x16x32_bf16 v[52:55], a[132:135], v[68:71], v[52:55]\n"
"v_exp_f32 v147, v147\n"
"v_mfma_f32_16x16x32_bf16 v[52:55], a[128:131], v[36:39], v[52:55]\n"
"ds_read_b128 a[164:167], v19 offset:8192\n"
"v_cvt_pk_bf16_f32 v199, v146, v147\n"
"v_mfma_f32_16x16x32_bf16 v[56:59], a[128:131], v[40:43], v[232:235]\n"
"v_exp_f32 v48, v48\n"
"v_mfma_f32_16x16x32_bf16 v[56:59], a[132:135], v[72:75], v[56:59]\n"
"buffer_load_dwordx4 v17, s[36:39], s52 offen offset:1024 lds\n"
"v_mfma_f32_16x16x32_bf16 v[56:59], a[136:139], v[104:107], v[56:59]\n"
"v_exp_f32 v49, v49\n"
"v_mfma_f32_16x16x32_bf16 v[56:59], a[140:143], v[136:139], v[56:59]\n"
"ds_read_b128 a[168:171], v20 offset:8192\n"
"v_cvt_pk_bf16_f32 v160, v48, v49\n"
"v_mfma_f32_16x16x32_bf16 v[60:63], a[140:143], v[140:143], v[236:239]\n"
"v_exp_f32 v50, v50\n"
"v_mfma_f32_16x16x32_bf16 v[60:63], a[136:139], v[108:111], v[60:63]\n"
"v_mfma_f32_16x16x32_bf16 v[60:63], a[132:135], v[76:79], v[60:63]\n"
"v_exp_f32 v51, v51\n"
"v_mfma_f32_16x16x32_bf16 v[60:63], a[128:131], v[44:47], v[60:63]\n"
"s_waitcnt lgkmcnt(1)\n"
"ds_read_b128 a[172:175], v21 offset:8192\n"
"v_cvt_pk_bf16_f32 v161, v50, v51\n"
"v_mfma_f32_16x16x32_bf16 v[92:95], a[144:147], v[44:47], v[236:239]\n"
"v_exp_f32 v52, v52\n"
"v_mfma_f32_16x16x32_bf16 v[92:95], a[148:151], v[76:79], v[92:95]\n"
"buffer_load_dwordx4 v16, s[36:39], s52 offen offset:2048 lds\n"
"v_mfma_f32_16x16x32_bf16 v[92:95], a[152:155], v[108:111], v[92:95]\n"
"v_exp_f32 v53, v53\n"
"v_mfma_f32_16x16x32_bf16 v[92:95], a[156:159], v[140:143], v[92:95]\n"
"ds_read_b128 a[176:179], v18 offset:12288\n"
"v_cvt_pk_bf16_f32 v168, v52, v53\n"
"v_mfma_f32_16x16x32_bf16 v[88:91], a[156:159], v[136:139], v[232:235]\n"
"v_exp_f32 v54, v54\n"
"v_mfma_f32_16x16x32_bf16 v[88:91], a[152:155], v[104:107], v[88:91]\n"
"v_mfma_f32_16x16x32_bf16 v[88:91], a[148:151], v[72:75], v[88:91]\n"
"v_exp_f32 v55, v55\n"
"v_mfma_f32_16x16x32_bf16 v[88:91], a[144:147], v[40:43], v[88:91]\n"
"ds_read_b128 a[180:183], v19 offset:12288\n"
"v_cvt_pk_bf16_f32 v169, v54, v55\n"
"v_mfma_f32_16x16x32_bf16 v[84:87], a[144:147], v[36:39], v[228:231]\n"
"v_exp_f32 v56, v56\n"
"v_mfma_f32_16x16x32_bf16 v[84:87], a[148:151], v[68:71], v[84:87]\n"
"buffer_load_dwordx4 v17, s[36:39], s52 offen offset:3072 lds\n"
"v_mfma_f32_16x16x32_bf16 v[84:87], a[152:155], v[100:103], v[84:87]\n"
"v_exp_f32 v57, v57\n"
"v_mfma_f32_16x16x32_bf16 v[84:87], a[156:159], v[132:135], v[84:87]\n"
"ds_read_b128 a[184:187], v20 offset:12288\n"
"v_cvt_pk_bf16_f32 v176, v56, v57\n"
"v_mfma_f32_16x16x32_bf16 v[80:83], a[156:159], v[128:131], v[224:227]\n"
"v_exp_f32 v58, v58\n"
"v_mfma_f32_16x16x32_bf16 v[80:83], a[152:155], v[96:99], v[80:83]\n"
"v_mfma_f32_16x16x32_bf16 v[80:83], a[148:151], v[64:67], v[80:83]\n"
"v_exp_f32 v59, v59\n"
"v_mfma_f32_16x16x32_bf16 v[80:83], a[144:147], v[32:35], v[80:83]\n"
"ds_read_b128 a[188:191], v21 offset:12288\n"
"v_cvt_pk_bf16_f32 v177, v58, v59\n"
"v_mfma_f32_16x16x32_bf16 v[112:115], a[160:163], v[32:35], v[224:227]\n"
"v_exp_f32 v60, v60\n"
"v_mfma_f32_16x16x32_bf16 v[112:115], a[164:167], v[64:67], v[112:115]\n"
"s_waitcnt lgkmcnt(1)\n"
"v_mfma_f32_16x16x32_bf16 v[112:115], a[168:171], v[96:99], v[112:115]\n"
"v_exp_f32 v61, v61\n"
"v_mfma_f32_16x16x32_bf16 v[112:115], a[172:175], v[128:131], v[112:115]\n"
"ds_read_b64_tr_b16 a[192:193], v22 offset:0\n"
"v_cvt_pk_bf16_f32 v184, v60, v61\n"
"v_mfma_f32_16x16x32_bf16 v[116:119], a[172:175], v[132:135], v[228:231]\n"
"s_add_u32 m0, s48, 147456\n"
"v_exp_f32 v62, v62\n"
"v_mfma_f32_16x16x32_bf16 v[116:119], a[168:171], v[100:103], v[116:119]\n"
"s_add_u32 s52, s48, 57344\n"
"buffer_load_dwordx4 v1, s[28:31], s52 offen offset:0 sc1 lds\n"
"v_mfma_f32_16x16x32_bf16 v[116:119], a[164:167], v[68:71], v[116:119]\n"
"v_exp_f32 v63, v63\n"
"v_mfma_f32_16x16x32_bf16 v[116:119], a[160:163], v[36:39], v[116:119]\n"
"ds_read_b64_tr_b16 a[194:195], v22 offset:4096\n"
"v_cvt_pk_bf16_f32 v185, v62, v63\n"
"v_mfma_f32_16x16x32_bf16 v[120:123], a[160:163], v[40:43], v[232:235]\n"
"v_exp_f32 v92, v92\n"
"v_mfma_f32_16x16x32_bf16 v[120:123], a[164:167], v[72:75], v[120:123]\n"
"ds_read_b64_tr_b16 a[196:197], v22 offset:8192\n"
"v_mfma_f32_16x16x32_bf16 v[120:123], a[168:171], v[104:107], v[120:123]\n"
"v_exp_f32 v93, v93\n"
"v_mfma_f32_16x16x32_bf16 v[120:123], a[172:175], v[136:139], v[120:123]\n"
"ds_read_b64_tr_b16 a[198:199], v22 offset:12288\n"
"v_cvt_pk_bf16_f32 v186, v92, v93\n"
"v_mfma_f32_16x16x32_bf16 v[124:127], a[172:175], v[140:143], v[236:239]\n"
"v_exp_f32 v94, v94\n"
"v_mfma_f32_16x16x32_bf16 v[124:127], a[168:171], v[108:111], v[124:127]\n"
"buffer_load_dwordx4 v1, s[28:31], s52 offen offset:1024 sc1 lds\n"
"v_mfma_f32_16x16x32_bf16 v[124:127], a[164:167], v[76:79], v[124:127]\n"
"v_exp_f32 v95, v95\n"
"v_mfma_f32_16x16x32_bf16 v[124:127], a[160:163], v[44:47], v[124:127]\n"
"ds_read_b64_tr_b16 a[200:201], v23 offset:0\n"
"v_cvt_pk_bf16_f32 v187, v94, v95\n"
"v_mfma_f32_16x16x32_bf16 v[156:159], a[176:179], v[44:47], v[236:239]\n"
"v_exp_f32 v88, v88\n"
"v_mfma_f32_16x16x32_bf16 v[156:159], a[180:183], v[76:79], v[156:159]\n"
"ds_read_b64_tr_b16 a[202:203], v23 offset:4096\n"
"v_add_u32 v22, 0xffff8000, v22\n"
"v_mfma_f32_16x16x32_bf16 v[156:159], a[184:187], v[108:111], v[156:159]\n"
"s_waitcnt lgkmcnt(2)\n"
"v_exp_f32 v89, v89\n"
"v_mfma_f32_16x16x32_bf16 v[156:159], a[188:191], v[140:143], v[156:159]\n"
"ds_read_b64_tr_b16 a[204:205], v23 offset:8192\n"
"v_cvt_pk_bf16_f32 v178, v88, v89\n"
"v_mfma_f32_16x16x32_bf16 v[152:155], a[188:191], v[136:139], v[232:235]\n"
"v_exp_f32 v90, v90\n"
"v_mfma_f32_16x16x32_bf16 v[152:155], a[184:187], v[104:107], v[152:155]\n"
"ds_read_b64_tr_b16 a[206:207], v23 offset:12288\n"
"v_add_u32 v23, 0xffff8000, v23\n"
"v_mfma_f32_16x16x32_bf16 v[152:155], a[180:183], v[72:75], v[152:155]\n"
"v_exp_f32 v91, v91\n"
"v_mfma_f32_16x16x32_bf16 v[152:155], a[176:179], v[40:43], v[152:155]\n"
"ds_read_b64_tr_b16 a[208:209], v24 offset:0\n"
"v_cvt_pk_bf16_f32 v179, v90, v91\n"
"v_mfma_f32_16x16x32_bf16 v[148:151], a[176:179], v[36:39], v[228:231]\n"
"v_exp_f32 v84, v84\n"
"v_mfma_f32_16x16x32_bf16 v[148:151], a[180:183], v[68:71], v[148:151]\n"
"buffer_load_dwordx4 v1, s[28:31], s52 offen offset:2048 sc1 lds\n"
"v_mfma_f32_16x16x32_bf16 v[148:151], a[184:187], v[100:103], v[148:151]\n"
"v_exp_f32 v85, v85\n"
"v_mfma_f32_16x16x32_bf16 v[148:151], a[188:191], v[132:135], v[148:151]\n"
"ds_read_b64_tr_b16 a[210:211], v24 offset:4096\n"
"v_cvt_pk_bf16_f32 v170, v84, v85\n"
"v_mfma_f32_16x16x32_bf16 v[144:147], a[188:191], v[128:131], v[224:227]\n"
"v_exp_f32 v86, v86\n"
"v_mfma_f32_16x16x32_bf16 v[144:147], a[184:187], v[96:99], v[144:147]\n"
"ds_read_b64_tr_b16 a[212:213], v24 offset:8192\n"
"v_mfma_f32_16x16x32_bf16 v[144:147], a[180:183], v[64:67], v[144:147]\n"
"v_exp_f32 v87, v87\n"
"v_mfma_f32_16x16x32_bf16 v[144:147], a[176:179], v[32:35], v[144:147]\n"
"ds_read_b64_tr_b16 a[214:215], v24 offset:12288\n"
"v_cvt_pk_bf16_f32 v171, v86, v87\n"
"v_mfma_f32_16x16x32_bf16 a[0:3], a[192:195], v[192:195], a[0:3]\n"
"v_exp_f32 v80, v80\n"
"v_mfma_f32_16x16x32_bf16 a[0:3], a[196:199], v[196:199], a[0:3]\n"
"buffer_load_dwordx4 v1, s[28:31], s52 offen offset:3072 sc1 lds\n"
"v_mfma_f32_16x16x32_bf16 a[4:7], a[196:199], v[204:207], a[4:7]\n"
"v_exp_f32 v81, v81\n"
"v_mfma_f32_16x16x32_bf16 a[4:7], a[192:195], v[200:203], a[4:7]\n"
"ds_read_b64_tr_b16 a[216:217], v25 offset:0\n"
"v_cvt_pk_bf16_f32 v162, v80, v81\n"
"v_mfma_f32_16x16x32_bf16 a[8:11], a[192:195], v[208:211], a[8:11]\n"
"v_exp_f32 v82, v82\n"
"v_mfma_f32_16x16x32_bf16 a[8:11], a[196:199], v[212:215], a[8:11]\n"
"ds_read_b64_tr_b16 a[218:219], v25 offset:4096\n"
"v_add_u32 v24, 0xffff8000, v24\n"
"v_mfma_f32_16x16x32_bf16 a[12:15], a[196:199], v[220:223], a[12:15]\n"
"v_exp_f32 v83, v83\n"
"v_mfma_f32_16x16x32_bf16 a[12:15], a[192:195], v[216:219], a[12:15]\n"
"s_waitcnt lgkmcnt(2)\n"
"ds_read_b64_tr_b16 a[220:221], v25 offset:8192\n"
"v_cvt_pk_bf16_f32 v163, v82, v83\n"
"v_mfma_f32_16x16x32_bf16 a[28:31], a[200:203], v[216:219], a[28:31]\n"
"v_exp_f32 v112, v112\n"
"v_mfma_f32_16x16x32_bf16 a[28:31], a[204:207], v[220:223], a[28:31]\n"
"ds_read_b64_tr_b16 a[222:223], v25 offset:12288\n"
"v_add_u32 v25, 0xffff8000, v25\n"
"v_mfma_f32_16x16x32_bf16 a[24:27], a[204:207], v[212:215], a[24:27]\n"
"v_exp_f32 v113, v113\n"
"v_mfma_f32_16x16x32_bf16 a[24:27], a[200:203], v[208:211], a[24:27]\n"
"ds_read_b64_tr_b16 a[192:193], v26 offset:0\n"
"v_cvt_pk_bf16_f32 v164, v112, v113\n"
"v_mfma_f32_16x16x32_bf16 a[20:23], a[200:203], v[200:203], a[20:23]\n"
"s_add_u32 m0, s48, 32768\n"
"v_exp_f32 v114, v114\n"
"v_mfma_f32_16x16x32_bf16 a[20:23], a[204:207], v[204:207], a[20:23]\n"
"s_add_u32 s52, s48, 106496\n"
"buffer_load_dwordx4 v1, s[28:31], s52 offen offset:0 sc1 lds\n"
"v_mfma_f32_16x16x32_bf16 a[16:19], a[204:207], v[196:199], a[16:19]\n"
"v_exp_f32 v115, v115\n"
"v_mfma_f32_16x16x32_bf16 a[16:19], a[200:203], v[192:195], a[16:19]\n"
"ds_read_b64_tr_b16 a[194:195], v26 offset:4096\n"
"v_cvt_pk_bf16_f32 v165, v114, v115\n"
"v_mfma_f32_16x16x32_bf16 a[32:35], a[208:211], v[192:195], a[32:35]\n"
"v_exp_f32 v116, v116\n"
"v_mfma_f32_16x16x32_bf16 a[32:35], a[212:215], v[196:199], a[32:35]\n"
"ds_read_b64_tr_b16 a[196:197], v26 offset:8192\n"
"v_mfma_f32_16x16x32_bf16 a[36:39], a[212:215], v[204:207], a[36:39]\n"
"v_exp_f32 v117, v117\n"
"v_mfma_f32_16x16x32_bf16 a[36:39], a[208:211], v[200:203], a[36:39]\n"
"ds_read_b64_tr_b16 a[198:199], v26 offset:12288\n"
"v_cvt_pk_bf16_f32 v172, v116, v117\n"
"v_mfma_f32_16x16x32_bf16 a[40:43], a[208:211], v[208:211], a[40:43]\n"
"v_exp_f32 v118, v118\n"
"v_mfma_f32_16x16x32_bf16 a[40:43], a[212:215], v[212:215], a[40:43]\n"
"ds_read_b64_tr_b16 a[200:201], v27 offset:0\n"
"v_add_u32 v26, 0xffff8000, v26\n"
"v_mfma_f32_16x16x32_bf16 a[44:47], a[212:215], v[220:223], a[44:47]\n"
"v_exp_f32 v119, v119\n"
"v_mfma_f32_16x16x32_bf16 a[44:47], a[208:211], v[216:219], a[44:47]\n"
"s_waitcnt lgkmcnt(2)\n"
"ds_read_b64_tr_b16 a[202:203], v27 offset:4096\n"
"v_cvt_pk_bf16_f32 v173, v118, v119\n"
"v_mfma_f32_16x16x32_bf16 a[60:63], a[216:219], v[216:219], a[60:63]\n"
".LPAD10:\n"
"s_cbranch_execz .LPAD13\n"
"v_exp_f32 v120, v120\n"
"v_mfma_f32_16x16x32_bf16 a[60:63], a[220:223], v[220:223], a[60:63]\n"
"buffer_load_dwordx4 v1, s[28:31], s52 offen offset:1024 sc1 lds\n"
"v_mfma_f32_16x16x32_bf16 a[56:59], a[220:223], v[212:215], a[56:59]\n"
"v_exp_f32 v121, v121\n"
"v_mfma_f32_16x16x32_bf16 a[56:59], a[216:219], v[208:211], a[56:59]\n"
"ds_read_b64_tr_b16 a[204:205], v27 offset:8192\n"
"v_cvt_pk_bf16_f32 v180, v120, v121\n"
"v_mfma_f32_16x16x32_bf16 a[52:55], a[216:219], v[200:203], a[52:55]\n"
"v_exp_f32 v122, v122\n"
"v_mfma_f32_16x16x32_bf16 a[52:55], a[220:223], v[204:207], a[52:55]\n"
"ds_read_b64_tr_b16 a[206:207], v27 offset:12288\n"
"v_add_u32 v27, 0xffff8000, v27\n"
"v_mfma_f32_16x16x32_bf16 a[48:51], a[220:223], v[196:199], a[48:51]\n"
"v_exp_f32 v123, v123\n"
"v_mfma_f32_16x16x32_bf16 a[48:51], a[216:219], v[192:195], a[48:51]\n"
"ds_read_b64_tr_b16 a[208:209], v28 offset:0\n"
"v_cvt_pk_bf16_f32 v181, v122, v123\n"
"v_mfma_f32_16x16x32_bf16 a[64:67], a[192:195], v[192:195], a[64:67]\n"
"s_waitcnt lgkmcnt(3)\n"
"v_exp_f32 v124, v124\n"
"v_mfma_f32_16x16x32_bf16 a[64:67], a[196:199], v[196:199], a[64:67]\n"
"ds_read_b64_tr_b16 a[210:211], v28 offset:4096\n"
"v_mfma_f32_16x16x32_bf16 a[68:71], a[196:199], v[204:207], a[68:71]\n"
"v_exp_f32 v125, v125\n"
"v_mfma_f32_16x16x32_bf16 a[68:71], a[192:195], v[200:203], a[68:71]\n"
"ds_read_b64_tr_b16 a[212:213], v28 offset:8192\n"
"v_cvt_pk_bf16_f32 v188, v124, v125\n"
"v_mfma_f32_16x16x32_bf16 a[72:75], a[192:195], v[208:211], a[72:75]\n"
"v_exp_f32 v126, v126\n"
"v_mfma_f32_16x16x32_bf16 a[72:75], a[196:199], v[212:215], a[72:75]\n"
"buffer_load_dwordx4 v1, s[28:31], s52 offen offset:2048 sc1 lds\n"
"v_mfma_f32_16x16x32_bf16 a[76:79], a[196:199], v[220:223], a[76:79]\n"
"v_exp_f32 v127, v127\n"
"v_mfma_f32_16x16x32_bf16 a[76:79], a[192:195], v[216:219], a[76:79]\n"
"ds_read_b64_tr_b16 a[214:215], v28 offset:12288\n"
"v_cvt_pk_bf16_f32 v189, v126, v127\n"
"v_mfma_f32_16x16x32_bf16 a[92:95], a[200:203], v[216:219], a[92:95]\n"
"s_waitcnt lgkmcnt(2)\n"
"v_exp_f32 v156, v156\n"
"v_mfma_f32_16x16x32_bf16 a[92:95], a[204:207], v[220:223], a[92:95]\n"
"ds_read_b64_tr_b16 a[216:217], v29 offset:0\n"
"v_add_u32 v28, 0xffff8000, v28\n"
"v_mfma_f32_16x16x32_bf16 a[88:91], a[204:207], v[212:215], a[88:91]\n"
"v_exp_f32 v157, v157\n"
"v_mfma_f32_16x16x32_bf16 a[88:91], a[200:203], v[208:211], a[88:91]\n"
"ds_read_b64_tr_b16 a[218:219], v29 offset:4096\n"
"v_cvt_pk_bf16_f32 v190, v156, v157\n"
"v_mfma_f32_16x16x32_bf16 a[84:87], a[200:203], v[200:203], a[84:87]\n"
"v_exp_f32 v158, v158\n"
"v_mfma_f32_16x16x32_bf16 a[84:87], a[204:207], v[204:207], a[84:87]\n"
"ds_read_b64_tr_b16 a[220:221], v29 offset:8192\n"
"v_mfma_f32_16x16x32_bf16 a[80:83], a[204:207], v[196:199], a[80:83]\n"
"v_exp_f32 v159, v159\n"
"v_mfma_f32_16x16x32_bf16 a[80:83], a[200:203], v[192:195], a[80:83]\n"
"ds_read_b64_tr_b16 a[222:223], v29 offset:12288\n"
"v_cvt_pk_bf16_f32 v191, v158, v159\n"
"v_mfma_f32_16x16x32_bf16 a[96:99], a[208:211], v[192:195], a[96:99]\n"
"s_waitcnt lgkmcnt(3)\n"
"v_exp_f32 v152, v152\n"
"v_mfma_f32_16x16x32_bf16 a[96:99], a[212:215], v[196:199], a[96:99]\n"
"buffer_load_dwordx4 v1, s[28:31], s52 offen offset:3072 sc1 lds\n"
"v_mfma_f32_16x16x32_bf16 a[100:103], a[212:215], v[204:207], a[100:103]\n"
"v_exp_f32 v153, v153\n"
"v_mfma_f32_16x16x32_bf16 a[100:103], a[208:211], v[200:203], a[100:103]\n"
"v_cvt_pk_bf16_f32 v182, v152, v153\n"
"v_add_u32 v29, 0xffff8000, v29\n"
"v_mfma_f32_16x16x32_bf16 a[104:107], a[208:211], v[208:211], a[104:107]\n"
"v_exp_f32 v154, v154\n"
"v_mfma_f32_16x16x32_bf16 a[104:107], a[212:215], v[212:215], a[104:107]\n"
"s_waitcnt vmcnt(16) lgkmcnt(0)\n"
"s_barrier\n"
"v_mfma_f32_16x16x32_bf16 a[108:111], a[212:215], v[220:223], a[108:111]\n"
"v_exp_f32 v155, v155\n"
"v_mfma_f32_16x16x32_bf16 a[108:111], a[208:211], v[216:219], a[108:111]\n"
"ds_read_b128 a[128:131], v18 offset:16384\n"
"v_cvt_pk_bf16_f32 v183, v154, v155\n"
"v_mfma_f32_16x16x32_bf16 a[124:127], a[216:219], v[216:219], a[124:127]\n"
"v_exp_f32 v148, v148\n"
"v_mfma_f32_16x16x32_bf16 a[124:127], a[220:223], v[220:223], a[124:127]\n"
"ds_read_b128 a[132:135], v19 offset:16384\n"
"v_mfma_f32_16x16x32_bf16 a[120:123], a[220:223], v[212:215], a[120:123]\n"
"v_exp_f32 v149, v149\n"
"v_mfma_f32_16x16x32_bf16 a[120:123], a[216:219], v[208:211], a[120:123]\n"
"ds_read_b128 a[136:139], v20 offset:16384\n"
"v_cvt_pk_bf16_f32 v174, v148, v149\n"
"v_mfma_f32_16x16x32_bf16 a[116:119], a[216:219], v[200:203], a[116:119]\n"
"v_exp_f32 v150, v150\n"
"v_mfma_f32_16x16x32_bf16 a[116:119], a[220:223], v[204:207], a[116:119]\n"
"ds_read_b128 a[140:143], v21 offset:16384\n"
"v_mfma_f32_16x16x32_bf16 a[112:115], a[220:223], v[196:199], a[112:115]\n"
"v_exp_f32 v151, v151\n"
"v_mfma_f32_16x16x32_bf16 a[112:115], a[216:219], v[192:195], a[112:115]\n"
"ds_read_b128 a[144:147], v18 offset:20480\n"
"v_cvt_pk_bf16_f32 v175, v150, v151\n"
"v_smfmac_f32_16x16x64_bf16 a[224:227], v[240:243], v[192:199], v244\n"
"ds_read_b128 a[148:151], v19 offset:20480\n"
"v_smfmac_f32_16x16x64_bf16 a[228:231], v[240:243], v[200:207], v244\n"
"ds_read_b128 a[152:155], v20 offset:20480\n"
"v_smfmac_f32_16x16x64_bf16 a[232:235], v[240:243], v[208:215], v244\n"
"ds_read_b128 a[156:159], v21 offset:20480\n"
"v_smfmac_f32_16x16x64_bf16 a[236:239], v[240:243], v[216:223], v244\n"
"s_waitcnt lgkmcnt(4)\n"
"v_mfma_f32_16x16x32_bf16 v[48:51], a[128:131], v[32:35], v[224:227]\n"
"s_add_u32 m0, s85, 90112\n"
"v_exp_f32 v144, v144\n"
"v_mfma_f32_16x16x32_bf16 v[48:51], a[132:135], v[64:67], v[48:51]\n"
"s_add_u32 s52, s85, 0\n"
"buffer_load_dwordx4 v1, s[28:31], s52 offen offset:0 sc1 lds\n"
"v_mfma_f32_16x16x32_bf16 v[48:51], a[136:139], v[96:99], v[48:51]\n"
"v_exp_f32 v145, v145\n"
"v_mfma_f32_16x16x32_bf16 v[48:51], a[140:143], v[128:131], v[48:51]\n"
"ds_read_b128 a[160:163], v18 offset:24576\n"
"v_cvt_pk_bf16_f32 v166, v144, v145\n"
"v_mfma_f32_16x16x32_bf16 v[52:55], a[140:143], v[132:135], v[228:231]\n"
"v_exp_f32 v146, v146\n"
"v_mfma_f32_16x16x32_bf16 v[52:55], a[136:139], v[100:103], v[52:55]\n"
"buffer_load_dwordx4 v1, s[28:31], s52 offen offset:1024 sc1 lds\n"
"v_mfma_f32_16x16x32_bf16 v[52:55], a[132:135], v[68:71], v[52:55]\n"
"v_exp_f32 v147, v147\n"
"v_mfma_f32_16x16x32_bf16 v[52:55], a[128:131], v[36:39], v[52:55]\n"
"ds_read_b128 a[164:167], v19 offset:24576\n"
"v_cvt_pk_bf16_f32 v167, v146, v147\n"
"v_mfma_f32_16x16x32_bf16 v[56:59], a[128:131], v[40:43], v[232:235]\n"
"s_add_u32 m0, s48, 0\n"
"v_exp_f32 v48, v48\n"
"v_mfma_f32_16x16x32_bf16 v[56:59], a[132:135], v[72:75], v[56:59]\n"
"s_add_u32 s52, s48, 73728\n"
"buffer_load_dwordx4 v1, s[28:31], s52 offen offset:0 sc1 lds\n"
"v_mfma_f32_16x16x32_bf16 v[56:59], a[136:139], v[104:107], v[56:59]\n"
"v_exp_f32 v49, v49\n"
"v_mfma_f32_16x16x32_bf16 v[56:59], a[140:143], v[136:139], v[56:59]\n"
"ds_read_b128 a[168:171], v20 offset:24576\n"
"v_cvt_pk_bf16_f32 v192, v48, v49\n"
"v_mfma_f32_16x16x32_bf16 v[60:63], a[140:143], v[140:143], v[236:239]\n"
"v_exp_f32 v50, v50\n"
"v_mfma_f32_16x16x32_bf16 v[60:63], a[136:139], v[108:111], v[60:63]\n"
"v_mfma_f32_16x16x32_bf16 v[60:63], a[132:135], v[76:79], v[60:63]\n"
"v_exp_f32 v51, v51\n"
"v_mfma_f32_16x16x32_bf16 v[60:63], a[128:131], v[44:47], v[60:63]\n"
"s_waitcnt lgkmcnt(1)\n"
"ds_read_b128 a[172:175], v21 offset:24576\n"
"v_cvt_pk_bf16_f32 v193, v50, v51\n"
"v_mfma_f32_16x16x32_bf16 v[92:95], a[144:147], v[44:47], v[236:239]\n"
"v_exp_f32 v52, v52\n"
"v_mfma_f32_16x16x32_bf16 v[92:95], a[148:151], v[76:79], v[92:95]\n"
"buffer_load_dwordx4 v1, s[28:31], s52 offen offset:1024 sc1 lds\n"
"v_mfma_f32_16x16x32_bf16 v[92:95], a[152:155], v[108:111], v[92:95]\n"
"v_exp_f32 v53, v53\n"
"v_mfma_f32_16x16x32_bf16 v[92:95], a[156:159], v[140:143], v[92:95]\n"
"ds_read_b128 a[176:179], v18 offset:28672\n"
"v_cvt_pk_bf16_f32 v200, v52, v53\n"
"v_mfma_f32_16x16x32_bf16 v[88:91], a[156:159], v[136:139], v[232:235]\n"
"v_exp_f32 v54, v54\n"
"v_mfma_f32_16x16x32_bf16 v[88:91], a[152:155], v[104:107], v[88:91]\n"
"buffer_load_dwordx4 v1, s[28:31], s52 offen offset:2048 sc1 lds\n"
"v_mfma_f32_16x16x32_bf16 v[88:91], a[148:151], v[72:75], v[88:91]\n"
"v_exp_f32 v55, v55\n"
"v_mfma_f32_16x16x32_bf16 v[88:91], a[144:147], v[40:43], v[88:91]\n"
"ds_read_b128 a[180:183], v19 offset:28672\n"
"v_cvt_pk_bf16_f32 v201, v54, v55\n"
"v_mfma_f32_16x16x32_bf16 v[84:87], a[144:147], v[36:39], v[228:231]\n"
"v_exp_f32 v56, v56\n"
"v_mfma_f32_16x16x32_bf16 v[84:87], a[148:151], v[68:71], v[84:87]\n"
"buffer_load_dwordx4 v1, s[28:31], s52 offen offset:3072 sc1 lds\n"
"v_mfma_f32_16x16x32_bf16 v[84:87], a[152:155], v[100:103], v[84:87]\n"
"v_exp_f32 v57, v57\n"
"v_mfma_f32_16x16x32_bf16 v[84:87], a[156:159], v[132:135], v[84:87]\n"
"ds_read_b128 a[184:187], v20 offset:28672\n"
"v_cvt_pk_bf16_f32 v208, v56, v57\n"
"v_mfma_f32_16x16x32_bf16 v[80:83], a[156:159], v[128:131], v[224:227]\n"
"v_exp_f32 v58, v58\n"
"v_mfma_f32_16x16x32_bf16 v[80:83], a[152:155], v[96:99], v[80:83]\n"
"v_mfma_f32_16x16x32_bf16 v[80:83], a[148:151], v[64:67], v[80:83]\n"
"v_exp_f32 v59, v59\n"
"v_mfma_f32_16x16x32_bf16 v[80:83], a[144:147], v[32:35], v[80:83]\n"
"ds_read_b128 a[188:191], v21 offset:28672\n"
"v_cvt_pk_bf16_f32 v209, v58, v59\n"
"v_mfma_f32_16x16x32_bf16 v[112:115], a[160:163], v[32:35], v[224:227]\n"
"v_exp_f32 v60, v60\n"
"v_mfma_f32_16x16x32_bf16 v[112:115], a[164:167], v[64:67], v[112:115]\n"
"s_waitcnt lgkmcnt(1)\n"
"v_mfma_f32_16x16x32_bf16 v[112:115], a[168:171], v[96:99], v[112:115]\n"
"v_exp_f32 v61, v61\n"
"v_mfma_f32_16x16x32_bf16 v[112:115], a[172:175], v[128:131], v[112:115]\n"
"ds_read_b64_tr_b16 a[192:193], v22 offset:0\n"
"v_cvt_pk_bf16_f32 v216, v60, v61\n"
"v_mfma_f32_16x16x32_bf16 v[116:119], a[172:175], v[132:135], v[228:231]\n"
"v_exp_f32 v62, v62\n"
"v_mfma_f32_16x16x32_bf16 v[116:119], a[168:171], v[100:103], v[116:119]\n"
"s_waitcnt vmcnt(14) lgkmcnt(2)\n"
"s_barrier\n"
"v_mfma_f32_16x16x32_bf16 v[116:119], a[164:167], v[68:71], v[116:119]\n"
"v_exp_f32 v63, v63\n"
"v_mfma_f32_16x16x32_bf16 v[116:119], a[160:163], v[36:39], v[116:119]\n"
"ds_read_b64_tr_b16 a[194:195], v22 offset:4096\n"
"v_cvt_pk_bf16_f32 v217, v62, v63\n"
"v_mfma_f32_16x16x32_bf16 v[120:123], a[160:163], v[40:43], v[232:235]\n"
"v_exp_f32 v92, v92\n"
"v_mfma_f32_16x16x32_bf16 v[120:123], a[164:167], v[72:75], v[120:123]\n"
"ds_read_b64_tr_b16 a[196:197], v22 offset:8192\n"
"v_mfma_f32_16x16x32_bf16 v[120:123], a[168:171], v[104:107], v[120:123]\n"
"v_exp_f32 v93, v93\n"
"v_mfma_f32_16x16x32_bf16 v[120:123], a[172:175], v[136:139], v[120:123]\n"
"ds_read_b64_tr_b16 a[198:199], v22 offset:12288\n"
"v_cvt_pk_bf16_f32 v218, v92, v93\n"
"v_mfma_f32_16x16x32_bf16 v[124:127], a[172:175], v[140:143], v[236:239]\n"
"v_exp_f32 v94, v94\n"
"v_mfma_f32_16x16x32_bf16 v[124:127], a[168:171], v[108:111], v[124:127]\n"
"s_mov_b32 s53, 0\n"
"buffer_load_dwordx4 a[128:131], v3, s[44:47], s53 offen offset:0 sc1\n"
"v_mfma_f32_16x16x32_bf16 v[124:127], a[164:167], v[76:79], v[124:127]\n"
"v_exp_f32 v95, v95\n"
"v_mfma_f32_16x16x32_bf16 v[124:127], a[160:163], v[44:47], v[124:127]\n"
"ds_read_b64_tr_b16 a[200:201], v23 offset:0\n"
"v_cvt_pk_bf16_f32 v219, v94, v95\n"
"v_mfma_f32_16x16x32_bf16 v[156:159], a[176:179], v[44:47], v[236:239]\n"
"v_exp_f32 v88, v88\n"
"v_mfma_f32_16x16x32_bf16 v[156:159], a[180:183], v[76:79], v[156:159]\n"
"ds_read_b64_tr_b16 a[202:203], v23 offset:4096\n"
"v_mfma_f32_16x16x32_bf16 v[156:159], a[184:187], v[108:111], v[156:159]\n"
"s_waitcnt lgkmcnt(2)\n"
"v_exp_f32 v89, v89\n"
"v_mfma_f32_16x16x32_bf16 v[156:159], a[188:191], v[140:143], v[156:159]\n"
"ds_read_b64_tr_b16 a[204:205], v23 offset:8192\n"
"v_cvt_pk_bf16_f32 v210, v88, v89\n"
"v_mfma_f32_16x16x32_bf16 v[152:155], a[188:191], v[136:139], v[232:235]\n"
"v_exp_f32 v90, v90\n"
"v_mfma_f32_16x16x32_bf16 v[152:155], a[184:187], v[104:107], v[152:155]\n"
"ds_read_b64_tr_b16 a[206:207], v23 offset:12288\n"
"v_mfma_f32_16x16x32_bf16 v[152:155], a[180:183], v[72:75], v[152:155]\n"
"v_exp_f32 v91, v91\n"
"v_mfma_f32_16x16x32_bf16 v[152:155], a[176:179], v[40:43], v[152:155]\n"
"ds_read_b64_tr_b16 a[208:209], v24 offset:0\n"
"v_cvt_pk_bf16_f32 v211, v90, v91\n"
"v_mfma_f32_16x16x32_bf16 v[148:151], a[176:179], v[36:39], v[228:231]\n"
"v_exp_f32 v84, v84\n"
"v_mfma_f32_16x16x32_bf16 v[148:151], a[180:183], v[68:71], v[148:151]\n"
"buffer_load_dwordx4 a[132:135], v3, s[44:47], s53 offen offset:1024 sc1\n"
"v_mfma_f32_16x16x32_bf16 v[148:151], a[184:187], v[100:103], v[148:151]\n"
"v_exp_f32 v85, v85\n"
"v_mfma_f32_16x16x32_bf16 v[148:151], a[188:191], v[132:135], v[148:151]\n"
"ds_read_b64_tr_b16 a[210:211], v24 offset:4096\n"
"v_cvt_pk_bf16_f32 v202, v84, v85\n"
"v_mfma_f32_16x16x32_bf16 v[144:147], a[188:191], v[128:131], v[224:227]\n"
"v_exp_f32 v86, v86\n"
"v_mfma_f32_16x16x32_bf16 v[144:147], a[184:187], v[96:99], v[144:147]\n"
"ds_read_b64_tr_b16 a[212:213], v24 offset:8192\n"
"v_mfma_f32_16x16x32_bf16 v[144:147], a[180:183], v[64:67], v[144:147]\n"
"v_exp_f32 v87, v87\n"
"v_mfma_f32_16x16x32_bf16 v[144:147], a[176:179], v[32:35], v[144:147]\n"
"ds_read_b64_tr_b16 a[214:215], v24 offset:12288\n"
"v_cvt_pk_bf16_f32 v203, v86, v87\n"
"v_mfma_f32_16x16x32_bf16 a[0:3], a[192:195], v[160:163], a[0:3]\n"
"v_exp_f32 v80, v80\n"
"v_mfma_f32_16x16x32_bf16 a[0:3], a[196:199], v[164:167], a[0:3]\n"
"buffer_load_dwordx4 a[136:139], v3, s[44:47], s53 offen offset:2048 sc1\n"
"v_mfma_f32_16x16x32_bf16 a[4:7], a[196:199], v[172:175], a[4:7]\n"
"v_exp_f32 v81, v81\n"
"v_mfma_f32_16x16x32_bf16 a[4:7], a[192:195], v[168:171], a[4:7]\n"
"ds_read_b64_tr_b16 a[216:217], v25 offset:0\n"
"v_cvt_pk_bf16_f32 v194, v80, v81\n"
"v_mfma_f32_16x16x32_bf16 a[8:11], a[192:195], v[176:179], a[8:11]\n"
"v_exp_f32 v82, v82\n"
"v_mfma_f32_16x16x32_bf16 a[8:11], a[196:199], v[180:183], a[8:11]\n"
"ds_read_b64_tr_b16 a[218:219], v25 offset:4096\n"
"v_mfma_f32_16x16x32_bf16 a[12:15], a[196:199], v[188:191], a[12:15]\n"
"v_exp_f32 v83, v83\n"
"v_mfma_f32_16x16x32_bf16 a[12:15], a[192:195], v[184:187], a[12:15]\n"
"s_waitcnt lgkmcnt(2)\n"
"ds_read_b64_tr_b16 a[220:221], v25 offset:8192\n"
"v_cvt_pk_bf16_f32 v195, v82, v83\n"
"v_mfma_f32_16x16x32_bf16 a[28:31], a[200:203], v[184:187], a[28:31]\n"
"v_exp_f32 v112, v112\n"
"v_mfma_f32_16x16x32_bf16 a[28:31], a[204:207], v[188:191], a[28:31]\n"
"ds_read_b64_tr_b16 a[222:223], v25 offset:12288\n"
"v_mfma_f32_16x16x32_bf16 a[24:27], a[204:207], v[180:183], a[24:27]\n"
"v_exp_f32 v113, v113\n"
"v_mfma_f32_16x16x32_bf16 a[24:27], a[200:203], v[176:179], a[24:27]\n"
"ds_read_b64_tr_b16 a[192:193], v26 offset:0\n"
"v_cvt_pk_bf16_f32 v196, v112, v113\n"
"v_mfma_f32_16x16x32_bf16 a[20:23], a[200:203], v[168:171], a[20:23]\n"
"v_exp_f32 v114, v114\n"
"v_mfma_f32_16x16x32_bf16 a[20:23], a[204:207], v[172:175], a[20:23]\n"
"buffer_load_dwordx4 a[140:143], v3, s[44:47], s53 offen offset:3072 sc1\n"
"v_mfma_f32_16x16x32_bf16 a[16:19], a[204:207], v[164:167], a[16:19]\n"
"v_exp_f32 v115, v115\n"
"v_mfma_f32_16x16x32_bf16 a[16:19], a[200:203], v[160:163], a[16:19]\n"
"ds_read_b64_tr_b16 a[194:195], v26 offset:4096\n"
"v_cvt_pk_bf16_f32 v197, v114, v115\n"
"v_mfma_f32_16x16x32_bf16 a[32:35], a[208:211], v[160:163], a[32:35]\n"
"v_exp_f32 v116, v116\n"
"v_mfma_f32_16x16x32_bf16 a[32:35], a[212:215], v[164:167], a[32:35]\n"
"ds_read_b64_tr_b16 a[196:197], v26 offset:8192\n"
"v_mfma_f32_16x16x32_bf16 a[36:39], a[212:215], v[172:175], a[36:39]\n"
"v_exp_f32 v117, v117\n"
"v_mfma_f32_16x16x32_bf16 a[36:39], a[208:211], v[168:171], a[36:39]\n"
"ds_read_b64_tr_b16 a[198:199], v26 offset:12288\n"
"v_cvt_pk_bf16_f32 v204, v116, v117\n"
"v_mfma_f32_16x16x32_bf16 a[40:43], a[208:211], v[176:179], a[40:43]\n"
"v_exp_f32 v118, v118\n"
"v_mfma_f32_16x16x32_bf16 a[40:43], a[212:215], v[180:183], a[40:43]\n"
"ds_read_b64_tr_b16 a[200:201], v27 offset:0\n"
"v_mfma_f32_16x16x32_bf16 a[44:47], a[212:215], v[188:191], a[44:47]\n"
"v_exp_f32 v119, v119\n"
"v_mfma_f32_16x16x32_bf16 a[44:47], a[208:211], v[184:187], a[44:47]\n"
"s_waitcnt lgkmcnt(2)\n"
"ds_read_b64_tr_b16 a[202:203], v27 offset:4096\n"
"v_cvt_pk_bf16_f32 v205, v118, v119\n"
"v_mfma_f32_16x16x32_bf16 a[60:63], a[216:219], v[184:187], a[60:63]\n"
"v_exp_f32 v120, v120\n"
"v_mfma_f32_16x16x32_bf16 a[60:63], a[220:223], v[188:191], a[60:63]\n"
"s_mov_b32 s53, 4096\n"
"buffer_load_dwordx4 a[144:147], v3, s[44:47], s53 offen offset:0 sc1\n"
"v_mfma_f32_16x16x32_bf16 a[56:59], a[220:223], v[180:183], a[56:59]\n"
"v_exp_f32 v121, v121\n"
"v_mfma_f32_16x16x32_bf16 a[56:59], a[216:219], v[176:179], a[56:59]\n"
"ds_read_b64_tr_b16 a[204:205], v27 offset:8192\n"
"v_cvt_pk_bf16_f32 v212, v120, v121\n"
"v_mfma_f32_16x16x32_bf16 a[52:55], a[216:219], v[168:171], a[52:55]\n"
"v_exp_f32 v122, v122\n"
"v_mfma_f32_16x16x32_bf16 a[52:55], a[220:223], v[172:175], a[52:55]\n"
"ds_read_b64_tr_b16 a[206:207], v27 offset:12288\n"
"v_mfma_f32_16x16x32_bf16 a[48:51], a[220:223], v[164:167], a[48:51]\n"
"v_exp_f32 v123, v123\n"
"v_mfma_f32_16x16x32_bf16 a[48:51], a[216:219], v[160:163], a[48:51]\n"
"ds_read_b64_tr_b16 a[208:209], v28 offset:0\n"
"v_cvt_pk_bf16_f32 v213, v122, v123\n"
"v_mfma_f32_16x16x32_bf16 a[64:67], a[192:195], v[160:163], a[64:67]\n"
"s_waitcnt lgkmcnt(3)\n"
"v_exp_f32 v124, v124\n"
"v_mfma_f32_16x16x32_bf16 a[64:67], a[196:199], v[164:167], a[64:67]\n"
"ds_read_b64_tr_b16 a[210:211], v28 offset:4096\n"
"v_mfma_f32_16x16x32_bf16 a[68:71], a[196:199], v[172:175], a[68:71]\n"
"v_exp_f32 v125, v125\n"
"v_mfma_f32_16x16x32_bf16 a[68:71], a[192:195], v[168:171], a[68:71]\n"
"ds_read_b64_tr_b16 a[212:213], v28 offset:8192\n"
"v_cvt_pk_bf16_f32 v220, v124, v125\n"
"v_mfma_f32_16x16x32_bf16 a[72:75], a[192:195], v[176:179], a[72:75]\n"
"v_exp_f32 v126, v126\n"
"v_mfma_f32_16x16x32_bf16 a[72:75], a[196:199], v[180:183], a[72:75]\n"
"buffer_load_dwordx4 a[148:151], v3, s[44:47], s53 offen offset:1024 sc1\n"
"v_mfma_f32_16x16x32_bf16 a[76:79], a[196:199], v[188:191], a[76:79]\n"
"v_exp_f32 v127, v127\n"
"v_mfma_f32_16x16x32_bf16 a[76:79], a[192:195], v[184:187], a[76:79]\n"
"ds_read_b64_tr_b16 a[214:215], v28 offset:12288\n"
"v_cvt_pk_bf16_f32 v221, v126, v127\n"
"v_mfma_f32_16x16x32_bf16 a[92:95], a[200:203], v[184:187], a[92:95]\n"
"s_waitcnt lgkmcnt(2)\n"
"v_exp_f32 v156, v156\n"
"v_mfma_f32_16x16x32_bf16 a[92:95], a[204:207], v[188:191], a[92:95]\n"
"ds_read_b64_tr_b16 a[216:217], v29 offset:0\n"
"v_mfma_f32_16x16x32_bf16 a[88:91], a[204:207], v[180:183], a[88:91]\n"
"v_exp_f32 v157, v157\n"
"v_mfma_f32_16x16x32_bf16 a[88:91], a[200:203], v[176:179], a[88:91]\n"
"ds_read_b64_tr_b16 a[218:219], v29 offset:4096\n"
"v_cvt_pk_bf16_f32 v222, v156, v157\n"
"v_mfma_f32_16x16x32_bf16 a[84:87], a[200:203], v[168:171], a[84:87]\n"
"v_exp_f32 v158, v158\n"
"v_mfma_f32_16x16x32_bf16 a[84:87], a[204:207], v[172:175], a[84:87]\n"
"ds_read_b64_tr_b16 a[220:221], v29 offset:8192\n"
"v_mfma_f32_16x16x32_bf16 a[80:83], a[204:207], v[164:167], a[80:83]\n"
"v_exp_f32 v159, v159\n"
"v_mfma_f32_16x16x32_bf16 a[80:83], a[200:203], v[160:163], a[80:83]\n"
"ds_read_b64_tr_b16 a[222:223], v29 offset:12288\n"
"v_cvt_pk_bf16_f32 v223, v158, v159\n"
"v_mfma_f32_16x16x32_bf16 a[96:99], a[208:211], v[160:163], a[96:99]\n"
"s_waitcnt lgkmcnt(3)\n"
"v_exp_f32 v152, v152\n"
"v_mfma_f32_16x16x32_bf16 a[96:99], a[212:215], v[164:167], a[96:99]\n"
"v_exp_f32 v144, v144\n"
"v_mfma_f32_16x16x32_bf16 a[100:103], a[212:215], v[172:175], a[100:103]\n"
"v_exp_f32 v153, v153\n"
"v_mfma_f32_16x16x32_bf16 a[100:103], a[208:211], v[168:171], a[100:103]\n"
"ds_read_b64_tr_b16 a[192:193], v22 offset:16384\n"
"v_cvt_pk_bf16_f32 v214, v152, v153\n"
"v_mfma_f32_16x16x32_bf16 a[104:107], a[208:211], v[176:179], a[104:107]\n"
"v_exp_f32 v154, v154\n"
"v_mfma_f32_16x16x32_bf16 a[104:107], a[212:215], v[180:183], a[104:107]\n"
"v_exp_f32 v145, v145\n"
"v_mfma_f32_16x16x32_bf16 a[108:111], a[212:215], v[188:191], a[108:111]\n"
"v_exp_f32 v155, v155\n"
"v_mfma_f32_16x16x32_bf16 a[108:111], a[208:211], v[184:187], a[108:111]\n"
"s_waitcnt lgkmcnt(1)\n"
"ds_read_b64_tr_b16 a[194:195], v22 offset:20480\n"
"v_cvt_pk_bf16_f32 v215, v154, v155\n"
"v_mfma_f32_16x16x32_bf16 a[124:127], a[216:219], v[184:187], a[124:127]\n"
"v_exp_f32 v148, v148\n"
"v_mfma_f32_16x16x32_bf16 a[124:127], a[220:223], v[188:191], a[124:127]\n"
"v_exp_f32 v146, v146\n"
"v_mfma_f32_16x16x32_bf16 a[120:123], a[220:223], v[180:183], a[120:123]\n"
"v_exp_f32 v149, v149\n"
"v_mfma_f32_16x16x32_bf16 a[120:123], a[216:219], v[176:179], a[120:123]\n"
"ds_read_b64_tr_b16 a[196:197], v22 offset:24576\n"
"v_cvt_pk_bf16_f32 v206, v148, v149\n"
"v_mfma_f32_16x16x32_bf16 a[116:119], a[216:219], v[168:171], a[116:119]\n"
"v_exp_f32 v150, v150\n"
"v_mfma_f32_16x16x32_bf16 a[116:119], a[220:223], v[172:175], a[116:119]\n"
"v_exp_f32 v147, v147\n"
"v_mfma_f32_16x16x32_bf16 a[112:115], a[220:223], v[164:167], a[112:115]\n"
"v_exp_f32 v151, v151\n"
"v_mfma_f32_16x16x32_bf16 a[112:115], a[216:219], v[160:163], a[112:115]\n"
"ds_read_b64_tr_b16 a[198:199], v22 offset:28672\n"
"v_cvt_pk_bf16_f32 v207, v150, v151\n"
"v_smfmac_f32_16x16x64_bf16 a[224:227], v[240:243], v[160:167], v244\n"
"v_cvt_pk_bf16_f32 v198, v144, v145\n"
"v_smfmac_f32_16x16x64_bf16 a[228:231], v[240:243], v[168:175], v244\n"
"v_cvt_pk_bf16_f32 v199, v146, v147\n"
"v_smfmac_f32_16x16x64_bf16 a[232:235], v[240:243], v[176:183], v244\n"
"ds_read_b64_tr_b16 a[200:201], v23 offset:16384\n"
"v_smfmac_f32_16x16x64_bf16 a[236:239], v[240:243], v[184:191], v244\n"
"ds_read_b64_tr_b16 a[202:203], v23 offset:20480\n"
"v_smfmac_f32_16x16x64_bf16 a[236:239], v[240:243], v[216:223], v244\n"
"ds_read_b64_tr_b16 a[204:205], v23 offset:24576\n"
"v_smfmac_f32_16x16x64_bf16 a[232:235], v[240:243], v[208:215], v244\n"
"ds_read_b64_tr_b16 a[206:207], v23 offset:28672\n"
"v_smfmac_f32_16x16x64_bf16 a[228:231], v[240:243], v[200:207], v244\n"
"buffer_load_dwordx4 a[152:155], v3, s[44:47], s53 offen offset:2048 sc1\n"
"v_smfmac_f32_16x16x64_bf16 a[224:227], v[240:243], v[192:199], v244\n"
"s_waitcnt lgkmcnt(4)\n"
"v_mfma_f32_16x16x32_bf16 a[0:3], a[192:195], v[192:195], a[0:3]\n"
"s_barrier\n"
"v_mfma_f32_16x16x32_bf16 a[0:3], a[196:199], v[196:199], a[0:3]\n"
"ds_read_b64_tr_b16 a[208:209], v24 offset:16384\n"
"v_mfma_f32_16x16x32_bf16 a[4:7], a[196:199], v[204:207], a[4:7]\n"
"buffer_load_dwordx4 a[156:159], v3, s[44:47], s53 offen offset:3072 sc1\n"
"v_mfma_f32_16x16x32_bf16 a[4:7], a[192:195], v[200:203], a[4:7]\n"
"s_add_u32 m0, s48, 16384\n"
"ds_read_b64_tr_b16 a[210:211], v24 offset:20480\n"
"v_mfma_f32_16x16x32_bf16 a[8:11], a[192:195], v[208:211], a[8:11]\n"
"s_add_u32 s52, s48, 90112\n"
"buffer_load_dwordx4 v1, s[28:31], s52 offen offset:0 sc1 lds\n"
"v_mfma_f32_16x16x32_bf16 a[8:11], a[196:199], v[212:215], a[8:11]\n"
"ds_read_b64_tr_b16 a[212:213], v24 offset:24576\n"
"v_mfma_f32_16x16x32_bf16 a[12:15], a[196:199], v[220:223], a[12:15]\n"
"buffer_load_dwordx4 v1, s[28:31], s52 offen offset:1024 sc1 lds\n"
"v_mfma_f32_16x16x32_bf16 a[12:15], a[192:195], v[216:219], a[12:15]\n"
"s_waitcnt lgkmcnt(2)\n"
"ds_read_b64_tr_b16 a[214:215], v24 offset:28672\n"
"v_accvgpr_read_b32 v245, a224\n"
"v_mfma_f32_16x16x32_bf16 a[28:31], a[200:203], v[216:219], a[28:31]\n"
"v_accvgpr_read_b32 v249, a225\n"
"v_accvgpr_read_b32 v246, a228\n"
"v_mfma_f32_16x16x32_bf16 a[28:31], a[204:207], v[220:223], a[28:31]\n"
"ds_read_b64_tr_b16 a[216:217], v25 offset:16384\n"
"v_accvgpr_read_b32 v250, a229\n"
"v_mfma_f32_16x16x32_bf16 a[24:27], a[204:207], v[212:215], a[24:27]\n"
"buffer_load_dwordx4 v1, s[28:31], s52 offen offset:2048 sc1 lds\n"
"v_mfma_f32_16x16x32_bf16 a[24:27], a[200:203], v[208:211], a[24:27]\n"
"ds_read_b64_tr_b16 a[218:219], v25 offset:20480\n"
"v_accvgpr_read_b32 v247, a232\n"
"v_mfma_f32_16x16x32_bf16 a[20:23], a[200:203], v[200:203], a[20:23]\n"
"v_accvgpr_read_b32 v251, a233\n"
"v_accvgpr_read_b32 v248, a236\n"
"v_mfma_f32_16x16x32_bf16 a[20:23], a[204:207], v[204:207], a[20:23]\n"
"ds_read_b64_tr_b16 a[220:221], v25 offset:24576\n"
"v_accvgpr_read_b32 v252, a237\n"
"v_mfma_f32_16x16x32_bf16 a[16:19], a[204:207], v[196:199], a[16:19]\n"
"buffer_load_dwordx4 v1, s[28:31], s52 offen offset:3072 sc1 lds\n"
"v_mfma_f32_16x16x32_bf16 a[16:19], a[200:203], v[192:195], a[16:19]\n"
"s_waitcnt lgkmcnt(2)\n"
"ds_read_b64_tr_b16 a[222:223], v25 offset:28672\n"
"v_add_f32 v245, v245, v249\n"
"v_mfma_f32_16x16x32_bf16 a[32:35], a[208:211], v[192:195], a[32:35]\n"
"v_add_f32 v246, v246, v250\n"
"v_add_f32 v247, v247, v251\n"
"v_mfma_f32_16x16x32_bf16 a[32:35], a[212:215], v[196:199], a[32:35]\n"
"ds_read_b64_tr_b16 a[192:193], v26 offset:16384\n"
"v_add_f32 v248, v248, v252\n"
"v_mfma_f32_16x16x32_bf16 a[36:39], a[212:215], v[204:207], a[36:39]\n"
"s_mov_b32 s53, 8192\n"
"buffer_load_dwordx4 a[160:163], v3, s[44:47], s53 offen offset:0 sc1\n"
"v_mfma_f32_16x16x32_bf16 a[36:39], a[208:211], v[200:203], a[36:39]\n"
"ds_read_b64_tr_b16 a[194:195], v26 offset:20480\n"
"v_add_f32 v249, v245, v246\n"
"v_mfma_f32_16x16x32_bf16 a[40:43], a[208:211], v[208:211], a[40:43]\n"
"v_add_f32 v250, v247, v248\n"
"v_add_f32 v249, v249, v250\n"
"v_mfma_f32_16x16x32_bf16 a[40:43], a[212:215], v[212:215], a[40:43]\n"
"ds_read_b64_tr_b16 a[196:197], v26 offset:24576\n"
"v_cmp_nge_f32 vcc, 0x53800000, v249\n"
"v_mfma_f32_16x16x32_bf16 a[44:47], a[212:215], v[220:223], a[44:47]\n"
"buffer_load_dwordx4 a[164:167], v3, s[44:47], s53 offen offset:1024 sc1\n"
"v_mfma_f32_16x16x32_bf16 a[44:47], a[208:211], v[216:219], a[44:47]\n"
"s_waitcnt lgkmcnt(2)\n"
"ds_read_b64_tr_b16 a[198:199], v26 offset:28672\n"
"v_mov_b32 v250, s83\n"
"v_mfma_f32_16x16x32_bf16 a[60:63], a[216:219], v[216:219], a[60:63]\n"
"v_lshlrev_b32 v250, 2, v250\n"
"v_add_u32 v250, 81920, v250\n"
"v_mfma_f32_16x16x32_bf16 a[60:63], a[220:223], v[220:223], a[60:63]\n"
"s_or_b32 s55, vcc_lo, vcc_hi\n"
"ds_read_b64_tr_b16 a[200:201], v27 offset:16384\n"
"v_mov_b32 v249, s55\n"
"v_mfma_f32_16x16x32_bf16 a[56:59], a[220:223], v[212:215], a[56:59]\n"
"buffer_load_dwordx4 a[168:171], v3, s[44:47], s53 offen offset:2048 sc1\n"
"v_mfma_f32_16x16x32_bf16 a[56:59], a[216:219], v[208:211], a[56:59]\n"
"ds_read_b64_tr_b16 a[202:203], v27 offset:20480\n"
"v_mfma_f32_16x16x32_bf16 a[52:55], a[216:219], v[200:203], a[52:55]\n"
"ds_write_b32 v250, v249\n"
"v_mfma_f32_16x16x32_bf16 a[52:55], a[220:223], v[204:207], a[52:55]\n"
"ds_read_b64_tr_b16 a[204:205], v27 offset:24576\n"
"v_mov_b32 v31, 81920\n"
"v_mfma_f32_16x16x32_bf16 a[48:51], a[220:223], v[196:199], a[48:51]\n"
"buffer_load_dwordx4 a[172:175], v3, s[44:47], s53 offen offset:3072 sc1\n"
"v_mfma_f32_16x16x32_bf16 a[48:51], a[216:219], v[192:195], a[48:51]\n"
"s_waitcnt lgkmcnt(3)\n"
"ds_read_b64_tr_b16 a[206:207], v27 offset:28672\n"
"v_mfma_f32_16x16x32_bf16 a[64:67], a[192:195], v[192:195], a[64:67]\n"
"v_rcp_f32 v245, v245\n"
"v_mfma_f32_16x16x32_bf16 a[64:67], a[196:199], v[196:199], a[64:67]\n"
"ds_read_b64_tr_b16 a[208:209], v28 offset:16384\n"
"v_mfma_f32_16x16x32_bf16 a[68:71], a[196:199], v[204:207], a[68:71]\n"
"s_mov_b32 s53, 12288\n"
"buffer_load_dwordx4 a[176:179], v3, s[44:47], s53 offen offset:0 sc1\n"
"v_mfma_f32_16x16x32_bf16 a[68:71], a[192:195], v[200:203], a[68:71]\n"
"ds_read_b64_tr_b16 a[210:211], v28 offset:20480\n"
"v_mfma_f32_16x16x32_bf16 a[72:75], a[192:195], v[208:211], a[72:75]\n"
"v_rcp_f32 v246, v246\n"
"v_mfma_f32_16x16x32_bf16 a[72:75], a[196:199], v[212:215], a[72:75]\n"
"ds_read_b64_tr_b16 a[212:213], v28 offset:24576\n"
"v_mfma_f32_16x16x32_bf16 a[76:79], a[196:199], v[220:223], a[76:79]\n"
"buffer_load_dwordx4 a[180:183], v3, s[44:47], s53 offen offset:1024 sc1\n"
"v_mfma_f32_16x16x32_bf16 a[76:79], a[192:195], v[216:219], a[76:79]\n"
"s_waitcnt lgkmcnt(2)\n"
"ds_read_b64_tr_b16 a[214:215], v28 offset:28672\n"
"v_mfma_f32_16x16x32_bf16 a[92:95], a[200:203], v[216:219], a[92:95]\n"
"v_rcp_f32 v247, v247\n"
"v_mfma_f32_16x16x32_bf16 a[92:95], a[204:207], v[220:223], a[92:95]\n"
"ds_read_b64_tr_b16 a[216:217], v29 offset:16384\n"
"v_mfma_f32_16x16x32_bf16 a[88:91], a[204:207], v[212:215], a[88:91]\n"
"buffer_load_dwordx4 a[184:187], v3, s[44:47], s53 offen offset:2048 sc1\n"
"v_mfma_f32_16x16x32_bf16 a[88:91], a[200:203], v[208:211], a[88:91]\n"
"ds_read_b64_tr_b16 a[218:219], v29 offset:20480\n"
"v_mfma_f32_16x16x32_bf16 a[84:87], a[200:203], v[200:203], a[84:87]\n"
"v_rcp_f32 v248, v248\n"
"v_mfma_f32_16x16x32_bf16 a[84:87], a[204:207], v[204:207], a[84:87]\n"
"ds_read_b64_tr_b16 a[220:221], v29 offset:24576\n"
"v_accvgpr_read_b32 v8, a0\n"
"v_mfma_f32_16x16x32_bf16 a[80:83], a[204:207], v[196:199], a[80:83]\n"
"buffer_load_dwordx4 a[188:191], v3, s[44:47], s53 offen offset:3072 sc1\n"
"v_mfma_f32_16x16x32_bf16 a[80:83], a[200:203], v[192:195], a[80:83]\n"
"s_waitcnt lgkmcnt(2)\n"
"ds_read_b64_tr_b16 a[222:223], v29 offset:28672\n"
"v_accvgpr_read_b32 v12, a16\n"
"v_mfma_f32_16x16x32_bf16 a[96:99], a[208:211], v[192:195], a[96:99]\n"
"s_waitcnt vmcnt(24) lgkmcnt(2)\n"
"s_barrier\n"
"v_mfma_f32_16x16x32_bf16 a[96:99], a[212:215], v[196:199], a[96:99]\n"
"s_add_u32 m0, s48, 49152\n"
"ds_read_b128 v[160:163], v2 offset:0\n"
"v_accvgpr_read_b32 v9, a1\n"
"v_mfma_f32_16x16x32_bf16 a[100:103], a[212:215], v[204:207], a[100:103]\n"
"s_add_u32 s52, s48, 122880\n"
"buffer_load_dwordx4 v1, s[28:31], s52 offen offset:0 sc1 lds\n"
"v_mfma_f32_16x16x32_bf16 a[100:103], a[208:211], v[200:203], a[100:103]\n"
"ds_read_b128 v[164:167], v2 offset:9216\n"
"v_accvgpr_read_b32 v13, a17\n"
"v_mfma_f32_16x16x32_bf16 a[104:107], a[208:211], v[208:211], a[104:107]\n"
"ds_read_b128 v[252:255], v31\n"
"v_accvgpr_read_b32 v10, a2\n"
"v_mfma_f32_16x16x32_bf16 a[104:107], a[212:215], v[212:215], a[104:107]\n"
"ds_read_b128 v[168:171], v2 offset:18432\n"
"v_accvgpr_read_b32 v14, a18\n"
"v_mfma_f32_16x16x32_bf16 a[108:111], a[212:215], v[220:223], a[108:111]\n"
"buffer_load_dwordx4 v1, s[28:31], s52 offen offset:1024 sc1 lds\n"
"v_mfma_f32_16x16x32_bf16 a[108:111], a[208:211], v[216:219], a[108:111]\n"
"ds_read_b128 v[172:175], v2 offset:27648\n"
"v_accvgpr_read_b32 v11, a3\n"
"v_mfma_f32_16x16x32_bf16 a[124:127], a[216:219], v[216:219], a[124:127]\n"
"s_waitcnt lgkmcnt(4)\n"
"v_accvgpr_read_b32 v15, a19\n"
"v_mul_f32 v8, v245, v8\n"
"v_mfma_f32_16x16x32_bf16 a[124:127], a[220:223], v[220:223], a[124:127]\n"
"s_waitcnt lgkmcnt(2)\n"
"ds_read_b128 v[176:179], v2 offset:1024\n"
"v_or_b32 v252, v252, v253\n"
"v_mfma_f32_16x16x32_bf16 a[120:123], a[220:223], v[212:215], a[120:123]\n"
".LPAD11:\n"
"s_cbranch_execz .LPAD14\n"
"buffer_load_dwordx4 v1, s[28:31], s52 offen offset:2048 sc1 lds\n"
"v_mfma_f32_16x16x32_bf16 a[120:123], a[216:219], v[208:211], a[120:123]\n"
"ds_read_b128 v[180:183], v2 offset:10240\n"
"v_or3_b32 v252, v252, v254, v255\n"
"v_mfma_f32_16x16x32_bf16 a[116:119], a[216:219], v[200:203], a[116:119]\n"
"v_readfirstlane_b32 s54, v252\n"
"v_mul_f32 v9, v245, v9\n"
"v_mfma_f32_16x16x32_bf16 a[116:119], a[220:223], v[204:207], a[116:119]\n"
"ds_read_b128 v[184:187], v2 offset:19456\n"
"v_mul_f32 v10, v245, v10\n"
"v_mfma_f32_16x16x32_bf16 a[112:115], a[220:223], v[196:199], a[112:115]\n"
"buffer_load_dwordx4 v1, s[28:31], s52 offen offset:3072 sc1 lds\n"
"v_mfma_f32_16x16x32_bf16 a[112:115], a[216:219], v[192:195], a[112:115]\n"
"s_waitcnt vmcnt(16)\n"
"s_andn2_b32 s54, s54, s60\n"
"s_cbranch_scc1 .LATT_SLOW\n"
"ds_read_b128 v[188:191], v2 offset:28672\n"
"v_mfma_f32_16x16x32_bf16 v[32:35], a[128:131], v[160:163], 0\n"
"s_add_u32 s55, s57, 0\n"
"v_mul_f32 v11, v245, v11\n"
"v_mul_f32 v12, v245, v12\n"
"v_mfma_f32_16x16x32_bf16 v[32:35], a[136:139], v[164:167], v[32:35]\n"
"s_waitcnt lgkmcnt(2)\n"
"s_lshl_b32 s54, s59, 2\n"
"ds_read_b128 v[192:195], v2 offset:2048\n"
"v_mul_f32 v13, v245, v13\n"
"v_mfma_f32_16x16x32_bf16 v[32:35], a[144:147], v[168:171], v[32:35]\n"
"s_add_u32 s54, s54, s58\n"
"v_mul_f32 v14, v245, v14\n"
"v_mul_f32 v15, v245, v15\n"
"v_mfma_f32_16x16x32_bf16 v[32:35], a[152:155], v[172:175], v[32:35]\n"
"s_lshl_b32 s54, s54, 11\n"
"ds_read_b128 v[196:199], v2 offset:11264\n"
"v_cvt_pk_bf16_f32 v112, v8, v9\n"
"v_mfma_f32_16x16x32_bf16 v[36:39], a[156:159], v[172:175], 0\n"
"s_add_u32 s51, s54, 65536\n"
"v_cvt_pk_bf16_f32 v113, v10, v11\n"
"v_cvt_pk_bf16_f32 v114, v12, v13\n"
"v_mfma_f32_16x16x32_bf16 v[36:39], a[148:151], v[168:171], v[36:39]\n"
"s_lshl_b32 s54, s59, 5\n"
"ds_read_b128 v[200:203], v2 offset:20480\n"
"v_cvt_pk_bf16_f32 v115, v14, v15\n"
"v_mfma_f32_16x16x32_bf16 v[36:39], a[140:143], v[164:167], v[36:39]\n"
"s_and_b32 s8, s63, 7\n"
"buffer_store_dwordx4 v[112:115], v1, s[40:43], s55 offen offset:0 sc1\n"
"v_mfma_f32_16x16x32_bf16 v[36:39], a[132:135], v[160:163], v[36:39]\n"
"ds_read_b128 v[204:207], v2 offset:29696\n"
"v_accvgpr_read_b32 v16, a4\n"
"v_mfma_f32_16x16x32_bf16 v[44:47], a[132:135], v[176:179], 0\n"
"s_add_u32 s54, s54, s8\n"
"v_accvgpr_read_b32 v20, a20\n"
"v_accvgpr_read_b32 v17, a5\n"
"v_mfma_f32_16x16x32_bf16 v[44:47], a[140:143], v[180:183], v[44:47]\n"
"s_waitcnt lgkmcnt(2)\n"
"s_lshl_b32 s54, s54, 8\n"
"ds_read_b128 v[160:163], v2 offset:3072\n"
"v_accvgpr_read_b32 v21, a21\n"
"v_mfma_f32_16x16x32_bf16 v[44:47], a[148:151], v[184:187], v[44:47]\n"
"s_add_u32 s8, s54, 131072\n"
"v_accvgpr_read_b32 v18, a6\n"
"v_accvgpr_read_b32 v22, a22\n"
"v_mfma_f32_16x16x32_bf16 v[44:47], a[156:159], v[188:191], v[44:47]\n"
"s_mul_i32 s54, s56, 36864\n"
"ds_read_b128 v[164:167], v2 offset:12288\n"
"v_accvgpr_read_b32 v19, a7\n"
"v_mfma_f32_16x16x32_bf16 v[40:43], a[152:155], v[188:191], 0\n"
"s_add_u32 s32, s64, s54\n"
"v_accvgpr_read_b32 v23, a23\n"
"v_mul_f32 v16, v246, v16\n"
"v_mfma_f32_16x16x32_bf16 v[40:43], a[144:147], v[184:187], v[40:43]\n"
"s_addc_u32 s33, s65, 0\n"
"ds_read_b128 v[168:171], v2 offset:21504\n"
"v_mul_f32 v17, v246, v17\n"
"v_mfma_f32_16x16x32_bf16 v[40:43], a[136:139], v[180:183], v[40:43]\n"
"s_mov_b32 s34, 36864\n"
"v_mul_f32 v18, v246, v18\n"
"v_mul_f32 v19, v246, v19\n"
"v_mfma_f32_16x16x32_bf16 v[40:43], a[128:131], v[176:179], v[40:43]\n"
"ds_read_b128 v[172:175], v2 offset:30720\n"
"v_mul_f32 v20, v246, v20\n"
"v_mfma_f32_16x16x32_bf16 v[48:51], a[128:131], v[192:195], 0\n"
"s_mov_b32 s35, 0x20000\n"
"v_mul_f32 v21, v246, v21\n"
"v_mul_f32 v22, v246, v22\n"
"v_mfma_f32_16x16x32_bf16 v[48:51], a[136:139], v[196:199], v[48:51]\n"
"s_waitcnt lgkmcnt(2)\n"
"s_add_u32 s36, s78, s54\n"
"ds_read_b128 v[176:179], v2 offset:4096\n"
"v_mul_f32 v23, v246, v23\n"
"v_mfma_f32_16x16x32_bf16 v[48:51], a[144:147], v[200:203], v[48:51]\n"
"s_addc_u32 s37, s79, 0\n"
"v_cvt_pk_bf16_f32 v116, v16, v17\n"
"v_cvt_pk_bf16_f32 v117, v18, v19\n"
"v_mfma_f32_16x16x32_bf16 v[48:51], a[152:155], v[204:207], v[48:51]\n"
"s_mov_b32 s38, 36864\n"
"ds_read_b128 v[180:183], v2 offset:13312\n"
"v_cvt_pk_bf16_f32 v118, v20, v21\n"
"v_mfma_f32_16x16x32_bf16 v[52:55], a[156:159], v[204:207], 0\n"
"s_mov_b32 s39, 0x20000\n"
"v_cvt_pk_bf16_f32 v119, v22, v23\n"
"v_accvgpr_read_b32 v8, a8\n"
"v_mfma_f32_16x16x32_bf16 v[52:55], a[148:151], v[200:203], v[52:55]\n"
"s_lshl_b32 s50, s61, 8\n"
"ds_read_b128 v[184:187], v2 offset:22528\n"
"v_accvgpr_read_b32 v12, a24\n"
"v_mfma_f32_16x16x32_bf16 v[52:55], a[140:143], v[196:199], v[52:55]\n"
"s_add_u32 s49, s50, 18432\n"
"buffer_store_dwordx4 v[116:119], v1, s[40:43], s55 offen offset:1024 sc1\n"
"v_mfma_f32_16x16x32_bf16 v[52:55], a[132:135], v[192:195], v[52:55]\n"
"ds_read_b128 v[188:191], v2 offset:31744\n"
"v_accvgpr_read_b32 v9, a9\n"
"v_mfma_f32_16x16x32_bf16 v[60:63], a[132:135], v[160:163], 0\n"
"s_add_u32 s55, s57, 557056\n"
"v_accvgpr_read_b32 v13, a25\n"
"v_accvgpr_read_b32 v10, a10\n"
"v_mfma_f32_16x16x32_bf16 v[60:63], a[140:143], v[164:167], v[60:63]\n"
"s_waitcnt lgkmcnt(2)\n"
"s_lshl_b32 s82, s61, 5\n"
"ds_read_b128 v[192:195], v2 offset:5120\n"
"v_accvgpr_read_b32 v14, a26\n"
"v_mfma_f32_16x16x32_bf16 v[60:63], a[148:151], v[168:171], v[60:63]\n"
"s_add_u32 s82, s82, 1024\n"
"v_accvgpr_read_b32 v11, a11\n"
"v_accvgpr_read_b32 v15, a27\n"
"v_mfma_f32_16x16x32_bf16 v[60:63], a[156:159], v[172:175], v[60:63]\n"
"s_mov_b32 s22, 179712\n"
"ds_read_b128 v[196:199], v2 offset:14336\n"
"v_mul_f32 v8, v247, v8\n"
"v_mfma_f32_16x16x32_bf16 v[56:59], a[152:155], v[172:175], 0\n"
"s_xor_b32 s67, s58, 1\n"
"v_mul_f32 v9, v247, v9\n"
"v_mul_f32 v10, v247, v10\n"
"v_mfma_f32_16x16x32_bf16 v[56:59], a[144:147], v[168:171], v[56:59]\n"
"s_mul_i32 s67, s67, 55296\n"
"ds_read_b128 v[200:203], v2 offset:23552\n"
"v_mul_f32 v11, v247, v11\n"
"v_mfma_f32_16x16x32_bf16 v[56:59], a[136:139], v[164:167], v[56:59]\n"
"s_add_u32 s67, s67, 0\n"
"v_mul_f32 v12, v247, v12\n"
"v_mul_f32 v13, v247, v13\n"
"v_mfma_f32_16x16x32_bf16 v[56:59], a[128:131], v[160:163], v[56:59]\n"
"ds_read_b128 v[204:207], v2 offset:32768\n"
"v_mul_f32 v14, v247, v14\n"
"v_mfma_f32_16x16x32_bf16 v[64:67], a[128:131], v[176:179], 0\n"
"s_xor_b32 s68, s58, 2\n"
"v_mul_f32 v15, v247, v15\n"
"v_cvt_pk_bf16_f32 v120, v8, v9\n"
"v_mfma_f32_16x16x32_bf16 v[64:67], a[136:139], v[180:183], v[64:67]\n"
"s_waitcnt lgkmcnt(2)\n"
"s_mul_i32 s68, s68, 55296\n"
"ds_read_b128 v[160:163], v2 offset:6144\n"
"v_cvt_pk_bf16_f32 v121, v10, v11\n"
"v_mfma_f32_16x16x32_bf16 v[64:67], a[144:147], v[184:187], v[64:67]\n"
"s_add_u32 s68, s68, 2304\n"
"v_cvt_pk_bf16_f32 v122, v12, v13\n"
"v_cvt_pk_bf16_f32 v123, v14, v15\n"
"v_mfma_f32_16x16x32_bf16 v[64:67], a[152:155], v[188:191], v[64:67]\n"
"s_xor_b32 s69, s58, 3\n"
"ds_read_b128 v[164:167], v2 offset:15360\n"
"v_accvgpr_read_b32 v16, a12\n"
"v_mfma_f32_16x16x32_bf16 v[68:71], a[156:159], v[188:191], 0\n"
"s_mul_i32 s69, s69, 55296\n"
"buffer_store_dwordx4 v[120:123], v1, s[40:43], s55 offen offset:0 sc1\n"
"v_mfma_f32_16x16x32_bf16 v[68:71], a[148:151], v[184:187], v[68:71]\n"
"s_add_u32 s69, s69, 4608\n"
"ds_read_b128 v[168:171], v2 offset:24576\n"
"v_accvgpr_read_b32 v20, a28\n"
"v_mfma_f32_16x16x32_bf16 v[68:71], a[140:143], v[180:183], v[68:71]\n"
"s_lshl2_add_u32 s6, s59, s61\n"
"v_accvgpr_read_b32 v17, a13\n"
"v_accvgpr_read_b32 v21, a29\n"
"v_mfma_f32_16x16x32_bf16 v[68:71], a[132:135], v[176:179], v[68:71]\n"
"ds_read_b128 v[172:175], v2 offset:33792\n"
"v_accvgpr_read_b32 v18, a14\n"
"v_mfma_f32_16x16x32_bf16 v[76:79], a[132:135], v[192:195], 0\n"
"s_lshl2_add_u32 s6, s6, s83\n"
"v_accvgpr_read_b32 v22, a30\n"
"v_accvgpr_read_b32 v19, a15\n"
"v_mfma_f32_16x16x32_bf16 v[76:79], a[140:143], v[196:199], v[76:79]\n"
"s_waitcnt lgkmcnt(2)\n"
"s_lshl_b32 s6, s6, 7\n"
"ds_read_b128 v[176:179], v2 offset:7168\n"
"v_accvgpr_read_b32 v23, a31\n"
"v_mfma_f32_16x16x32_bf16 v[76:79], a[148:151], v[200:203], v[76:79]\n"
"v_mul_f32 v16, v248, v16\n"
"v_mul_f32 v17, v248, v17\n"
"v_mfma_f32_16x16x32_bf16 v[76:79], a[156:159], v[204:207], v[76:79]\n"
"ds_read_b128 v[180:183], v2 offset:16384\n"
"v_mul_f32 v18, v248, v18\n"
"v_mfma_f32_16x16x32_bf16 v[72:75], a[152:155], v[204:207], 0\n"
"v_mul_f32 v19, v248, v19\n"
"v_mul_f32 v20, v248, v20\n"
"v_mfma_f32_16x16x32_bf16 v[72:75], a[144:147], v[200:203], v[72:75]\n"
"ds_read_b128 v[184:187], v2 offset:25600\n"
"v_mul_f32 v21, v248, v21\n"
"v_mfma_f32_16x16x32_bf16 v[72:75], a[136:139], v[196:199], v[72:75]\n"
"v_mul_f32 v22, v248, v22\n"
"v_mul_f32 v23, v248, v23\n"
"v_mfma_f32_16x16x32_bf16 v[72:75], a[128:131], v[192:195], v[72:75]\n"
"ds_read_b128 v[188:191], v2 offset:34816\n"
"v_cvt_pk_bf16_f32 v124, v16, v17\n"
"v_mfma_f32_16x16x32_bf16 v[80:83], a[128:131], v[160:163], 0\n"
"v_cvt_pk_bf16_f32 v125, v18, v19\n"
"v_cvt_pk_bf16_f32 v126, v20, v21\n"
"v_mfma_f32_16x16x32_bf16 v[80:83], a[136:139], v[164:167], v[80:83]\n"
"s_waitcnt lgkmcnt(2)\n"
"ds_read_b128 v[192:195], v2 offset:8192\n"
"v_cvt_pk_bf16_f32 v127, v22, v23\n"
"v_mfma_f32_16x16x32_bf16 v[80:83], a[144:147], v[168:171], v[80:83]\n"
"buffer_store_dwordx4 v[124:127], v1, s[40:43], s55 offen offset:1024 sc1\n"
"v_mfma_f32_16x16x32_bf16 v[80:83], a[152:155], v[172:175], v[80:83]\n"
"s_add_u32 s55, s57, 2048\n"
"ds_read_b128 v[196:199], v2 offset:17408\n"
"v_accvgpr_read_b32 v8, a32\n"
"v_mfma_f32_16x16x32_bf16 v[84:87], a[156:159], v[172:175], 0\n"
"v_accvgpr_read_b32 v12, a48\n"
"v_accvgpr_read_b32 v9, a33\n"
"v_mfma_f32_16x16x32_bf16 v[84:87], a[148:151], v[168:171], v[84:87]\n"
"ds_read_b128 v[200:203], v2 offset:26624\n"
"v_accvgpr_read_b32 v13, a49\n"
"v_mfma_f32_16x16x32_bf16 v[84:87], a[140:143], v[164:167], v[84:87]\n"
"v_accvgpr_read_b32 v10, a34\n"
"v_accvgpr_read_b32 v14, a50\n"
"v_mfma_f32_16x16x32_bf16 v[84:87], a[132:135], v[160:163], v[84:87]\n"
"ds_read_b128 v[204:207], v2 offset:35840\n"
"v_add_u32 v2, 0x9000, v2\n"
"v_mfma_f32_16x16x32_bf16 v[92:95], a[132:135], v[176:179], 0\n"
"s_waitcnt vmcnt(38)\n"
"s_barrier\n"
"s_cmp_lg_u32 s83, 0\n"
"s_cbranch_scc1 .LSIG_G\n"
"s_mov_b64 exec, 1\n"
"v_mov_b32 v31, 1\n"
"buffer_atomic_add v31, off, s[24:27], s8 sc1\n"
"s_mov_b64 exec, -1\n"
".LSIG_G:\n"
"v_mfma_f32_16x16x32_bf16 v[92:95], a[140:143], v[180:183], v[92:95]\n"
"s_waitcnt lgkmcnt(2)\n"
"ds_read_b128 v[160:163], v2 offset:0\n"
"v_accvgpr_read_b32 v11, a35\n"
"v_mfma_f32_16x16x32_bf16 v[92:95], a[148:151], v[184:187], v[92:95]\n"
"v_accvgpr_read_b32 v15, a51\n"
"v_mul_f32 v8, v245, v8\n"
"v_mfma_f32_16x16x32_bf16 v[92:95], a[156:159], v[188:191], v[92:95]\n"
"ds_read_b128 v[164:167], v2 offset:9216\n"
"v_mul_f32 v9, v245, v9\n"
"v_mfma_f32_16x16x32_bf16 v[88:91], a[152:155], v[188:191], 0\n"
"v_mul_f32 v10, v245, v10\n"
"v_mul_f32 v11, v245, v11\n"
"v_mfma_f32_16x16x32_bf16 v[88:91], a[144:147], v[184:187], v[88:91]\n"
"ds_read_b128 v[168:171], v2 offset:18432\n"
"v_mul_f32 v12, v245, v12\n"
"v_mfma_f32_16x16x32_bf16 v[88:91], a[136:139], v[180:183], v[88:91]\n"
"v_mul_f32 v13, v245, v13\n"
"v_mul_f32 v14, v245, v14\n"
"v_mfma_f32_16x16x32_bf16 v[88:91], a[128:131], v[176:179], v[88:91]\n"
"ds_read_b128 v[172:175], v2 offset:27648\n"
"v_mul_f32 v15, v245, v15\n"
"v_mfma_f32_16x16x32_bf16 v[96:99], a[128:131], v[192:195], 0\n"
"v_cvt_pk_bf16_f32 v144, v8, v9\n"
"v_cvt_pk_bf16_f32 v145, v10, v11\n"
"v_mfma_f32_16x16x32_bf16 v[96:99], a[136:139], v[196:199], v[96:99]\n"
"s_waitcnt lgkmcnt(2)\n"
"ds_read_b128 v[176:179], v2 offset:1024\n"
"v_cvt_pk_bf16_f32 v146, v12, v13\n"
"v_mfma_f32_16x16x32_bf16 v[96:99], a[144:147], v[200:203], v[96:99]\n"
"v_cvt_pk_bf16_f32 v147, v14, v15\n"
"v_accvgpr_read_b32 v16, a36\n"
"v_mfma_f32_16x16x32_bf16 v[96:99], a[152:155], v[204:207], v[96:99]\n"
"ds_read_b128 v[180:183], v2 offset:10240\n"
"v_accvgpr_read_b32 v20, a52\n"
"v_mfma_f32_16x16x32_bf16 v[100:103], a[156:159], v[204:207], 0\n"
"buffer_store_dwordx4 v[144:147], v1, s[40:43], s55 offen offset:0 sc1\n"
"v_mfma_f32_16x16x32_bf16 v[100:103], a[148:151], v[200:203], v[100:103]\n"
"ds_read_b128 v[184:187], v2 offset:19456\n"
"v_accvgpr_read_b32 v17, a37\n"
"v_mfma_f32_16x16x32_bf16 v[100:103], a[140:143], v[196:199], v[100:103]\n"
"v_accvgpr_read_b32 v21, a53\n"
"v_accvgpr_read_b32 v18, a38\n"
"v_mfma_f32_16x16x32_bf16 v[100:103], a[132:135], v[192:195], v[100:103]\n"
"s_waitcnt vmcnt(9)\n"
"ds_read_b128 v[188:191], v2 offset:28672\n"
"v_accvgpr_read_b32 v22, a54\n"
"v_mfma_f32_16x16x32_bf16 v[32:35], a[160:163], v[160:163], v[32:35]\n"
"v_lshrrev_b32 v29, 9, v3\n"
"v_mul_u32_u24 v29, 504, v29\n"
"v_mfma_f32_16x16x32_bf16 v[32:35], a[168:171], v[164:167], v[32:35]\n"
"s_waitcnt lgkmcnt(2)\n"
"ds_read_b128 v[192:195], v2 offset:2048\n"
"v_sub_u32 v29, v3, v29\n"
"v_mfma_f32_16x16x32_bf16 v[32:35], a[176:179], v[168:171], v[32:35]\n"
"s_mov_b32 s53, 16384\n"
"buffer_load_dwordx4 a[192:195], v3, s[44:47], s53 offen offset:0 sc1\n"
"v_mfma_f32_16x16x32_bf16 v[32:35], a[184:187], v[172:175], v[32:35]\n"
"ds_read_b128 v[196:199], v2 offset:11264\n"
"v_accvgpr_read_b32 v19, a39\n"
"v_mfma_f32_16x16x32_bf16 v[36:39], a[188:191], v[172:175], v[36:39]\n"
"v_accvgpr_read_b32 v23, a55\n"
"v_mul_f32 v16, v246, v16\n"
"v_mfma_f32_16x16x32_bf16 v[36:39], a[180:183], v[168:171], v[36:39]\n"
"ds_read_b128 v[200:203], v2 offset:20480\n"
"v_mul_f32 v17, v246, v17\n"
"v_mfma_f32_16x16x32_bf16 v[36:39], a[172:175], v[164:167], v[36:39]\n"
"buffer_load_dwordx4 a[196:199], v3, s[44:47], s53 offen offset:1024 sc1\n"
"v_mfma_f32_16x16x32_bf16 v[36:39], a[164:167], v[160:163], v[36:39]\n"
"ds_read_b128 v[204:207], v2 offset:29696\n"
"v_mul_f32 v18, v246, v18\n"
"v_mfma_f32_16x16x32_bf16 v[44:47], a[164:167], v[176:179], v[44:47]\n"
"v_mul_f32 v19, v246, v19\n"
"v_mul_f32 v20, v246, v20\n"
"v_mfma_f32_16x16x32_bf16 v[44:47], a[172:175], v[180:183], v[44:47]\n"
"s_waitcnt lgkmcnt(2)\n"
"ds_read_b128 v[160:163], v2 offset:3072\n"
"v_mul_f32 v21, v246, v21\n"
"v_mfma_f32_16x16x32_bf16 v[44:47], a[180:183], v[184:187], v[44:47]\n"
"buffer_load_dwordx4 a[200:203], v3, s[44:47], s53 offen offset:2048 sc1\n"
"v_mfma_f32_16x16x32_bf16 v[44:47], a[188:191], v[188:191], v[44:47]\n"
"ds_read_b128 v[164:167], v2 offset:12288\n"
"v_mul_f32 v22, v246, v22\n"
"v_mfma_f32_16x16x32_bf16 v[40:43], a[184:187], v[188:191], v[40:43]\n"
"v_mul_f32 v23, v246, v23\n"
"v_cvt_pk_bf16_f32 v148, v16, v17\n"
"v_mfma_f32_16x16x32_bf16 v[40:43], a[176:179], v[184:187], v[40:43]\n"
"ds_read_b128 v[168:171], v2 offset:21504\n"
"v_cvt_pk_bf16_f32 v149, v18, v19\n"
"v_mfma_f32_16x16x32_bf16 v[40:43], a[168:171], v[180:183], v[40:43]\n"
"buffer_load_dwordx4 a[204:207], v3, s[44:47], s53 offen offset:3072 sc1\n"
"v_mfma_f32_16x16x32_bf16 v[40:43], a[160:163], v[176:179], v[40:43]\n"
"ds_read_b128 v[172:175], v2 offset:30720\n"
"v_cvt_pk_bf16_f32 v150, v20, v21\n"
"v_mfma_f32_16x16x32_bf16 v[48:51], a[160:163], v[192:195], v[48:51]\n"
"v_cvt_pk_bf16_f32 v151, v22, v23\n"
"v_accvgpr_read_b32 v8, a40\n"
"v_mfma_f32_16x16x32_bf16 v[48:51], a[168:171], v[196:199], v[48:51]\n"
"s_waitcnt lgkmcnt(2)\n"
"ds_read_b128 v[176:179], v2 offset:4096\n"
"v_accvgpr_read_b32 v12, a56\n"
"v_mfma_f32_16x16x32_bf16 v[48:51], a[176:179], v[200:203], v[48:51]\n"
"s_mov_b32 s53, 20480\n"
"buffer_load_dwordx4 a[208:211], v3, s[44:47], s53 offen offset:0 sc1\n"
"v_mfma_f32_16x16x32_bf16 v[48:51], a[184:187], v[204:207], v[48:51]\n"
"ds_read_b128 v[180:183], v2 offset:13312\n"
"v_accvgpr_read_b32 v9, a41\n"
"v_mfma_f32_16x16x32_bf16 v[52:55], a[188:191], v[204:207], v[52:55]\n"
"buffer_store_dwordx4 v[148:151], v1, s[40:43], s55 offen offset:1024 sc1\n"
"v_mfma_f32_16x16x32_bf16 v[52:55], a[180:183], v[200:203], v[52:55]\n"
"s_add_u32 s55, s57, 559104\n"
"ds_read_b128 v[184:187], v2 offset:22528\n"
"v_accvgpr_read_b32 v13, a57\n"
"v_mfma_f32_16x16x32_bf16 v[52:55], a[172:175], v[196:199], v[52:55]\n"
"buffer_load_dwordx4 a[212:215], v3, s[44:47], s53 offen offset:1024 sc1\n"
"v_mfma_f32_16x16x32_bf16 v[52:55], a[164:167], v[192:195], v[52:55]\n"
"ds_read_b128 v[188:191], v2 offset:31744\n"
"v_accvgpr_read_b32 v10, a42\n"
"v_mfma_f32_16x16x32_bf16 v[60:63], a[164:167], v[160:163], v[60:63]\n"
"v_accvgpr_read_b32 v14, a58\n"
"v_accvgpr_read_b32 v11, a43\n"
"v_mfma_f32_16x16x32_bf16 v[60:63], a[172:175], v[164:167], v[60:63]\n"
"s_waitcnt lgkmcnt(2)\n"
"ds_read_b128 v[192:195], v2 offset:5120\n"
"v_accvgpr_read_b32 v15, a59\n"
"v_mfma_f32_16x16x32_bf16 v[60:63], a[180:183], v[168:171], v[60:63]\n"
"buffer_load_dwordx4 a[216:219], v3, s[44:47], s53 offen offset:2048 sc1\n"
"v_mfma_f32_16x16x32_bf16 v[60:63], a[188:191], v[172:175], v[60:63]\n"
"ds_read_b128 v[196:199], v2 offset:14336\n"
"v_mul_f32 v8, v247, v8\n"
"v_mfma_f32_16x16x32_bf16 v[56:59], a[184:187], v[172:175], v[56:59]\n"
"v_mul_f32 v9, v247, v9\n"
"v_mul_f32 v10, v247, v10\n"
"v_mfma_f32_16x16x32_bf16 v[56:59], a[176:179], v[168:171], v[56:59]\n"
"ds_read_b128 v[200:203], v2 offset:23552\n"
"v_mul_f32 v11, v247, v11\n"
"v_mfma_f32_16x16x32_bf16 v[56:59], a[168:171], v[164:167], v[56:59]\n"
"buffer_load_dwordx4 a[220:223], v3, s[44:47], s53 offen offset:3072 sc1\n"
"v_mfma_f32_16x16x32_bf16 v[56:59], a[160:163], v[160:163], v[56:59]\n"
"ds_read_b128 v[204:207], v2 offset:32768\n"
"v_mul_f32 v12, v247, v12\n"
"v_mfma_f32_16x16x32_bf16 v[64:67], a[160:163], v[176:179], v[64:67]\n"
"v_mul_f32 v13, v247, v13\n"
"v_mul_f32 v14, v247, v14\n"
"v_mfma_f32_16x16x32_bf16 v[64:67], a[168:171], v[180:183], v[64:67]\n"
"s_waitcnt lgkmcnt(2)\n"
"s_add_u32 m0, s85, 65536\n"
"ds_read_b128 v[160:163], v2 offset:6144\n"
"v_mul_f32 v15, v247, v15\n"
"v_mfma_f32_16x16x32_bf16 v[64:67], a[176:179], v[184:187], v[64:67]\n"
"s_add_u32 s52, s85, 139264\n"
"buffer_load_dwordx4 v1, s[28:31], s52 offen offset:0 sc1 lds\n"
"v_mfma_f32_16x16x32_bf16 v[64:67], a[184:187], v[188:191], v[64:67]\n"
"ds_read_b128 v[164:167], v2 offset:15360\n"
"v_cvt_pk_bf16_f32 v152, v8, v9\n"
"v_mfma_f32_16x16x32_bf16 v[68:71], a[188:191], v[188:191], v[68:71]\n"
"v_cvt_pk_bf16_f32 v153, v10, v11\n"
"v_cvt_pk_bf16_f32 v154, v12, v13\n"
"v_mfma_f32_16x16x32_bf16 v[68:71], a[180:183], v[184:187], v[68:71]\n"
"ds_read_b128 v[168:171], v2 offset:24576\n"
"v_cvt_pk_bf16_f32 v155, v14, v15\n"
"v_mfma_f32_16x16x32_bf16 v[68:71], a[172:175], v[180:183], v[68:71]\n"
"buffer_load_dwordx4 v1, s[28:31], s52 offen offset:1024 sc1 lds\n"
"v_mfma_f32_16x16x32_bf16 v[68:71], a[164:167], v[176:179], v[68:71]\n"
"ds_read_b128 v[172:175], v2 offset:33792\n"
"v_accvgpr_read_b32 v16, a44\n"
"v_mfma_f32_16x16x32_bf16 v[76:79], a[164:167], v[192:195], v[76:79]\n"
"buffer_store_dwordx4 v[152:155], v1, s[40:43], s55 offen offset:0 sc1\n"
"v_mfma_f32_16x16x32_bf16 v[76:79], a[172:175], v[196:199], v[76:79]\n"
"s_waitcnt lgkmcnt(2)\n"
"ds_read_b128 v[176:179], v2 offset:7168\n"
"v_accvgpr_read_b32 v20, a60\n"
"v_mfma_f32_16x16x32_bf16 v[76:79], a[180:183], v[200:203], v[76:79]\n"
"v_accvgpr_read_b32 v17, a45\n"
"v_accvgpr_read_b32 v21, a61\n"
"v_mfma_f32_16x16x32_bf16 v[76:79], a[188:191], v[204:207], v[76:79]\n"
"ds_read_b128 v[180:183], v2 offset:16384\n"
"v_accvgpr_read_b32 v18, a46\n"
"v_mfma_f32_16x16x32_bf16 v[72:75], a[184:187], v[204:207], v[72:75]\n"
"v_accvgpr_read_b32 v22, a62\n"
"v_accvgpr_read_b32 v19, a47\n"
"v_mfma_f32_16x16x32_bf16 v[72:75], a[176:179], v[200:203], v[72:75]\n"
"ds_read_b128 v[184:187], v2 offset:25600\n"
"v_accvgpr_read_b32 v23, a63\n"
"v_mfma_f32_16x16x32_bf16 v[72:75], a[168:171], v[196:199], v[72:75]\n"
"v_mul_f32 v16, v248, v16\n"
"v_mul_f32 v17, v248, v17\n"
"v_mfma_f32_16x16x32_bf16 v[72:75], a[160:163], v[192:195], v[72:75]\n"
"ds_read_b128 v[188:191], v2 offset:34816\n"
"v_mul_f32 v18, v248, v18\n"
"v_mfma_f32_16x16x32_bf16 v[80:83], a[160:163], v[160:163], v[80:83]\n"
"v_mul_f32 v19, v248, v19\n"
"v_mul_f32 v20, v248, v20\n"
"v_mfma_f32_16x16x32_bf16 v[80:83], a[168:171], v[164:167], v[80:83]\n"
"s_waitcnt lgkmcnt(2)\n"
"ds_read_b128 v[192:195], v2 offset:8192\n"
"v_mul_f32 v21, v248, v21\n"
"v_mfma_f32_16x16x32_bf16 v[80:83], a[176:179], v[168:171], v[80:83]\n"
"v_mul_f32 v22, v248, v22\n"
"v_mul_f32 v23, v248, v23\n"
"v_mfma_f32_16x16x32_bf16 v[80:83], a[184:187], v[172:175], v[80:83]\n"
"ds_read_b128 v[196:199], v2 offset:17408\n"
"v_cvt_pk_bf16_f32 v156, v16, v17\n"
"v_mfma_f32_16x16x32_bf16 v[84:87], a[188:191], v[172:175], v[84:87]\n"
"v_cvt_pk_bf16_f32 v157, v18, v19\n"
"v_cvt_pk_bf16_f32 v158, v20, v21\n"
"v_mfma_f32_16x16x32_bf16 v[84:87], a[180:183], v[168:171], v[84:87]\n"
"ds_read_b128 v[200:203], v2 offset:26624\n"
"v_cvt_pk_bf16_f32 v159, v22, v23\n"
"v_mfma_f32_16x16x32_bf16 v[84:87], a[172:175], v[164:167], v[84:87]\n"
"buffer_store_dwordx4 v[156:159], v1, s[40:43], s55 offen offset:1024 sc1\n"
"v_mfma_f32_16x16x32_bf16 v[84:87], a[164:167], v[160:163], v[84:87]\n"
"ds_read_b128 v[204:207], v2 offset:35840\n"
"v_add_u32 v2, 0xfffe1000, v2\n"
"v_mfma_f32_16x16x32_bf16 v[92:95], a[164:167], v[176:179], v[92:95]\n"
"s_waitcnt vmcnt(30)\n"
"s_barrier\n"
"v_mfma_f32_16x16x32_bf16 v[92:95], a[172:175], v[180:183], v[92:95]\n"
"s_waitcnt lgkmcnt(2)\n"
"s_add_u32 s55, s57, 4096\n"
"ds_read_b128 v[160:163], v2 offset:0\n"
"v_accvgpr_read_b32 v8, a64\n"
"v_mfma_f32_16x16x32_bf16 v[92:95], a[180:183], v[184:187], v[92:95]\n"
"v_accvgpr_read_b32 v12, a80\n"
"v_accvgpr_read_b32 v9, a65\n"
"v_mfma_f32_16x16x32_bf16 v[92:95], a[188:191], v[188:191], v[92:95]\n"
"ds_read_b128 v[164:167], v2 offset:9216\n"
"v_accvgpr_read_b32 v13, a81\n"
"v_mfma_f32_16x16x32_bf16 v[88:91], a[184:187], v[188:191], v[88:91]\n"
"v_accvgpr_read_b32 v10, a66\n"
"v_accvgpr_read_b32 v14, a82\n"
"v_mfma_f32_16x16x32_bf16 v[88:91], a[176:179], v[184:187], v[88:91]\n"
"ds_read_b128 v[168:171], v2 offset:18432\n"
"v_accvgpr_read_b32 v11, a67\n"
"v_mfma_f32_16x16x32_bf16 v[88:91], a[168:171], v[180:183], v[88:91]\n"
"v_accvgpr_read_b32 v15, a83\n"
"v_mul_f32 v8, v245, v8\n"
"v_mfma_f32_16x16x32_bf16 v[88:91], a[160:163], v[176:179], v[88:91]\n"
"ds_read_b128 v[172:175], v2 offset:27648\n"
"v_mul_f32 v9, v245, v9\n"
"v_mfma_f32_16x16x32_bf16 v[96:99], a[160:163], v[192:195], v[96:99]\n"
"v_mul_f32 v10, v245, v10\n"
"v_mul_f32 v11, v245, v11\n"
"v_mfma_f32_16x16x32_bf16 v[96:99], a[168:171], v[196:199], v[96:99]\n"
"s_waitcnt lgkmcnt(2)\n"
"ds_read_b128 v[176:179], v2 offset:1024\n"
"v_mul_f32 v12, v245, v12\n"
"v_mfma_f32_16x16x32_bf16 v[96:99], a[176:179], v[200:203], v[96:99]\n"
"v_mul_f32 v13, v245, v13\n"
"v_mul_f32 v14, v245, v14\n"
"v_mfma_f32_16x16x32_bf16 v[96:99], a[184:187], v[204:207], v[96:99]\n"
"ds_read_b128 v[180:183], v2 offset:10240\n"
"v_mul_f32 v15, v245, v15\n"
"v_mfma_f32_16x16x32_bf16 v[100:103], a[188:191], v[204:207], v[100:103]\n"
"v_cvt_pk_bf16_f32 v128, v8, v9\n"
"v_cvt_pk_bf16_f32 v129, v10, v11\n"
"v_mfma_f32_16x16x32_bf16 v[100:103], a[180:183], v[200:203], v[100:103]\n"
"ds_read_b128 v[184:187], v2 offset:19456\n"
"v_cvt_pk_bf16_f32 v130, v12, v13\n"
"v_mfma_f32_16x16x32_bf16 v[100:103], a[172:175], v[196:199], v[100:103]\n"
"v_cvt_pk_bf16_f32 v131, v14, v15\n"
"v_accvgpr_read_b32 v16, a68\n"
"v_mfma_f32_16x16x32_bf16 v[100:103], a[164:167], v[192:195], v[100:103]\n"
"s_waitcnt vmcnt(4)\n"
"ds_read_b128 v[188:191], v2 offset:28672\n"
"v_accvgpr_read_b32 v20, a84\n"
"v_mfma_f32_16x16x32_bf16 v[32:35], a[192:195], v[160:163], v[32:35]\n"
"buffer_store_dwordx4 v[128:131], v1, s[40:43], s55 offen offset:0 sc1\n"
"v_accvgpr_read_b32 v17, a69\n"
"v_mfma_f32_16x16x32_bf16 v[32:35], a[200:203], v[164:167], v[32:35]\n"
"s_waitcnt lgkmcnt(2)\n"
"ds_read_b128 v[192:195], v2 offset:2048\n"
"v_accvgpr_read_b32 v21, a85\n"
"v_accvgpr_read_b32 v18, a70\n"
"v_mfma_f32_16x16x32_bf16 v[32:35], a[208:211], v[168:171], v[32:35]\n"
"s_mov_b32 s53, 24576\n"
"buffer_load_dwordx4 a[128:131], v3, s[44:47], s53 offen offset:0 sc1\n"
"v_accvgpr_read_b32 v22, a86\n"
"v_mfma_f32_16x16x32_bf16 v[32:35], a[216:219], v[172:175], v[32:35]\n"
"ds_read_b128 v[196:199], v2 offset:11264\n"
"v_accvgpr_read_b32 v19, a71\n"
"v_accvgpr_read_b32 v23, a87\n"
"v_mfma_f32_16x16x32_bf16 v[36:39], a[220:223], v[172:175], v[36:39]\n"
"v_mul_f32 v16, v246, v16\n"
"v_mul_f32 v17, v246, v17\n"
"v_mul_f32 v18, v246, v18\n"
"v_mfma_f32_16x16x32_bf16 v[36:39], a[212:215], v[168:171], v[36:39]\n"
"ds_read_b128 v[200:203], v2 offset:20480\n"
"v_mul_f32 v19, v246, v19\n"
"v_mul_f32 v20, v246, v20\n"
"v_mfma_f32_16x16x32_bf16 v[36:39], a[204:207], v[164:167], v[36:39]\n"
"buffer_load_dwordx4 a[132:135], v3, s[44:47], s53 offen offset:1024 sc1\n"
"v_mul_f32 v21, v246, v21\n"
"v_mfma_f32_16x16x32_bf16 v[36:39], a[196:199], v[160:163], v[36:39]\n"
"ds_read_b128 v[204:207], v2 offset:29696\n"
"v_mul_f32 v22, v246, v22\n"
"v_mul_f32 v23, v246, v23\n"
"v_mfma_f32_16x16x32_bf16 v[44:47], a[196:199], v[176:179], v[44:47]\n"
"v_cvt_pk_bf16_f32 v132, v16, v17\n"
"v_cvt_pk_bf16_f32 v133, v18, v19\n"
"v_cvt_pk_bf16_f32 v134, v20, v21\n"
"v_mfma_f32_16x16x32_bf16 v[44:47], a[204:207], v[180:183], v[44:47]\n"
"s_waitcnt lgkmcnt(2)\n"
"ds_read_b128 v[160:163], v2 offset:3072\n"
"v_cvt_pk_bf16_f32 v135, v22, v23\n"
"v_accvgpr_read_b32 v8, a72\n"
"v_mfma_f32_16x16x32_bf16 v[44:47], a[212:215], v[184:187], v[44:47]\n"
"buffer_load_dwordx4 a[136:139], v3, s[44:47], s53 offen offset:2048 sc1\n"
"v_accvgpr_read_b32 v12, a88\n"
"v_mfma_f32_16x16x32_bf16 v[44:47], a[220:223], v[188:191], v[44:47]\n"
"ds_read_b128 v[164:167], v2 offset:12288\n"
"v_accvgpr_read_b32 v9, a73\n"
"v_accvgpr_read_b32 v13, a89\n"
"v_mfma_f32_16x16x32_bf16 v[40:43], a[216:219], v[188:191], v[40:43]\n"
"buffer_store_dwordx4 v[132:135], v1, s[40:43], s55 offen offset:1024 sc1\n"
"v_accvgpr_read_b32 v10, a74\n"
"v_mfma_f32_16x16x32_bf16 v[40:43], a[208:211], v[184:187], v[40:43]\n"
"s_add_u32 s55, s57, 561152\n"
"ds_read_b128 v[168:171], v2 offset:21504\n"
"v_accvgpr_read_b32 v14, a90\n"
"v_accvgpr_read_b32 v11, a75\n"
"v_mfma_f32_16x16x32_bf16 v[40:43], a[200:203], v[180:183], v[40:43]\n"
"buffer_load_dwordx4 a[140:143], v3, s[44:47], s53 offen offset:3072 sc1\n"
"v_accvgpr_read_b32 v15, a91\n"
"v_mfma_f32_16x16x32_bf16 v[40:43], a[192:195], v[176:179], v[40:43]\n"
"ds_read_b128 v[172:175], v2 offset:30720\n"
"v_mul_f32 v8, v247, v8\n"
"v_mul_f32 v9, v247, v9\n"
"v_mfma_f32_16x16x32_bf16 v[48:51], a[192:195], v[192:195], v[48:51]\n"
"v_mul_f32 v10, v247, v10\n"
"v_mul_f32 v11, v247, v11\n"
"v_mul_f32 v12, v247, v12\n"
"v_mfma_f32_16x16x32_bf16 v[48:51], a[200:203], v[196:199], v[48:51]\n"
"s_waitcnt lgkmcnt(2)\n"
"ds_read_b128 v[176:179], v2 offset:4096\n"
"v_mul_f32 v13, v247, v13\n"
"v_mul_f32 v14, v247, v14\n"
"v_mfma_f32_16x16x32_bf16 v[48:51], a[208:211], v[200:203], v[48:51]\n"
"s_mov_b32 s53, 28672\n"
"buffer_load_dwordx4 a[144:147], v3, s[44:47], s53 offen offset:0 sc1\n"
"v_mul_f32 v15, v247, v15\n"
"v_mfma_f32_16x16x32_bf16 v[48:51], a[216:219], v[204:207], v[48:51]\n"
"ds_read_b128 v[180:183], v2 offset:13312\n"
"v_cvt_pk_bf16_f32 v136, v8, v9\n"
"v_cvt_pk_bf16_f32 v137, v10, v11\n"
"v_mfma_f32_16x16x32_bf16 v[52:55], a[220:223], v[204:207], v[52:55]\n"
"v_cvt_pk_bf16_f32 v138, v12, v13\n"
"v_cvt_pk_bf16_f32 v139, v14, v15\n"
"v_accvgpr_read_b32 v16, a76\n"
"v_mfma_f32_16x16x32_bf16 v[52:55], a[212:215], v[200:203], v[52:55]\n"
".LPAD12:\n"
"s_cbranch_execz .LPAD15\n"
"ds_read_b128 v[184:187], v2 offset:22528\n"
"v_accvgpr_read_b32 v20, a92\n"
"v_accvgpr_read_b32 v17, a77\n"
"v_mfma_f32_16x16x32_bf16 v[52:55], a[204:207], v[196:199], v[52:55]\n"
"buffer_load_dwordx4 a[148:151], v3, s[44:47], s53 offen offset:1024 sc1\n"
"v_accvgpr_read_b32 v21, a93\n"
"v_mfma_f32_16x16x32_bf16 v[52:55], a[196:199], v[192:195], v[52:55]\n"
"ds_read_b128 v[188:191], v2 offset:31744\n"
"v_accvgpr_read_b32 v18, a78\n"
"v_accvgpr_read_b32 v22, a94\n"
"v_mfma_f32_16x16x32_bf16 v[60:63], a[196:199], v[160:163], v[60:63]\n"
"buffer_store_dwordx4 v[136:139], v1, s[40:43], s55 offen offset:0 sc1\n"
"v_accvgpr_read_b32 v19, a79\n"
"v_mfma_f32_16x16x32_bf16 v[60:63], a[204:207], v[164:167], v[60:63]\n"
"s_waitcnt lgkmcnt(2)\n"
"ds_read_b128 v[192:195], v2 offset:5120\n"
"v_accvgpr_read_b32 v23, a95\n"
"v_mul_f32 v16, v248, v16\n"
"v_mfma_f32_16x16x32_bf16 v[60:63], a[212:215], v[168:171], v[60:63]\n"
"buffer_load_dwordx4 a[152:155], v3, s[44:47], s53 offen offset:2048 sc1\n"
"v_mul_f32 v17, v248, v17\n"
"v_mfma_f32_16x16x32_bf16 v[60:63], a[220:223], v[172:175], v[60:63]\n"
"ds_read_b128 v[196:199], v2 offset:14336\n"
"v_mul_f32 v18, v248, v18\n"
"v_mul_f32 v19, v248, v19\n"
"v_mfma_f32_16x16x32_bf16 v[56:59], a[216:219], v[172:175], v[56:59]\n"
"v_mul_f32 v20, v248, v20\n"
"v_mul_f32 v21, v248, v21\n"
"v_mul_f32 v22, v248, v22\n"
"v_mfma_f32_16x16x32_bf16 v[56:59], a[208:211], v[168:171], v[56:59]\n"
"ds_read_b128 v[200:203], v2 offset:23552\n"
"v_mul_f32 v23, v248, v23\n"
"v_cvt_pk_bf16_f32 v140, v16, v17\n"
"v_mfma_f32_16x16x32_bf16 v[56:59], a[200:203], v[164:167], v[56:59]\n"
"buffer_load_dwordx4 a[156:159], v3, s[44:47], s53 offen offset:3072 sc1\n"
"v_cvt_pk_bf16_f32 v141, v18, v19\n"
"v_mfma_f32_16x16x32_bf16 v[56:59], a[192:195], v[160:163], v[56:59]\n"
"ds_read_b128 v[204:207], v2 offset:32768\n"
"v_cvt_pk_bf16_f32 v142, v20, v21\n"
"v_cvt_pk_bf16_f32 v143, v22, v23\n"
"v_mfma_f32_16x16x32_bf16 v[64:67], a[192:195], v[176:179], v[64:67]\n"
"buffer_store_dwordx4 v[140:143], v1, s[40:43], s55 offen offset:1024 sc1\n"
"v_accvgpr_read_b32 v8, a96\n"
"v_mfma_f32_16x16x32_bf16 v[64:67], a[200:203], v[180:183], v[64:67]\n"
"s_waitcnt lgkmcnt(2)\n"
"s_add_u32 m0, s84, 90112\n"
"ds_read_b128 v[160:163], v2 offset:6144\n"
"v_accvgpr_read_b32 v12, a112\n"
"v_accvgpr_read_b32 v9, a97\n"
"v_mfma_f32_16x16x32_bf16 v[64:67], a[208:211], v[184:187], v[64:67]\n"
"s_mov_b32 s52, 147456\n"
"buffer_load_dwordx4 v30, s[28:31], s52 offen offset:0 sc1 lds\n"
"v_accvgpr_read_b32 v13, a113\n"
"v_mfma_f32_16x16x32_bf16 v[64:67], a[216:219], v[188:191], v[64:67]\n"
"s_add_u32 s55, s57, 6144\n"
"ds_read_b128 v[164:167], v2 offset:15360\n"
"v_accvgpr_read_b32 v10, a98\n"
"v_accvgpr_read_b32 v14, a114\n"
"v_mfma_f32_16x16x32_bf16 v[68:71], a[220:223], v[188:191], v[68:71]\n"
"v_accvgpr_read_b32 v11, a99\n"
"v_accvgpr_read_b32 v15, a115\n"
"v_mul_f32 v8, v245, v8\n"
"v_mfma_f32_16x16x32_bf16 v[68:71], a[212:215], v[184:187], v[68:71]\n"
"ds_read_b128 v[168:171], v2 offset:24576\n"
"v_mul_f32 v9, v245, v9\n"
"v_mul_f32 v10, v245, v10\n"
"v_mfma_f32_16x16x32_bf16 v[68:71], a[204:207], v[180:183], v[68:71]\n"
"buffer_load_dwordx4 v30, s[28:31], s52 offen offset:1024 sc1 lds\n"
"v_mul_f32 v11, v245, v11\n"
"v_mfma_f32_16x16x32_bf16 v[68:71], a[196:199], v[176:179], v[68:71]\n"
"ds_read_b128 v[172:175], v2 offset:33792\n"
"v_mul_f32 v12, v245, v12\n"
"v_mul_f32 v13, v245, v13\n"
"v_mfma_f32_16x16x32_bf16 v[76:79], a[196:199], v[192:195], v[76:79]\n"
"v_mul_f32 v14, v245, v14\n"
"v_mul_f32 v15, v245, v15\n"
"v_cvt_pk_bf16_f32 v208, v8, v9\n"
"v_mfma_f32_16x16x32_bf16 v[76:79], a[204:207], v[196:199], v[76:79]\n"
"s_waitcnt lgkmcnt(2)\n"
"ds_read_b128 v[176:179], v2 offset:7168\n"
"v_cvt_pk_bf16_f32 v209, v10, v11\n"
"v_cvt_pk_bf16_f32 v210, v12, v13\n"
"v_mfma_f32_16x16x32_bf16 v[76:79], a[212:215], v[200:203], v[76:79]\n"
"buffer_load_dwordx4 v30, s[28:31], s52 offen offset:2048 sc1 lds\n"
"v_cvt_pk_bf16_f32 v211, v14, v15\n"
"v_mfma_f32_16x16x32_bf16 v[76:79], a[220:223], v[204:207], v[76:79]\n"
"ds_read_b128 v[180:183], v2 offset:16384\n"
"v_accvgpr_read_b32 v16, a100\n"
"v_accvgpr_read_b32 v20, a116\n"
"v_mfma_f32_16x16x32_bf16 v[72:75], a[216:219], v[204:207], v[72:75]\n"
"v_accvgpr_read_b32 v17, a101\n"
"v_accvgpr_read_b32 v21, a117\n"
"v_accvgpr_read_b32 v18, a102\n"
"v_mfma_f32_16x16x32_bf16 v[72:75], a[208:211], v[200:203], v[72:75]\n"
"ds_read_b128 v[184:187], v2 offset:25600\n"
"v_accvgpr_read_b32 v22, a118\n"
"v_accvgpr_read_b32 v19, a103\n"
"v_mfma_f32_16x16x32_bf16 v[72:75], a[200:203], v[196:199], v[72:75]\n"
"buffer_load_dwordx4 v30, s[28:31], s52 offen offset:3072 sc1 lds\n"
"v_accvgpr_read_b32 v23, a119\n"
"v_mfma_f32_16x16x32_bf16 v[72:75], a[192:195], v[192:195], v[72:75]\n"
"ds_read_b128 v[188:191], v2 offset:34816\n"
"v_mul_f32 v16, v246, v16\n"
"v_mul_f32 v17, v246, v17\n"
"v_mfma_f32_16x16x32_bf16 v[80:83], a[192:195], v[160:163], v[80:83]\n"
"buffer_store_dwordx4 v[208:211], v1, s[40:43], s55 offen offset:0 sc1\n"
"v_mul_f32 v18, v246, v18\n"
"v_mfma_f32_16x16x32_bf16 v[80:83], a[200:203], v[164:167], v[80:83]\n"
"s_waitcnt lgkmcnt(2)\n"
"s_add_u32 m0, s84, 94208\n"
"ds_read_b128 v[192:195], v2 offset:8192\n"
"v_mul_f32 v19, v246, v19\n"
"v_mul_f32 v20, v246, v20\n"
"v_mfma_f32_16x16x32_bf16 v[80:83], a[208:211], v[168:171], v[80:83]\n"
"s_mov_b32 s52, 151552\n"
"buffer_load_dwordx4 v30, s[28:31], s52 offen offset:0 sc1 lds\n"
"v_mul_f32 v21, v246, v21\n"
"v_mfma_f32_16x16x32_bf16 v[80:83], a[216:219], v[172:175], v[80:83]\n"
"ds_read_b128 v[196:199], v2 offset:17408\n"
"v_mul_f32 v22, v246, v22\n"
"v_mul_f32 v23, v246, v23\n"
"v_mfma_f32_16x16x32_bf16 v[84:87], a[220:223], v[172:175], v[84:87]\n"
"v_cvt_pk_bf16_f32 v212, v16, v17\n"
"v_cvt_pk_bf16_f32 v213, v18, v19\n"
"v_cvt_pk_bf16_f32 v214, v20, v21\n"
"v_mfma_f32_16x16x32_bf16 v[84:87], a[212:215], v[168:171], v[84:87]\n"
"ds_read_b128 v[200:203], v2 offset:26624\n"
"v_cvt_pk_bf16_f32 v215, v22, v23\n"
"v_accvgpr_read_b32 v8, a104\n"
"v_mfma_f32_16x16x32_bf16 v[84:87], a[204:207], v[164:167], v[84:87]\n"
"buffer_load_dwordx4 v30, s[28:31], s52 offen offset:1024 sc1 lds\n"
"v_accvgpr_read_b32 v12, a120\n"
"v_mfma_f32_16x16x32_bf16 v[84:87], a[196:199], v[160:163], v[84:87]\n"
"ds_read_b128 v[204:207], v2 offset:35840\n"
"v_add_u32 v2, 0x9000, v2\n"
"v_accvgpr_read_b32 v9, a105\n"
"v_mfma_f32_16x16x32_bf16 v[92:95], a[196:199], v[176:179], v[92:95]\n"
"s_waitcnt vmcnt(21)\n"
"s_barrier\n"
"v_mfma_f32_16x16x32_bf16 v[92:95], a[204:207], v[180:183], v[92:95]\n"
"s_waitcnt lgkmcnt(2)\n"
"ds_read_b128 v[160:163], v2 offset:0\n"
"v_accvgpr_read_b32 v13, a121\n"
"v_accvgpr_read_b32 v10, a106\n"
"v_mfma_f32_16x16x32_bf16 v[92:95], a[212:215], v[184:187], v[92:95]\n"
"buffer_load_dwordx4 v30, s[28:31], s52 offen offset:2048 sc1 lds\n"
"v_accvgpr_read_b32 v14, a122\n"
"v_mfma_f32_16x16x32_bf16 v[92:95], a[220:223], v[188:191], v[92:95]\n"
"ds_read_b128 v[164:167], v2 offset:9216\n"
"v_accvgpr_read_b32 v11, a107\n"
"v_accvgpr_read_b32 v15, a123\n"
"v_mfma_f32_16x16x32_bf16 v[88:91], a[216:219], v[188:191], v[88:91]\n"
"v_mul_f32 v8, v247, v8\n"
"v_mul_f32 v9, v247, v9\n"
"v_mul_f32 v10, v247, v10\n"
"v_mfma_f32_16x16x32_bf16 v[88:91], a[208:211], v[184:187], v[88:91]\n"
"ds_read_b128 v[168:171], v2 offset:18432\n"
"v_mul_f32 v11, v247, v11\n"
"v_mul_f32 v12, v247, v12\n"
"v_mfma_f32_16x16x32_bf16 v[88:91], a[200:203], v[180:183], v[88:91]\n"
"buffer_load_dwordx4 v30, s[28:31], s52 offen offset:3072 sc1 lds\n"
"v_mul_f32 v13, v247, v13\n"
"v_mfma_f32_16x16x32_bf16 v[88:91], a[192:195], v[176:179], v[88:91]\n"
"ds_read_b128 v[172:175], v2 offset:27648\n"
"v_mul_f32 v14, v247, v14\n"
"v_mul_f32 v15, v247, v15\n"
"v_mfma_f32_16x16x32_bf16 v[96:99], a[192:195], v[192:195], v[96:99]\n"
"v_cvt_pk_bf16_f32 v216, v8, v9\n"
"v_cvt_pk_bf16_f32 v217, v10, v11\n"
"v_cvt_pk_bf16_f32 v218, v12, v13\n"
"v_mfma_f32_16x16x32_bf16 v[96:99], a[200:203], v[196:199], v[96:99]\n"
"s_waitcnt lgkmcnt(2)\n"
"s_add_u32 m0, s84, 98304\n"
"ds_read_b128 v[176:179], v2 offset:1024\n"
"v_cvt_pk_bf16_f32 v219, v14, v15\n"
"v_accvgpr_read_b32 v16, a108\n"
"v_mfma_f32_16x16x32_bf16 v[96:99], a[208:211], v[200:203], v[96:99]\n"
"s_mov_b32 s52, 155648\n"
"buffer_load_dwordx4 v30, s[28:31], s52 offen offset:0 sc1 lds\n"
"v_accvgpr_read_b32 v20, a124\n"
"v_mfma_f32_16x16x32_bf16 v[96:99], a[216:219], v[204:207], v[96:99]\n"
"ds_read_b128 v[180:183], v2 offset:10240\n"
"v_accvgpr_read_b32 v17, a109\n"
"v_accvgpr_read_b32 v21, a125\n"
"v_mfma_f32_16x16x32_bf16 v[100:103], a[220:223], v[204:207], v[100:103]\n"
"v_accvgpr_read_b32 v18, a110\n"
"v_accvgpr_read_b32 v22, a126\n"
"v_accvgpr_read_b32 v19, a111\n"
"v_mfma_f32_16x16x32_bf16 v[100:103], a[212:215], v[200:203], v[100:103]\n"
"ds_read_b128 v[184:187], v2 offset:19456\n"
"v_accvgpr_read_b32 v23, a127\n"
"v_mul_f32 v16, v248, v16\n"
"v_mfma_f32_16x16x32_bf16 v[100:103], a[204:207], v[196:199], v[100:103]\n"
"v_mul_f32 v17, v248, v17\n"
"v_mul_f32 v18, v248, v18\n"
"v_mul_f32 v19, v248, v19\n"
"v_mfma_f32_16x16x32_bf16 v[100:103], a[196:199], v[192:195], v[100:103]\n"
"s_waitcnt vmcnt(11)\n"
"ds_read_b128 v[188:191], v2 offset:28672\n"
"v_mul_f32 v20, v248, v20\n"
"v_mul_f32 v21, v248, v21\n"
"v_mfma_f32_16x16x32_bf16 v[32:35], a[128:131], v[160:163], v[32:35]\n"
"buffer_store_dwordx4 v[212:215], v1, s[40:43], s55 offen offset:1024 sc1\n"
"v_mul_f32 v22, v248, v22\n"
"v_mfma_f32_16x16x32_bf16 v[32:35], a[136:139], v[164:167], v[32:35]\n"
"s_waitcnt lgkmcnt(2)\n"
"s_add_u32 s55, s57, 563200\n"
"ds_read_b128 v[192:195], v2 offset:2048\n"
"v_mul_f32 v23, v248, v23\n"
"v_cvt_pk_bf16_f32 v220, v16, v17\n"
"v_mfma_f32_16x16x32_bf16 v[32:35], a[144:147], v[168:171], v[32:35]\n"
"s_mov_b32 s53, 32768\n"
"buffer_load_dwordx2 a[160:161], v29, s[44:47], s53 offen offset:0 sc1\n"
"v_cvt_pk_bf16_f32 v221, v18, v19\n"
"v_mfma_f32_16x16x32_bf16 v[32:35], a[152:155], v[172:175], v[32:35]\n"
"ds_read_b128 v[196:199], v2 offset:11264\n"
"v_cvt_pk_bf16_f32 v222, v20, v21\n"
"v_cvt_pk_bf16_f32 v223, v22, v23\n"
"v_mfma_f32_16x16x32_bf16 v[36:39], a[156:159], v[172:175], v[36:39]\n"
"buffer_load_dwordx2 a[162:163], v29, s[44:47], s53 offen offset:512 sc1\n"
"v_mfma_f32_16x16x32_bf16 v[36:39], a[148:151], v[168:171], v[36:39]\n"
"ds_read_b128 v[200:203], v2 offset:20480\n"
"v_lshrrev_b32 v6, 4, v0\n"
"v_mfma_f32_16x16x32_bf16 v[36:39], a[140:143], v[164:167], v[36:39]\n"
"buffer_load_dwordx2 a[164:165], v29, s[44:47], s53 offen offset:1024 sc1\n"
"v_mfma_f32_16x16x32_bf16 v[36:39], a[132:135], v[160:163], v[36:39]\n"
"ds_read_b128 v[204:207], v2 offset:29696\n"
"v_and_b32 v7, 15, v0\n"
"v_mfma_f32_16x16x32_bf16 v[44:47], a[132:135], v[176:179], v[44:47]\n"
"v_lshl_add_u32 v5, v7, 2, v6\n"
"v_lshlrev_b32 v5, 2, v5\n"
"v_mfma_f32_16x16x32_bf16 v[44:47], a[140:143], v[180:183], v[44:47]\n"
"s_waitcnt lgkmcnt(2)\n"
"ds_read_b128 v[160:163], v2 offset:3072\n"
"v_xor_b32 v254, s58, v6\n"
"v_mfma_f32_16x16x32_bf16 v[44:47], a[148:151], v[184:187], v[44:47]\n"
"buffer_load_dwordx2 a[166:167], v29, s[44:47], s53 offen offset:1536 sc1\n"
"v_mfma_f32_16x16x32_bf16 v[44:47], a[156:159], v[188:191], v[44:47]\n"
"ds_read_b128 v[164:167], v2 offset:12288\n"
"v_mul_u32_u24 v254, 24, v254\n"
"v_mfma_f32_16x16x32_bf16 v[40:43], a[152:155], v[188:191], v[40:43]\n"
"buffer_load_dwordx2 a[168:169], v29, s[44:47], s53 offen offset:2048 sc1\n"
"v_mfma_f32_16x16x32_bf16 v[40:43], a[144:147], v[184:187], v[40:43]\n"
"ds_read_b128 v[168:171], v2 offset:21504\n"
"v_add_u32 v254, v254, v6\n"
"v_mfma_f32_16x16x32_bf16 v[40:43], a[136:139], v[180:183], v[40:43]\n"
"buffer_load_dwordx2 a[170:171], v29, s[44:47], s53 offen offset:2560 sc1\n"
"v_mfma_f32_16x16x32_bf16 v[40:43], a[128:131], v[176:179], v[40:43]\n"
"ds_read_b128 v[172:175], v2 offset:30720\n"
"v_mul_u32_u24 v254, 2304, v254\n"
"v_mfma_f32_16x16x32_bf16 v[48:51], a[128:131], v[192:195], v[48:51]\n"
"v_lshl_add_u32 v254, v7, 4, v254\n"
"v_add_u32 v254, 0xffffff00, v254\n"
"v_mfma_f32_16x16x32_bf16 v[48:51], a[136:139], v[196:199], v[48:51]\n"
"s_waitcnt lgkmcnt(2)\n"
"ds_read_b128 v[176:179], v2 offset:4096\n"
"v_sub_u32 v255, v6, 1\n"
"v_mfma_f32_16x16x32_bf16 v[48:51], a[144:147], v[200:203], v[48:51]\n"
"buffer_load_dwordx2 a[172:173], v29, s[44:47], s53 offen offset:3072 sc1\n"
"v_mfma_f32_16x16x32_bf16 v[48:51], a[152:155], v[204:207], v[48:51]\n"
"ds_read_b128 v[180:183], v2 offset:13312\n"
"v_and_b32 v255, 0x40000000, v255\n"
"v_mfma_f32_16x16x32_bf16 v[52:55], a[156:159], v[204:207], v[52:55]\n"
"buffer_load_dwordx2 a[174:175], v29, s[44:47], s53 offen offset:3584 sc1\n"
"v_mfma_f32_16x16x32_bf16 v[52:55], a[148:151], v[200:203], v[52:55]\n"
"ds_read_b128 v[184:187], v2 offset:22528\n"
"v_add_u32 v254, v254, v255\n"
"v_mfma_f32_16x16x32_bf16 v[52:55], a[140:143], v[196:199], v[52:55]\n"
"s_mov_b32 s53, 36864\n"
"buffer_load_dwordx4 a[176:179], v3, s[44:47], s53 offen offset:0 sc1\n"
"v_mfma_f32_16x16x32_bf16 v[52:55], a[132:135], v[192:195], v[52:55]\n"
"ds_read_b128 v[188:191], v2 offset:31744\n"
"v_lshl_add_u32 v4, v6, 2, s58\n"
"v_mfma_f32_16x16x32_bf16 v[60:63], a[132:135], v[160:163], v[60:63]\n"
"buffer_store_dwordx4 v[216:219], v1, s[40:43], s55 offen offset:0 sc1\n"
"v_mfma_f32_16x16x32_bf16 v[60:63], a[140:143], v[164:167], v[60:63]\n"
"s_waitcnt lgkmcnt(2)\n"
"ds_read_b128 v[192:195], v2 offset:5120\n"
"v_mul_u32_u24 v4, 1152, v4\n"
"v_mfma_f32_16x16x32_bf16 v[60:63], a[148:151], v[168:171], v[60:63]\n"
"buffer_load_dwordx4 a[180:183], v3, s[44:47], s53 offen offset:1024 sc1\n"
"v_mfma_f32_16x16x32_bf16 v[60:63], a[156:159], v[172:175], v[60:63]\n"
"ds_read_b128 v[196:199], v2 offset:14336\n"
"v_lshl_add_u32 v4, v7, 4, v4\n"
"v_mfma_f32_16x16x32_bf16 v[56:59], a[152:155], v[172:175], v[56:59]\n"
"buffer_load_dwordx4 a[184:187], v3, s[44:47], s53 offen offset:2048 sc1\n"
"v_mfma_f32_16x16x32_bf16 v[56:59], a[144:147], v[168:171], v[56:59]\n"
"ds_read_b128 v[200:203], v2 offset:23552\n"
"v_mov_b32 v251, s83\n"
"v_mfma_f32_16x16x32_bf16 v[56:59], a[136:139], v[164:167], v[56:59]\n"
"buffer_load_dwordx4 a[188:191], v3, s[44:47], s53 offen offset:3072 sc1\n"
"v_mfma_f32_16x16x32_bf16 v[56:59], a[128:131], v[160:163], v[56:59]\n"
"ds_read_b128 v[204:207], v2 offset:32768\n"
"v_lshlrev_b32 v251, 12, v251\n"
"v_mfma_f32_16x16x32_bf16 v[64:67], a[128:131], v[176:179], v[64:67]\n"
"v_add_u32 v251, 126976, v251\n"
"v_lshl_add_u32 v7, v7, 1, v251\n"
"v_mfma_f32_16x16x32_bf16 v[64:67], a[136:139], v[180:183], v[64:67]\n"
"s_waitcnt lgkmcnt(2)\n"
"ds_read_b128 v[160:163], v2 offset:6144\n"
"v_lshl_add_u32 v18, v6, 8, v7\n"
"v_mfma_f32_16x16x32_bf16 v[64:67], a[144:147], v[184:187], v[64:67]\n"
"s_add_u32 s54, s6, .LP3_W3-.LK_START\n"
"buffer_load_dword a255, off, s[12:15], s54\n"
"v_mfma_f32_16x16x32_bf16 v[64:67], a[152:155], v[188:191], v[64:67]\n"
"ds_read_b128 v[164:167], v2 offset:15360\n"
"v_lshl_add_u32 v19, v6, 5, v7\n"
"v_mfma_f32_16x16x32_bf16 v[68:71], a[156:159], v[188:191], v[68:71]\n"
"v_add_u32 v251, v251, v1\n"
"v_lshrrev_b32 v7, 1, v0\n"
"v_mfma_f32_16x16x32_bf16 v[68:71], a[148:151], v[184:187], v[68:71]\n"
"s_add_u32 m0, s84, 126976\n"
"ds_read_b128 v[168:171], v2 offset:24576\n"
"v_lshl_add_u32 v7, v7, 2, s58\n"
"v_mfma_f32_16x16x32_bf16 v[68:71], a[140:143], v[180:183], v[68:71]\n"
"s_mov_b32 s52, 184320\n"
"buffer_load_dwordx4 v30, s[28:31], s52 offen offset:0 sc1 lds\n"
"v_mfma_f32_16x16x32_bf16 v[68:71], a[132:135], v[176:179], v[68:71]\n"
"ds_read_b128 v[172:175], v2 offset:33792\n"
"v_mul_u32_u24 v7, 1152, v7\n"
"v_mfma_f32_16x16x32_bf16 v[76:79], a[132:135], v[192:195], v[76:79]\n"
"v_and_b32 v17, 1, v0\n"
"v_lshl_add_u32 v17, v17, 4, v7\n"
"v_mfma_f32_16x16x32_bf16 v[76:79], a[140:143], v[196:199], v[76:79]\n"
"s_waitcnt lgkmcnt(2)\n"
"ds_read_b128 v[176:179], v2 offset:7168\n"
"v_mov_b32 v233, 0\n"
"v_mfma_f32_16x16x32_bf16 v[76:79], a[148:151], v[200:203], v[76:79]\n"
"buffer_load_dwordx4 v30, s[28:31], s52 offen offset:1024 sc1 lds\n"
"v_mfma_f32_16x16x32_bf16 v[76:79], a[156:159], v[204:207], v[76:79]\n"
"ds_read_b128 v[180:183], v2 offset:16384\n"
"v_mov_b32 v234, 0\n"
"v_mfma_f32_16x16x32_bf16 v[72:75], a[152:155], v[204:207], v[72:75]\n"
"v_mov_b32 v235, 0\n"
"v_mov_b32 v236, 0\n"
"v_mfma_f32_16x16x32_bf16 v[72:75], a[144:147], v[200:203], v[72:75]\n"
"ds_read_b128 v[184:187], v2 offset:25600\n"
"v_mov_b32 v237, 0\n"
"v_mfma_f32_16x16x32_bf16 v[72:75], a[136:139], v[196:199], v[72:75]\n"
"buffer_load_dwordx4 v30, s[28:31], s52 offen offset:2048 sc1 lds\n"
"v_mfma_f32_16x16x32_bf16 v[72:75], a[128:131], v[192:195], v[72:75]\n"
"ds_read_b128 v[188:191], v2 offset:34816\n"
"v_mov_b32 v238, 0\n"
"v_mfma_f32_16x16x32_bf16 v[80:83], a[128:131], v[160:163], v[80:83]\n"
"buffer_store_dwordx4 v[220:223], v1, s[40:43], s55 offen offset:1024 sc1\n"
"v_mfma_f32_16x16x32_bf16 v[80:83], a[136:139], v[164:167], v[80:83]\n"
"s_waitcnt lgkmcnt(2)\n"
"ds_read_b128 v[192:195], v2 offset:8192\n"
"v_mov_b32 v239, 0\n"
"v_mfma_f32_16x16x32_bf16 v[80:83], a[144:147], v[168:171], v[80:83]\n"
"buffer_load_dwordx4 v30, s[28:31], s52 offen offset:3072 sc1 lds\n"
"v_mfma_f32_16x16x32_bf16 v[80:83], a[152:155], v[172:175], v[80:83]\n"
"ds_read_b128 v[196:199], v2 offset:17408\n"
"v_mov_b32 v240, 0\n"
"v_mfma_f32_16x16x32_bf16 v[84:87], a[156:159], v[172:175], v[84:87]\n"
"v_mov_b32 v241, 0\n"
"v_mov_b32 v242, 0\n"
"v_mfma_f32_16x16x32_bf16 v[84:87], a[148:151], v[168:171], v[84:87]\n"
"s_add_u32 m0, s84, 131072\n"
"ds_read_b128 v[200:203], v2 offset:26624\n"
"v_mov_b32 v243, 0\n"
"v_mfma_f32_16x16x32_bf16 v[84:87], a[140:143], v[164:167], v[84:87]\n"
"s_mov_b32 s52, 188416\n"
"buffer_load_dwordx4 v30, s[28:31], s52 offen offset:0 sc1 lds\n"
"v_mfma_f32_16x16x32_bf16 v[84:87], a[132:135], v[160:163], v[84:87]\n"
"ds_read_b128 v[204:207], v2 offset:35840\n"
"v_add_u32 v2, 0xd000, v2\n"
"v_mfma_f32_16x16x32_bf16 v[92:95], a[132:135], v[176:179], v[92:95]\n"
"s_waitcnt vmcnt(21)\n"
"s_barrier\n"
"v_mfma_f32_16x16x32_bf16 v[92:95], a[140:143], v[180:183], v[92:95]\n"
"s_waitcnt lgkmcnt(2)\n"
"ds_read_b128 v[160:163], v2 offset:0\n"
"v_mov_b32 v244, 0\n"
"v_mfma_f32_16x16x32_bf16 v[92:95], a[148:151], v[184:187], v[92:95]\n"
"buffer_load_dwordx4 v30, s[28:31], s52 offen offset:1024 sc1 lds\n"
"v_mfma_f32_16x16x32_bf16 v[92:95], a[156:159], v[188:191], v[92:95]\n"
"ds_read_b128 v[164:167], v2 offset:9216\n"
"v_mov_b32 v245, 0\n"
"v_mfma_f32_16x16x32_bf16 v[88:91], a[152:155], v[188:191], v[88:91]\n"
"v_mov_b32 v246, 0\n"
"v_mov_b32 v247, 0\n"
"v_mfma_f32_16x16x32_bf16 v[88:91], a[144:147], v[184:187], v[88:91]\n"
"ds_read_b128 v[168:171], v2 offset:18432\n"
"v_mov_b32 v248, 0\n"
"v_mfma_f32_16x16x32_bf16 v[88:91], a[136:139], v[180:183], v[88:91]\n"
"buffer_load_dwordx4 v30, s[28:31], s52 offen offset:2048 sc1 lds\n"
"v_mfma_f32_16x16x32_bf16 v[88:91], a[128:131], v[176:179], v[88:91]\n"
"ds_read_b128 v[172:175], v2 offset:27648\n"
"v_mov_b32 v249, 0\n"
"v_mfma_f32_16x16x32_bf16 v[96:99], a[128:131], v[192:195], v[96:99]\n"
"v_mov_b32 v250, 0\n"
"v_mfma_f32_16x16x32_bf16 v[96:99], a[136:139], v[196:199], v[96:99]\n"
"s_waitcnt lgkmcnt(2)\n"
"ds_read_b128 v[176:179], v2 offset:1024\n"
"v_mfma_f32_16x16x32_bf16 v[96:99], a[144:147], v[200:203], v[96:99]\n"
"buffer_load_dwordx4 v30, s[28:31], s52 offen offset:3072 sc1 lds\n"
"v_mfma_f32_16x16x32_bf16 v[96:99], a[152:155], v[204:207], v[96:99]\n"
"ds_read_b128 v[180:183], v2 offset:10240\n"
"v_mfma_f32_16x16x32_bf16 v[100:103], a[156:159], v[204:207], v[100:103]\n"
"v_mfma_f32_16x16x32_bf16 v[100:103], a[148:151], v[200:203], v[100:103]\n"
"s_add_u32 m0, s84, 135168\n"
"ds_read_b128 v[184:187], v2 offset:19456\n"
"v_mfma_f32_16x16x32_bf16 v[100:103], a[140:143], v[196:199], v[100:103]\n"
"s_mov_b32 s52, 192512\n"
"buffer_load_dwordx4 v30, s[28:31], s52 offen offset:0 sc1 lds\n"
"v_mfma_f32_16x16x32_bf16 v[100:103], a[132:135], v[192:195], v[100:103]\n"
"s_waitcnt vmcnt(11)\n"
"ds_read_b128 v[188:191], v2 offset:28672\n"
"v_mfma_f32_16x16x32_bf16 v[32:35], a[160:163], v[160:163], v[32:35]\n"
"v_mfma_f32_16x16x32_bf16 v[32:35], a[168:171], v[164:167], v[32:35]\n"
"s_waitcnt lgkmcnt(2)\n"
"ds_read_b128 v[192:195], v2 offset:2048\n"
"v_mfma_f32_16x16x32_bf16 v[32:35], a[176:179], v[168:171], v[32:35]\n"
"s_mov_b32 s53, 40960\n"
"buffer_load_dwordx4 a[192:195], v3, s[44:47], s53 offen offset:0 sc1\n"
"v_mfma_f32_16x16x32_bf16 v[32:35], a[184:187], v[172:175], v[32:35]\n"
"ds_read_b128 v[196:199], v2 offset:11264\n"
"v_mfma_f32_16x16x32_bf16 v[36:39], a[188:191], v[172:175], v[36:39]\n"
"v_mfma_f32_16x16x32_bf16 v[36:39], a[180:183], v[168:171], v[36:39]\n"
"ds_read_b128 v[200:203], v2 offset:20480\n"
"v_mfma_f32_16x16x32_bf16 v[36:39], a[172:175], v[164:167], v[36:39]\n"
"buffer_load_dwordx4 a[196:199], v3, s[44:47], s53 offen offset:1024 sc1\n"
"v_mfma_f32_16x16x32_bf16 v[36:39], a[164:167], v[160:163], v[36:39]\n"
"ds_read_b128 v[204:207], v2 offset:29696\n"
"v_mfma_f32_16x16x32_bf16 v[44:47], a[164:167], v[176:179], v[44:47]\n"
"v_mfma_f32_16x16x32_bf16 v[44:47], a[172:175], v[180:183], v[44:47]\n"
"s_waitcnt lgkmcnt(2)\n"
"ds_read_b128 v[160:163], v2 offset:3072\n"
"v_mfma_f32_16x16x32_bf16 v[44:47], a[180:183], v[184:187], v[44:47]\n"
"buffer_load_dwordx4 a[200:203], v3, s[44:47], s53 offen offset:2048 sc1\n"
"v_mfma_f32_16x16x32_bf16 v[44:47], a[188:191], v[188:191], v[44:47]\n"
"ds_read_b128 v[164:167], v2 offset:12288\n"
"v_mfma_f32_16x16x32_bf16 v[40:43], a[184:187], v[188:191], v[40:43]\n"
"v_mfma_f32_16x16x32_bf16 v[40:43], a[176:179], v[184:187], v[40:43]\n"
"ds_read_b128 v[168:171], v2 offset:21504\n"
"v_mfma_f32_16x16x32_bf16 v[40:43], a[168:171], v[180:183], v[40:43]\n"
"buffer_load_dwordx4 a[204:207], v3, s[44:47], s53 offen offset:3072 sc1\n"
"v_mfma_f32_16x16x32_bf16 v[40:43], a[160:163], v[176:179], v[40:43]\n"
"ds_read_b128 v[172:175], v2 offset:30720\n"
"v_mfma_f32_16x16x32_bf16 v[48:51], a[160:163], v[192:195], v[48:51]\n"
"v_mfma_f32_16x16x32_bf16 v[48:51], a[168:171], v[196:199], v[48:51]\n"
"s_waitcnt lgkmcnt(2)\n"
"ds_read_b128 v[176:179], v2 offset:4096\n"
"v_mfma_f32_16x16x32_bf16 v[48:51], a[176:179], v[200:203], v[48:51]\n"
"s_mov_b32 s53, 45056\n"
"buffer_load_dwordx4 a[208:211], v3, s[44:47], s53 offen offset:0 sc1\n"
"v_mfma_f32_16x16x32_bf16 v[48:51], a[184:187], v[204:207], v[48:51]\n"
"ds_read_b128 v[180:183], v2 offset:13312\n"
"v_mfma_f32_16x16x32_bf16 v[52:55], a[188:191], v[204:207], v[52:55]\n"
"v_mfma_f32_16x16x32_bf16 v[52:55], a[180:183], v[200:203], v[52:55]\n"
"ds_read_b128 v[184:187], v2 offset:22528\n"
"v_mfma_f32_16x16x32_bf16 v[52:55], a[172:175], v[196:199], v[52:55]\n"
"buffer_load_dwordx4 a[212:215], v3, s[44:47], s53 offen offset:1024 sc1\n"
"v_mfma_f32_16x16x32_bf16 v[52:55], a[164:167], v[192:195], v[52:55]\n"
"ds_read_b128 v[188:191], v2 offset:31744\n"
"v_mfma_f32_16x16x32_bf16 v[60:63], a[164:167], v[160:163], v[60:63]\n"
"v_mfma_f32_16x16x32_bf16 v[60:63], a[172:175], v[164:167], v[60:63]\n"
"s_waitcnt lgkmcnt(2)\n"
"ds_read_b128 v[192:195], v2 offset:5120\n"
"v_mfma_f32_16x16x32_bf16 v[60:63], a[180:183], v[168:171], v[60:63]\n"
"buffer_load_dwordx4 a[216:219], v3, s[44:47], s53 offen offset:2048 sc1\n"
"v_mfma_f32_16x16x32_bf16 v[60:63], a[188:191], v[172:175], v[60:63]\n"
"ds_read_b128 v[196:199], v2 offset:14336\n"
"v_mfma_f32_16x16x32_bf16 v[56:59], a[184:187], v[172:175], v[56:59]\n"
"v_mfma_f32_16x16x32_bf16 v[56:59], a[176:179], v[168:171], v[56:59]\n"
"ds_read_b128 v[200:203], v2 offset:23552\n"
"v_mfma_f32_16x16x32_bf16 v[56:59], a[168:171], v[164:167], v[56:59]\n"
"buffer_load_dwordx4 a[220:223], v3, s[44:47], s53 offen offset:3072 sc1\n"
"v_mfma_f32_16x16x32_bf16 v[56:59], a[160:163], v[160:163], v[56:59]\n"
"ds_read_b128 v[204:207], v2 offset:32768\n"
"v_mfma_f32_16x16x32_bf16 v[64:67], a[160:163], v[176:179], v[64:67]\n"
"v_mfma_f32_16x16x32_bf16 v[64:67], a[168:171], v[180:183], v[64:67]\n"
"s_waitcnt lgkmcnt(2)\n"
"s_add_u32 m0, s84, 0\n"
"ds_read_b128 v[160:163], v2 offset:6144\n"
"v_mfma_f32_16x16x32_bf16 v[64:67], a[176:179], v[184:187], v[64:67]\n"
"s_mov_b32 s52, 221184\n"
"buffer_load_dwordx4 v30, s[28:31], s52 offen offset:0 sc1 lds\n"
"v_mfma_f32_16x16x32_bf16 v[64:67], a[184:187], v[188:191], v[64:67]\n"
"ds_read_b128 v[164:167], v2 offset:15360\n"
"v_mfma_f32_16x16x32_bf16 v[68:71], a[188:191], v[188:191], v[68:71]\n"
"v_mfma_f32_16x16x32_bf16 v[68:71], a[180:183], v[184:187], v[68:71]\n"
"ds_read_b128 v[168:171], v2 offset:24576\n"
"v_mfma_f32_16x16x32_bf16 v[68:71], a[172:175], v[180:183], v[68:71]\n"
"buffer_load_dwordx4 v30, s[28:31], s52 offen offset:1024 sc1 lds\n"
"v_mfma_f32_16x16x32_bf16 v[68:71], a[164:167], v[176:179], v[68:71]\n"
"ds_read_b128 v[172:175], v2 offset:33792\n"
"v_mfma_f32_16x16x32_bf16 v[76:79], a[164:167], v[192:195], v[76:79]\n"
"v_mfma_f32_16x16x32_bf16 v[76:79], a[172:175], v[196:199], v[76:79]\n"
"s_waitcnt lgkmcnt(2)\n"
"ds_read_b128 v[176:179], v2 offset:7168\n"
"v_mfma_f32_16x16x32_bf16 v[76:79], a[180:183], v[200:203], v[76:79]\n"
"buffer_load_dwordx4 v30, s[28:31], s52 offen offset:2048 sc1 lds\n"
"v_mfma_f32_16x16x32_bf16 v[76:79], a[188:191], v[204:207], v[76:79]\n"
"ds_read_b128 v[180:183], v2 offset:16384\n"
"v_mfma_f32_16x16x32_bf16 v[72:75], a[184:187], v[204:207], v[72:75]\n"
"v_mfma_f32_16x16x32_bf16 v[72:75], a[176:179], v[200:203], v[72:75]\n"
"ds_read_b128 v[184:187], v2 offset:25600\n"
"v_mfma_f32_16x16x32_bf16 v[72:75], a[168:171], v[196:199], v[72:75]\n"
"buffer_load_dwordx4 v30, s[28:31], s52 offen offset:3072 sc1 lds\n"
"v_mfma_f32_16x16x32_bf16 v[72:75], a[160:163], v[192:195], v[72:75]\n"
"ds_read_b128 v[188:191], v2 offset:34816\n"
"v_mfma_f32_16x16x32_bf16 v[80:83], a[160:163], v[160:163], v[80:83]\n"
"v_mfma_f32_16x16x32_bf16 v[80:83], a[168:171], v[164:167], v[80:83]\n"
"s_waitcnt lgkmcnt(2)\n"
"s_add_u32 m0, s84, 4096\n"
"ds_read_b128 v[192:195], v2 offset:8192\n"
"v_mfma_f32_16x16x32_bf16 v[80:83], a[176:179], v[168:171], v[80:83]\n"
"s_mov_b32 s52, 225280\n"
"buffer_load_dwordx4 v30, s[28:31], s52 offen offset:0 sc1 lds\n"
"v_mfma_f32_16x16x32_bf16 v[80:83], a[184:187], v[172:175], v[80:83]\n"
"ds_read_b128 v[196:199], v2 offset:17408\n"
"v_mfma_f32_16x16x32_bf16 v[84:87], a[188:191], v[172:175], v[84:87]\n"
"v_mfma_f32_16x16x32_bf16 v[84:87], a[180:183], v[168:171], v[84:87]\n"
"ds_read_b128 v[200:203], v2 offset:26624\n"
"v_mfma_f32_16x16x32_bf16 v[84:87], a[172:175], v[164:167], v[84:87]\n"
"buffer_load_dwordx4 v30, s[28:31], s52 offen offset:1024 sc1 lds\n"
"v_mfma_f32_16x16x32_bf16 v[84:87], a[164:167], v[160:163], v[84:87]\n"
"ds_read_b128 v[204:207], v2 offset:35840\n"
"v_add_u32 v2, 0x9000, v2\n"
"v_mfma_f32_16x16x32_bf16 v[92:95], a[164:167], v[176:179], v[92:95]\n"
"s_waitcnt vmcnt(14)\n"
"s_barrier\n"
"v_mfma_f32_16x16x32_bf16 v[92:95], a[172:175], v[180:183], v[92:95]\n"
"s_waitcnt lgkmcnt(2)\n"
"ds_read_b128 v[160:163], v2 offset:0\n"
"v_mfma_f32_16x16x32_bf16 v[92:95], a[180:183], v[184:187], v[92:95]\n"
"buffer_load_dwordx4 v30, s[28:31], s52 offen offset:2048 sc1 lds\n"
"v_mfma_f32_16x16x32_bf16 v[92:95], a[188:191], v[188:191], v[92:95]\n"
"ds_read_b128 v[164:167], v2 offset:9216\n"
"v_mfma_f32_16x16x32_bf16 v[88:91], a[184:187], v[188:191], v[88:91]\n"
"v_mfma_f32_16x16x32_bf16 v[88:91], a[176:179], v[184:187], v[88:91]\n"
"ds_read_b128 v[168:171], v2 offset:18432\n"
"v_mfma_f32_16x16x32_bf16 v[88:91], a[168:171], v[180:183], v[88:91]\n"
"buffer_load_dwordx4 v30, s[28:31], s52 offen offset:3072 sc1 lds\n"
"v_mfma_f32_16x16x32_bf16 v[88:91], a[160:163], v[176:179], v[88:91]\n"
"ds_read_b128 v[172:175], v2 offset:27648\n"
"v_mfma_f32_16x16x32_bf16 v[96:99], a[160:163], v[192:195], v[96:99]\n"
"v_mfma_f32_16x16x32_bf16 v[96:99], a[168:171], v[196:199], v[96:99]\n"
"s_waitcnt lgkmcnt(2)\n"
"s_add_u32 m0, s84, 8192\n"
"ds_read_b128 v[176:179], v2 offset:1024\n"
"v_mfma_f32_16x16x32_bf16 v[96:99], a[176:179], v[200:203], v[96:99]\n"
"s_mov_b32 s52, 229376\n"
"buffer_load_dwordx4 v30, s[28:31], s52 offen offset:0 sc1 lds\n"
"v_mfma_f32_16x16x32_bf16 v[96:99], a[184:187], v[204:207], v[96:99]\n"
"ds_read_b128 v[180:183], v2 offset:10240\n"
"v_mfma_f32_16x16x32_bf16 v[100:103], a[188:191], v[204:207], v[100:103]\n"
"v_mfma_f32_16x16x32_bf16 v[100:103], a[180:183], v[200:203], v[100:103]\n"
"ds_read_b128 v[184:187], v2 offset:19456\n"
"v_mfma_f32_16x16x32_bf16 v[100:103], a[172:175], v[196:199], v[100:103]\n"
"v_mfma_f32_16x16x32_bf16 v[100:103], a[164:167], v[192:195], v[100:103]\n"
"s_waitcnt vmcnt(9)\n"
"ds_read_b128 v[188:191], v2 offset:28672\n"
"v_mfma_f32_16x16x32_bf16 v[32:35], a[192:195], v[160:163], v[32:35]\n"
"v_mfma_f32_16x16x32_bf16 v[32:35], a[200:203], v[164:167], v[32:35]\n"
"s_waitcnt lgkmcnt(2)\n"
"ds_read_b128 v[192:195], v2 offset:2048\n"
"v_mfma_f32_16x16x32_bf16 v[32:35], a[208:211], v[168:171], v[32:35]\n"
"s_mov_b32 s53, 49152\n"
"buffer_load_dwordx4 a[128:131], v3, s[44:47], s53 offen offset:0 sc1\n"
"v_mfma_f32_16x16x32_bf16 v[32:35], a[216:219], v[172:175], v[32:35]\n"
".LPAD13:\n"
"s_cbranch_execz .LPAD16\n"
"ds_read_b128 v[196:199], v2 offset:11264\n"
".LP3_W3:\n"
"v_mfma_f32_16x16x32_bf16 v[36:39], a[220:223], v[172:175], v[36:39]\n"
"v_mfma_f32_16x16x32_bf16 v[36:39], a[212:215], v[168:171], v[36:39]\n"
"ds_read_b128 v[200:203], v2 offset:20480\n"
"v_mfma_f32_16x16x32_bf16 v[36:39], a[204:207], v[164:167], v[36:39]\n"
"buffer_load_dwordx4 a[132:135], v3, s[44:47], s53 offen offset:1024 sc1\n"
"v_mfma_f32_16x16x32_bf16 v[36:39], a[196:199], v[160:163], v[36:39]\n"
"ds_read_b128 v[204:207], v2 offset:29696\n"
"v_mfma_f32_16x16x32_bf16 v[44:47], a[196:199], v[176:179], v[44:47]\n"
"v_mfma_f32_16x16x32_bf16 v[44:47], a[204:207], v[180:183], v[44:47]\n"
"s_waitcnt lgkmcnt(2)\n"
"ds_read_b128 v[160:163], v2 offset:3072\n"
"v_mfma_f32_16x16x32_bf16 v[44:47], a[212:215], v[184:187], v[44:47]\n"
"buffer_load_dwordx4 a[136:139], v3, s[44:47], s53 offen offset:2048 sc1\n"
"v_mfma_f32_16x16x32_bf16 v[44:47], a[220:223], v[188:191], v[44:47]\n"
"ds_read_b128 v[164:167], v2 offset:12288\n"
"v_mfma_f32_16x16x32_bf16 v[40:43], a[216:219], v[188:191], v[40:43]\n"
"v_mfma_f32_16x16x32_bf16 v[40:43], a[208:211], v[184:187], v[40:43]\n"
"ds_read_b128 v[168:171], v2 offset:21504\n"
"v_mfma_f32_16x16x32_bf16 v[40:43], a[200:203], v[180:183], v[40:43]\n"
"buffer_load_dwordx4 a[140:143], v3, s[44:47], s53 offen offset:3072 sc1\n"
"v_mfma_f32_16x16x32_bf16 v[40:43], a[192:195], v[176:179], v[40:43]\n"
"ds_read_b128 v[172:175], v2 offset:30720\n"
"v_mfma_f32_16x16x32_bf16 v[48:51], a[192:195], v[192:195], v[48:51]\n"
"v_mfma_f32_16x16x32_bf16 v[48:51], a[200:203], v[196:199], v[48:51]\n"
"s_waitcnt lgkmcnt(2)\n"
"ds_read_b128 v[176:179], v2 offset:4096\n"
"v_mfma_f32_16x16x32_bf16 v[48:51], a[208:211], v[200:203], v[48:51]\n"
"s_mov_b32 s53, 53248\n"
"buffer_load_dwordx4 a[144:147], v3, s[44:47], s53 offen offset:0 sc1\n"
"v_mfma_f32_16x16x32_bf16 v[48:51], a[216:219], v[204:207], v[48:51]\n"
"ds_read_b128 v[180:183], v2 offset:13312\n"
"v_mfma_f32_16x16x32_bf16 v[52:55], a[220:223], v[204:207], v[52:55]\n"
"v_mfma_f32_16x16x32_bf16 v[52:55], a[212:215], v[200:203], v[52:55]\n"
"ds_read_b128 v[184:187], v2 offset:22528\n"
"v_mfma_f32_16x16x32_bf16 v[52:55], a[204:207], v[196:199], v[52:55]\n"
"buffer_load_dwordx4 a[148:151], v3, s[44:47], s53 offen offset:1024 sc1\n"
"v_mfma_f32_16x16x32_bf16 v[52:55], a[196:199], v[192:195], v[52:55]\n"
"ds_read_b128 v[188:191], v2 offset:31744\n"
"v_mfma_f32_16x16x32_bf16 v[60:63], a[196:199], v[160:163], v[60:63]\n"
"v_mfma_f32_16x16x32_bf16 v[60:63], a[204:207], v[164:167], v[60:63]\n"
"s_waitcnt lgkmcnt(2)\n"
"ds_read_b128 v[192:195], v2 offset:5120\n"
"v_mfma_f32_16x16x32_bf16 v[60:63], a[212:215], v[168:171], v[60:63]\n"
"buffer_load_dwordx4 a[152:155], v3, s[44:47], s53 offen offset:2048 sc1\n"
"v_mfma_f32_16x16x32_bf16 v[60:63], a[220:223], v[172:175], v[60:63]\n"
"ds_read_b128 v[196:199], v2 offset:14336\n"
"v_mfma_f32_16x16x32_bf16 v[56:59], a[216:219], v[172:175], v[56:59]\n"
"v_mfma_f32_16x16x32_bf16 v[56:59], a[208:211], v[168:171], v[56:59]\n"
"ds_read_b128 v[200:203], v2 offset:23552\n"
"v_mfma_f32_16x16x32_bf16 v[56:59], a[200:203], v[164:167], v[56:59]\n"
"buffer_load_dwordx4 a[156:159], v3, s[44:47], s53 offen offset:3072 sc1\n"
"v_mfma_f32_16x16x32_bf16 v[56:59], a[192:195], v[160:163], v[56:59]\n"
"ds_read_b128 v[204:207], v2 offset:32768\n"
"v_mfma_f32_16x16x32_bf16 v[64:67], a[192:195], v[176:179], v[64:67]\n"
"v_mfma_f32_16x16x32_bf16 v[64:67], a[200:203], v[180:183], v[64:67]\n"
"s_waitcnt lgkmcnt(2)\n"
"s_add_u32 m0, s84, 36864\n"
"ds_read_b128 v[160:163], v2 offset:6144\n"
"v_mfma_f32_16x16x32_bf16 v[64:67], a[208:211], v[184:187], v[64:67]\n"
"s_mov_b32 s52, 258048\n"
"buffer_load_dwordx4 v30, s[28:31], s52 offen offset:0 sc1 lds\n"
"v_mfma_f32_16x16x32_bf16 v[64:67], a[216:219], v[188:191], v[64:67]\n"
"ds_read_b128 v[164:167], v2 offset:15360\n"
"v_mfma_f32_16x16x32_bf16 v[68:71], a[220:223], v[188:191], v[68:71]\n"
"v_mfma_f32_16x16x32_bf16 v[68:71], a[212:215], v[184:187], v[68:71]\n"
"ds_read_b128 v[168:171], v2 offset:24576\n"
"v_mfma_f32_16x16x32_bf16 v[68:71], a[204:207], v[180:183], v[68:71]\n"
"buffer_load_dwordx4 v30, s[28:31], s52 offen offset:1024 sc1 lds\n"
"v_mfma_f32_16x16x32_bf16 v[68:71], a[196:199], v[176:179], v[68:71]\n"
"ds_read_b128 v[172:175], v2 offset:33792\n"
"v_mfma_f32_16x16x32_bf16 v[76:79], a[196:199], v[192:195], v[76:79]\n"
"v_mfma_f32_16x16x32_bf16 v[76:79], a[204:207], v[196:199], v[76:79]\n"
"s_waitcnt lgkmcnt(2)\n"
"ds_read_b128 v[176:179], v2 offset:7168\n"
"v_mfma_f32_16x16x32_bf16 v[76:79], a[212:215], v[200:203], v[76:79]\n"
"buffer_load_dwordx4 v30, s[28:31], s52 offen offset:2048 sc1 lds\n"
"v_mfma_f32_16x16x32_bf16 v[76:79], a[220:223], v[204:207], v[76:79]\n"
"ds_read_b128 v[180:183], v2 offset:16384\n"
"v_mfma_f32_16x16x32_bf16 v[72:75], a[216:219], v[204:207], v[72:75]\n"
"v_mfma_f32_16x16x32_bf16 v[72:75], a[208:211], v[200:203], v[72:75]\n"
"ds_read_b128 v[184:187], v2 offset:25600\n"
"v_mfma_f32_16x16x32_bf16 v[72:75], a[200:203], v[196:199], v[72:75]\n"
"buffer_load_dwordx4 v30, s[28:31], s52 offen offset:3072 sc1 lds\n"
"v_mfma_f32_16x16x32_bf16 v[72:75], a[192:195], v[192:195], v[72:75]\n"
"ds_read_b128 v[188:191], v2 offset:34816\n"
"v_mfma_f32_16x16x32_bf16 v[80:83], a[192:195], v[160:163], v[80:83]\n"
"v_mfma_f32_16x16x32_bf16 v[80:83], a[200:203], v[164:167], v[80:83]\n"
"s_waitcnt lgkmcnt(2)\n"
"s_add_u32 m0, s84, 40960\n"
"ds_read_b128 v[192:195], v2 offset:8192\n"
"v_mfma_f32_16x16x32_bf16 v[80:83], a[208:211], v[168:171], v[80:83]\n"
"s_mov_b32 s52, 262144\n"
"buffer_load_dwordx4 v30, s[28:31], s52 offen offset:0 sc1 lds\n"
"v_mfma_f32_16x16x32_bf16 v[80:83], a[216:219], v[172:175], v[80:83]\n"
"ds_read_b128 v[196:199], v2 offset:17408\n"
"v_mfma_f32_16x16x32_bf16 v[84:87], a[220:223], v[172:175], v[84:87]\n"
"v_mfma_f32_16x16x32_bf16 v[84:87], a[212:215], v[168:171], v[84:87]\n"
"ds_read_b128 v[200:203], v2 offset:26624\n"
"v_mfma_f32_16x16x32_bf16 v[84:87], a[204:207], v[164:167], v[84:87]\n"
"buffer_load_dwordx4 v30, s[28:31], s52 offen offset:1024 sc1 lds\n"
"v_mfma_f32_16x16x32_bf16 v[84:87], a[196:199], v[160:163], v[84:87]\n"
"ds_read_b128 v[204:207], v2 offset:35840\n"
"v_add_u32 v2, 0xfffe1000, v2\n"
"v_mfma_f32_16x16x32_bf16 v[92:95], a[196:199], v[176:179], v[92:95]\n"
"s_waitcnt vmcnt(14)\n"
"s_barrier\n"
"s_cmp_lg_u32 s83, 0\n"
"s_cbranch_scc1 .LSIG_O\n"
"s_mov_b64 exec, 1\n"
"v_mov_b32 v31, 1\n"
"buffer_atomic_add v31, off, s[24:27], s51 sc1\n"
"s_mov_b64 exec, -1\n"
".LSIG_O:\n"
"v_mfma_f32_16x16x32_bf16 v[92:95], a[204:207], v[180:183], v[92:95]\n"
"s_waitcnt lgkmcnt(2)\n"
"ds_read_b128 v[160:163], v2 offset:0\n"
"v_mfma_f32_16x16x32_bf16 v[92:95], a[212:215], v[184:187], v[92:95]\n"
"buffer_load_dwordx4 v30, s[28:31], s52 offen offset:2048 sc1 lds\n"
"v_mfma_f32_16x16x32_bf16 v[92:95], a[220:223], v[188:191], v[92:95]\n"
"ds_read_b128 v[164:167], v2 offset:9216\n"
"v_mfma_f32_16x16x32_bf16 v[88:91], a[216:219], v[188:191], v[88:91]\n"
"v_mfma_f32_16x16x32_bf16 v[88:91], a[208:211], v[184:187], v[88:91]\n"
"ds_read_b128 v[168:171], v2 offset:18432\n"
"v_mfma_f32_16x16x32_bf16 v[88:91], a[200:203], v[180:183], v[88:91]\n"
"buffer_load_dwordx4 v30, s[28:31], s52 offen offset:3072 sc1 lds\n"
"v_mfma_f32_16x16x32_bf16 v[88:91], a[192:195], v[176:179], v[88:91]\n"
"ds_read_b128 v[172:175], v2 offset:27648\n"
"v_mfma_f32_16x16x32_bf16 v[96:99], a[192:195], v[192:195], v[96:99]\n"
"v_mfma_f32_16x16x32_bf16 v[96:99], a[200:203], v[196:199], v[96:99]\n"
"s_waitcnt lgkmcnt(2)\n"
"s_add_u32 m0, s84, 45056\n"
"ds_read_b128 v[176:179], v2 offset:1024\n"
"v_mfma_f32_16x16x32_bf16 v[96:99], a[208:211], v[200:203], v[96:99]\n"
"s_mov_b32 s52, 266240\n"
"buffer_load_dwordx4 v30, s[28:31], s52 offen offset:0 sc1 lds\n"
"v_mfma_f32_16x16x32_bf16 v[96:99], a[216:219], v[204:207], v[96:99]\n"
"ds_read_b128 v[180:183], v2 offset:10240\n"
"v_mfma_f32_16x16x32_bf16 v[100:103], a[220:223], v[204:207], v[100:103]\n"
"v_mfma_f32_16x16x32_bf16 v[100:103], a[212:215], v[200:203], v[100:103]\n"
"ds_read_b128 v[184:187], v2 offset:19456\n"
"v_mfma_f32_16x16x32_bf16 v[100:103], a[204:207], v[196:199], v[100:103]\n"
"v_mfma_f32_16x16x32_bf16 v[100:103], a[196:199], v[192:195], v[100:103]\n"
"s_waitcnt vmcnt(9)\n"
"ds_read_b128 v[188:191], v2 offset:28672\n"
"v_mfma_f32_16x16x32_bf16 v[32:35], a[128:131], v[160:163], v[32:35]\n"
"v_mfma_f32_16x16x32_bf16 v[32:35], a[136:139], v[164:167], v[32:35]\n"
"s_waitcnt lgkmcnt(2)\n"
"ds_read_b128 v[192:195], v2 offset:2048\n"
"v_mfma_f32_16x16x32_bf16 v[32:35], a[144:147], v[168:171], v[32:35]\n"
"s_mov_b32 s53, 57344\n"
"buffer_load_dwordx4 a[160:163], v3, s[44:47], s53 offen offset:0 sc1\n"
"v_mfma_f32_16x16x32_bf16 v[32:35], a[152:155], v[172:175], v[32:35]\n"
"ds_read_b128 v[196:199], v2 offset:11264\n"
"v_mfma_f32_16x16x32_bf16 v[36:39], a[156:159], v[172:175], v[36:39]\n"
"v_mfma_f32_16x16x32_bf16 v[36:39], a[148:151], v[168:171], v[36:39]\n"
"ds_read_b128 v[200:203], v2 offset:20480\n"
"v_mfma_f32_16x16x32_bf16 v[36:39], a[140:143], v[164:167], v[36:39]\n"
"buffer_load_dwordx4 a[164:167], v3, s[44:47], s53 offen offset:1024 sc1\n"
"v_mfma_f32_16x16x32_bf16 v[36:39], a[132:135], v[160:163], v[36:39]\n"
"ds_read_b128 v[204:207], v2 offset:29696\n"
"v_mfma_f32_16x16x32_bf16 v[44:47], a[132:135], v[176:179], v[44:47]\n"
"v_mfma_f32_16x16x32_bf16 v[44:47], a[140:143], v[180:183], v[44:47]\n"
"s_waitcnt lgkmcnt(2)\n"
"ds_read_b128 v[160:163], v2 offset:3072\n"
"v_mfma_f32_16x16x32_bf16 v[44:47], a[148:151], v[184:187], v[44:47]\n"
"buffer_load_dwordx4 a[168:171], v3, s[44:47], s53 offen offset:2048 sc1\n"
"v_mfma_f32_16x16x32_bf16 v[44:47], a[156:159], v[188:191], v[44:47]\n"
"ds_read_b128 v[164:167], v2 offset:12288\n"
"v_mfma_f32_16x16x32_bf16 v[40:43], a[152:155], v[188:191], v[40:43]\n"
"v_mfma_f32_16x16x32_bf16 v[40:43], a[144:147], v[184:187], v[40:43]\n"
"ds_read_b128 v[168:171], v2 offset:21504\n"
"v_mfma_f32_16x16x32_bf16 v[40:43], a[136:139], v[180:183], v[40:43]\n"
"buffer_load_dwordx4 a[172:175], v3, s[44:47], s53 offen offset:3072 sc1\n"
"v_mfma_f32_16x16x32_bf16 v[40:43], a[128:131], v[176:179], v[40:43]\n"
"ds_read_b128 v[172:175], v2 offset:30720\n"
"v_mfma_f32_16x16x32_bf16 v[48:51], a[128:131], v[192:195], v[48:51]\n"
"v_mfma_f32_16x16x32_bf16 v[48:51], a[136:139], v[196:199], v[48:51]\n"
"s_waitcnt lgkmcnt(2)\n"
"ds_read_b128 v[176:179], v2 offset:4096\n"
"v_mfma_f32_16x16x32_bf16 v[48:51], a[144:147], v[200:203], v[48:51]\n"
"s_mov_b32 s53, 61440\n"
"buffer_load_dwordx4 a[176:179], v3, s[44:47], s53 offen offset:0 sc1\n"
"v_mfma_f32_16x16x32_bf16 v[48:51], a[152:155], v[204:207], v[48:51]\n"
"ds_read_b128 v[180:183], v2 offset:13312\n"
"v_mfma_f32_16x16x32_bf16 v[52:55], a[156:159], v[204:207], v[52:55]\n"
"v_mfma_f32_16x16x32_bf16 v[52:55], a[148:151], v[200:203], v[52:55]\n"
"ds_read_b128 v[184:187], v2 offset:22528\n"
"v_mfma_f32_16x16x32_bf16 v[52:55], a[140:143], v[196:199], v[52:55]\n"
"buffer_load_dwordx4 a[180:183], v3, s[44:47], s53 offen offset:1024 sc1\n"
"v_mfma_f32_16x16x32_bf16 v[52:55], a[132:135], v[192:195], v[52:55]\n"
"ds_read_b128 v[188:191], v2 offset:31744\n"
"v_mfma_f32_16x16x32_bf16 v[60:63], a[132:135], v[160:163], v[60:63]\n"
"v_mfma_f32_16x16x32_bf16 v[60:63], a[140:143], v[164:167], v[60:63]\n"
"s_waitcnt lgkmcnt(2)\n"
"ds_read_b128 v[192:195], v2 offset:5120\n"
"v_mfma_f32_16x16x32_bf16 v[60:63], a[148:151], v[168:171], v[60:63]\n"
"buffer_load_dwordx4 a[184:187], v3, s[44:47], s53 offen offset:2048 sc1\n"
"v_mfma_f32_16x16x32_bf16 v[60:63], a[156:159], v[172:175], v[60:63]\n"
"ds_read_b128 v[196:199], v2 offset:14336\n"
"v_mfma_f32_16x16x32_bf16 v[56:59], a[152:155], v[172:175], v[56:59]\n"
"v_mfma_f32_16x16x32_bf16 v[56:59], a[144:147], v[168:171], v[56:59]\n"
"ds_read_b128 v[200:203], v2 offset:23552\n"
"v_mfma_f32_16x16x32_bf16 v[56:59], a[136:139], v[164:167], v[56:59]\n"
"buffer_load_dwordx4 a[188:191], v3, s[44:47], s53 offen offset:3072 sc1\n"
"v_mfma_f32_16x16x32_bf16 v[56:59], a[128:131], v[160:163], v[56:59]\n"
"ds_read_b128 v[204:207], v2 offset:32768\n"
"v_mfma_f32_16x16x32_bf16 v[64:67], a[128:131], v[176:179], v[64:67]\n"
"v_mfma_f32_16x16x32_bf16 v[64:67], a[136:139], v[180:183], v[64:67]\n"
"s_waitcnt lgkmcnt(2)\n"
"s_add_u32 m0, s84, 90112\n"
"ds_read_b128 v[160:163], v2 offset:6144\n"
"v_mfma_f32_16x16x32_bf16 v[64:67], a[144:147], v[184:187], v[64:67]\n"
"s_mov_b32 s52, 294912\n"
"buffer_load_dwordx4 v30, s[28:31], s52 offen offset:0 sc1 lds\n"
"v_mfma_f32_16x16x32_bf16 v[64:67], a[152:155], v[188:191], v[64:67]\n"
"ds_read_b128 v[164:167], v2 offset:15360\n"
"v_mfma_f32_16x16x32_bf16 v[68:71], a[156:159], v[188:191], v[68:71]\n"
"v_mfma_f32_16x16x32_bf16 v[68:71], a[148:151], v[184:187], v[68:71]\n"
"ds_read_b128 v[168:171], v2 offset:24576\n"
"v_mfma_f32_16x16x32_bf16 v[68:71], a[140:143], v[180:183], v[68:71]\n"
"buffer_load_dwordx4 v30, s[28:31], s52 offen offset:1024 sc1 lds\n"
"v_mfma_f32_16x16x32_bf16 v[68:71], a[132:135], v[176:179], v[68:71]\n"
"ds_read_b128 v[172:175], v2 offset:33792\n"
"v_mfma_f32_16x16x32_bf16 v[76:79], a[132:135], v[192:195], v[76:79]\n"
"v_mfma_f32_16x16x32_bf16 v[76:79], a[140:143], v[196:199], v[76:79]\n"
"s_waitcnt lgkmcnt(2)\n"
"ds_read_b128 v[176:179], v2 offset:7168\n"
"v_mfma_f32_16x16x32_bf16 v[76:79], a[148:151], v[200:203], v[76:79]\n"
"buffer_load_dwordx4 v30, s[28:31], s52 offen offset:2048 sc1 lds\n"
"v_mfma_f32_16x16x32_bf16 v[76:79], a[156:159], v[204:207], v[76:79]\n"
"ds_read_b128 v[180:183], v2 offset:16384\n"
"v_mfma_f32_16x16x32_bf16 v[72:75], a[152:155], v[204:207], v[72:75]\n"
"v_mfma_f32_16x16x32_bf16 v[72:75], a[144:147], v[200:203], v[72:75]\n"
"ds_read_b128 v[184:187], v2 offset:25600\n"
"v_mfma_f32_16x16x32_bf16 v[72:75], a[136:139], v[196:199], v[72:75]\n"
"buffer_load_dwordx4 v30, s[28:31], s52 offen offset:3072 sc1 lds\n"
"v_mfma_f32_16x16x32_bf16 v[72:75], a[128:131], v[192:195], v[72:75]\n"
"ds_read_b128 v[188:191], v2 offset:34816\n"
"v_mfma_f32_16x16x32_bf16 v[80:83], a[128:131], v[160:163], v[80:83]\n"
"v_mfma_f32_16x16x32_bf16 v[80:83], a[136:139], v[164:167], v[80:83]\n"
"s_waitcnt lgkmcnt(2)\n"
"s_add_u32 m0, s84, 94208\n"
"ds_read_b128 v[192:195], v2 offset:8192\n"
"v_mfma_f32_16x16x32_bf16 v[80:83], a[144:147], v[168:171], v[80:83]\n"
"s_mov_b32 s52, 299008\n"
"buffer_load_dwordx4 v30, s[28:31], s52 offen offset:0 sc1 lds\n"
"v_mfma_f32_16x16x32_bf16 v[80:83], a[152:155], v[172:175], v[80:83]\n"
"ds_read_b128 v[196:199], v2 offset:17408\n"
"v_mfma_f32_16x16x32_bf16 v[84:87], a[156:159], v[172:175], v[84:87]\n"
"v_mfma_f32_16x16x32_bf16 v[84:87], a[148:151], v[168:171], v[84:87]\n"
"ds_read_b128 v[200:203], v2 offset:26624\n"
"v_mfma_f32_16x16x32_bf16 v[84:87], a[140:143], v[164:167], v[84:87]\n"
"buffer_load_dwordx4 v30, s[28:31], s52 offen offset:1024 sc1 lds\n"
"v_mfma_f32_16x16x32_bf16 v[84:87], a[132:135], v[160:163], v[84:87]\n"
"ds_read_b128 v[204:207], v2 offset:35840\n"
"v_add_u32 v2, 0x9000, v2\n"
"v_mfma_f32_16x16x32_bf16 v[92:95], a[132:135], v[176:179], v[92:95]\n"
"s_waitcnt vmcnt(14)\n"
"s_barrier\n"
"v_mfma_f32_16x16x32_bf16 v[92:95], a[140:143], v[180:183], v[92:95]\n"
"s_waitcnt lgkmcnt(2)\n"
"ds_read_b128 v[160:163], v2 offset:0\n"
"v_mfma_f32_16x16x32_bf16 v[92:95], a[148:151], v[184:187], v[92:95]\n"
"buffer_load_dwordx4 v30, s[28:31], s52 offen offset:2048 sc1 lds\n"
"v_mfma_f32_16x16x32_bf16 v[92:95], a[156:159], v[188:191], v[92:95]\n"
"ds_read_b128 v[164:167], v2 offset:9216\n"
"v_mfma_f32_16x16x32_bf16 v[88:91], a[152:155], v[188:191], v[88:91]\n"
"v_mfma_f32_16x16x32_bf16 v[88:91], a[144:147], v[184:187], v[88:91]\n"
"ds_read_b128 v[168:171], v2 offset:18432\n"
"v_mfma_f32_16x16x32_bf16 v[88:91], a[136:139], v[180:183], v[88:91]\n"
"buffer_load_dwordx4 v30, s[28:31], s52 offen offset:3072 sc1 lds\n"
"v_mfma_f32_16x16x32_bf16 v[88:91], a[128:131], v[176:179], v[88:91]\n"
"ds_read_b128 v[172:175], v2 offset:27648\n"
"v_mfma_f32_16x16x32_bf16 v[96:99], a[128:131], v[192:195], v[96:99]\n"
"v_mfma_f32_16x16x32_bf16 v[96:99], a[136:139], v[196:199], v[96:99]\n"
"s_waitcnt lgkmcnt(2)\n"
"s_add_u32 m0, s84, 98304\n"
"ds_read_b128 v[176:179], v2 offset:1024\n"
"v_mfma_f32_16x16x32_bf16 v[96:99], a[144:147], v[200:203], v[96:99]\n"
"s_mov_b32 s52, 303104\n"
"buffer_load_dwordx4 v30, s[28:31], s52 offen offset:0 sc1 lds\n"
"v_mfma_f32_16x16x32_bf16 v[96:99], a[152:155], v[204:207], v[96:99]\n"
"ds_read_b128 v[180:183], v2 offset:10240\n"
"v_mfma_f32_16x16x32_bf16 v[100:103], a[156:159], v[204:207], v[100:103]\n"
"v_mfma_f32_16x16x32_bf16 v[100:103], a[148:151], v[200:203], v[100:103]\n"
"ds_read_b128 v[184:187], v2 offset:19456\n"
"v_mfma_f32_16x16x32_bf16 v[100:103], a[140:143], v[196:199], v[100:103]\n"
"v_mfma_f32_16x16x32_bf16 v[100:103], a[132:135], v[192:195], v[100:103]\n"
"s_waitcnt vmcnt(9)\n"
"ds_read_b128 v[188:191], v2 offset:28672\n"
"v_mfma_f32_16x16x32_bf16 v[32:35], a[160:163], v[160:163], v[32:35]\n"
"v_mfma_f32_16x16x32_bf16 v[32:35], a[168:171], v[164:167], v[32:35]\n"
"s_waitcnt lgkmcnt(2)\n"
"ds_read_b128 v[192:195], v2 offset:2048\n"
"v_mfma_f32_16x16x32_bf16 v[32:35], a[176:179], v[168:171], v[32:35]\n"
"s_mov_b32 s53, 65536\n"
"buffer_load_dwordx4 a[192:195], v3, s[44:47], s53 offen offset:0 sc1\n"
"v_mfma_f32_16x16x32_bf16 v[32:35], a[184:187], v[172:175], v[32:35]\n"
"ds_read_b128 v[196:199], v2 offset:11264\n"
"v_mfma_f32_16x16x32_bf16 v[36:39], a[188:191], v[172:175], v[36:39]\n"
"buffer_load_dwordx4 a[196:199], v3, s[44:47], s53 offen offset:1024 sc1\n"
"v_mfma_f32_16x16x32_bf16 v[36:39], a[180:183], v[168:171], v[36:39]\n"
"ds_read_b128 v[200:203], v2 offset:20480\n"
"v_mfma_f32_16x16x32_bf16 v[36:39], a[172:175], v[164:167], v[36:39]\n"
"buffer_load_dwordx4 a[200:203], v3, s[44:47], s53 offen offset:2048 sc1\n"
"v_mfma_f32_16x16x32_bf16 v[36:39], a[164:167], v[160:163], v[36:39]\n"
"ds_read_b128 v[204:207], v2 offset:29696\n"
"v_mfma_f32_16x16x32_bf16 v[44:47], a[164:167], v[176:179], v[44:47]\n"
"v_mfma_f32_16x16x32_bf16 v[44:47], a[172:175], v[180:183], v[44:47]\n"
"s_waitcnt lgkmcnt(2)\n"
"ds_read_b128 v[160:163], v2 offset:3072\n"
"v_mfma_f32_16x16x32_bf16 v[44:47], a[180:183], v[184:187], v[44:47]\n"
"buffer_load_dwordx4 a[204:207], v3, s[44:47], s53 offen offset:3072 sc1\n"
"v_mfma_f32_16x16x32_bf16 v[44:47], a[188:191], v[188:191], v[44:47]\n"
"ds_read_b128 v[164:167], v2 offset:12288\n"
"v_mfma_f32_16x16x32_bf16 v[40:43], a[184:187], v[188:191], v[40:43]\n"
"s_mov_b32 s53, 69632\n"
"buffer_load_dwordx2 a[208:209], v29, s[44:47], s53 offen offset:0 sc1\n"
"v_mfma_f32_16x16x32_bf16 v[40:43], a[176:179], v[184:187], v[40:43]\n"
"ds_read_b128 v[168:171], v2 offset:21504\n"
"v_mfma_f32_16x16x32_bf16 v[40:43], a[168:171], v[180:183], v[40:43]\n"
"buffer_load_dwordx2 a[210:211], v29, s[44:47], s53 offen offset:512 sc1\n"
"v_mfma_f32_16x16x32_bf16 v[40:43], a[160:163], v[176:179], v[40:43]\n"
"ds_read_b128 v[172:175], v2 offset:30720\n"
"v_mfma_f32_16x16x32_bf16 v[48:51], a[160:163], v[192:195], v[48:51]\n"
"v_mfma_f32_16x16x32_bf16 v[48:51], a[168:171], v[196:199], v[48:51]\n"
"s_waitcnt lgkmcnt(2)\n"
"ds_read_b128 v[176:179], v2 offset:4096\n"
"v_mfma_f32_16x16x32_bf16 v[48:51], a[176:179], v[200:203], v[48:51]\n"
"buffer_load_dwordx2 a[212:213], v29, s[44:47], s53 offen offset:1024 sc1\n"
"v_mfma_f32_16x16x32_bf16 v[48:51], a[184:187], v[204:207], v[48:51]\n"
"ds_read_b128 v[180:183], v2 offset:13312\n"
"v_mfma_f32_16x16x32_bf16 v[52:55], a[188:191], v[204:207], v[52:55]\n"
"buffer_load_dwordx2 a[214:215], v29, s[44:47], s53 offen offset:1536 sc1\n"
"v_mfma_f32_16x16x32_bf16 v[52:55], a[180:183], v[200:203], v[52:55]\n"
"ds_read_b128 v[184:187], v2 offset:22528\n"
"v_mfma_f32_16x16x32_bf16 v[52:55], a[172:175], v[196:199], v[52:55]\n"
"buffer_load_dwordx2 a[216:217], v29, s[44:47], s53 offen offset:2048 sc1\n"
"v_mfma_f32_16x16x32_bf16 v[52:55], a[164:167], v[192:195], v[52:55]\n"
"ds_read_b128 v[188:191], v2 offset:31744\n"
"v_mfma_f32_16x16x32_bf16 v[60:63], a[164:167], v[160:163], v[60:63]\n"
"v_mfma_f32_16x16x32_bf16 v[60:63], a[172:175], v[164:167], v[60:63]\n"
"s_waitcnt lgkmcnt(2)\n"
"ds_read_b128 v[192:195], v2 offset:5120\n"
"v_mfma_f32_16x16x32_bf16 v[60:63], a[180:183], v[168:171], v[60:63]\n"
"buffer_load_dwordx2 a[218:219], v29, s[44:47], s53 offen offset:2560 sc1\n"
"v_mfma_f32_16x16x32_bf16 v[60:63], a[188:191], v[172:175], v[60:63]\n"
"ds_read_b128 v[196:199], v2 offset:14336\n"
"v_mfma_f32_16x16x32_bf16 v[56:59], a[184:187], v[172:175], v[56:59]\n"
"buffer_load_dwordx2 a[220:221], v29, s[44:47], s53 offen offset:3072 sc1\n"
"v_mfma_f32_16x16x32_bf16 v[56:59], a[176:179], v[168:171], v[56:59]\n"
"ds_read_b128 v[200:203], v2 offset:23552\n"
"v_mfma_f32_16x16x32_bf16 v[56:59], a[168:171], v[164:167], v[56:59]\n"
"buffer_load_dwordx2 a[222:223], v29, s[44:47], s53 offen offset:3584 sc1\n"
"v_mfma_f32_16x16x32_bf16 v[56:59], a[160:163], v[160:163], v[56:59]\n"
"ds_read_b128 v[204:207], v2 offset:32768\n"
"v_mfma_f32_16x16x32_bf16 v[64:67], a[160:163], v[176:179], v[64:67]\n"
"v_mfma_f32_16x16x32_bf16 v[64:67], a[168:171], v[180:183], v[64:67]\n"
"s_waitcnt lgkmcnt(2)\n"
"ds_read_b128 v[160:163], v2 offset:6144\n"
"v_mfma_f32_16x16x32_bf16 v[64:67], a[176:179], v[184:187], v[64:67]\n"
"buffer_load_dword v253, off, s[24:27], s51 sc1\n"
"v_mfma_f32_16x16x32_bf16 v[64:67], a[184:187], v[188:191], v[64:67]\n"
"ds_read_b128 v[164:167], v2 offset:15360\n"
"v_mfma_f32_16x16x32_bf16 v[68:71], a[188:191], v[188:191], v[68:71]\n"
"v_mfma_f32_16x16x32_bf16 v[68:71], a[180:183], v[184:187], v[68:71]\n"
"s_add_u32 m0, s84, 126976\n"
"ds_read_b128 v[168:171], v2 offset:24576\n"
"v_mfma_f32_16x16x32_bf16 v[68:71], a[172:175], v[180:183], v[68:71]\n"
"s_mov_b32 s52, 331776\n"
"buffer_load_dwordx4 v30, s[28:31], s52 offen offset:0 sc1 lds\n"
"v_mfma_f32_16x16x32_bf16 v[68:71], a[164:167], v[176:179], v[68:71]\n"
"ds_read_b128 v[172:175], v2 offset:33792\n"
"v_mfma_f32_16x16x32_bf16 v[76:79], a[164:167], v[192:195], v[76:79]\n"
"v_mfma_f32_16x16x32_bf16 v[76:79], a[172:175], v[196:199], v[76:79]\n"
"s_waitcnt lgkmcnt(2)\n"
"ds_read_b128 v[176:179], v2 offset:7168\n"
"v_mfma_f32_16x16x32_bf16 v[76:79], a[180:183], v[200:203], v[76:79]\n"
"buffer_load_dwordx4 v30, s[28:31], s52 offen offset:1024 sc1 lds\n"
"v_mfma_f32_16x16x32_bf16 v[76:79], a[188:191], v[204:207], v[76:79]\n"
"ds_read_b128 v[180:183], v2 offset:16384\n"
"v_mfma_f32_16x16x32_bf16 v[72:75], a[184:187], v[204:207], v[72:75]\n"
"v_mfma_f32_16x16x32_bf16 v[72:75], a[176:179], v[200:203], v[72:75]\n"
"ds_read_b128 v[184:187], v2 offset:25600\n"
"v_mfma_f32_16x16x32_bf16 v[72:75], a[168:171], v[196:199], v[72:75]\n"
"buffer_load_dwordx4 v30, s[28:31], s52 offen offset:2048 sc1 lds\n"
"v_mfma_f32_16x16x32_bf16 v[72:75], a[160:163], v[192:195], v[72:75]\n"
"ds_read_b128 v[188:191], v2 offset:34816\n"
"v_mfma_f32_16x16x32_bf16 v[80:83], a[160:163], v[160:163], v[80:83]\n"
"v_mfma_f32_16x16x32_bf16 v[80:83], a[168:171], v[164:167], v[80:83]\n"
"s_waitcnt lgkmcnt(2)\n"
"ds_read_b128 v[192:195], v2 offset:8192\n"
"v_mfma_f32_16x16x32_bf16 v[80:83], a[176:179], v[168:171], v[80:83]\n"
"buffer_load_dwordx4 v30, s[28:31], s52 offen offset:3072 sc1 lds\n"
"v_mfma_f32_16x16x32_bf16 v[80:83], a[184:187], v[172:175], v[80:83]\n"
"ds_read_b128 v[196:199], v2 offset:17408\n"
"v_mfma_f32_16x16x32_bf16 v[84:87], a[188:191], v[172:175], v[84:87]\n"
"v_mfma_f32_16x16x32_bf16 v[84:87], a[180:183], v[168:171], v[84:87]\n"
"s_add_u32 m0, s84, 131072\n"
"ds_read_b128 v[200:203], v2 offset:26624\n"
"v_mfma_f32_16x16x32_bf16 v[84:87], a[172:175], v[164:167], v[84:87]\n"
"s_mov_b32 s52, 335872\n"
"buffer_load_dwordx4 v30, s[28:31], s52 offen offset:0 sc1 lds\n"
"v_mfma_f32_16x16x32_bf16 v[84:87], a[164:167], v[160:163], v[84:87]\n"
"ds_read_b128 v[204:207], v2 offset:35840\n"
"v_add_u32 v2, 0xd000, v2\n"
"v_mfma_f32_16x16x32_bf16 v[92:95], a[164:167], v[176:179], v[92:95]\n"
"s_waitcnt vmcnt(18)\n"
"s_barrier\n"
"v_mfma_f32_16x16x32_bf16 v[92:95], a[172:175], v[180:183], v[92:95]\n"
"s_waitcnt lgkmcnt(2)\n"
"ds_read_b128 v[160:163], v2 offset:0\n"
"v_mfma_f32_16x16x32_bf16 v[92:95], a[180:183], v[184:187], v[92:95]\n"
"buffer_load_dwordx4 v30, s[28:31], s52 offen offset:1024 sc1 lds\n"
"v_mfma_f32_16x16x32_bf16 v[92:95], a[188:191], v[188:191], v[92:95]\n"
"ds_read_b128 v[164:167], v2 offset:9216\n"
"v_mfma_f32_16x16x32_bf16 v[88:91], a[184:187], v[188:191], v[88:91]\n"
"v_mfma_f32_16x16x32_bf16 v[88:91], a[176:179], v[184:187], v[88:91]\n"
"ds_read_b128 v[168:171], v2 offset:18432\n"
"v_mfma_f32_16x16x32_bf16 v[88:91], a[168:171], v[180:183], v[88:91]\n"
"buffer_load_dwordx4 v30, s[28:31], s52 offen offset:2048 sc1 lds\n"
"v_mfma_f32_16x16x32_bf16 v[88:91], a[160:163], v[176:179], v[88:91]\n"
"ds_read_b128 v[172:175], v2 offset:27648\n"
"v_mfma_f32_16x16x32_bf16 v[96:99], a[160:163], v[192:195], v[96:99]\n"
"v_mfma_f32_16x16x32_bf16 v[96:99], a[168:171], v[196:199], v[96:99]\n"
"s_waitcnt lgkmcnt(2)\n"
"ds_read_b128 v[176:179], v2 offset:1024\n"
"v_mfma_f32_16x16x32_bf16 v[96:99], a[176:179], v[200:203], v[96:99]\n"
"buffer_load_dwordx4 v30, s[28:31], s52 offen offset:3072 sc1 lds\n"
"v_mfma_f32_16x16x32_bf16 v[96:99], a[184:187], v[204:207], v[96:99]\n"
"ds_read_b128 v[180:183], v2 offset:10240\n"
"v_mfma_f32_16x16x32_bf16 v[100:103], a[188:191], v[204:207], v[100:103]\n"
"v_mfma_f32_16x16x32_bf16 v[100:103], a[180:183], v[200:203], v[100:103]\n"
"s_add_u32 m0, s84, 135168\n"
"ds_read_b128 v[184:187], v2 offset:19456\n"
"v_mfma_f32_16x16x32_bf16 v[100:103], a[172:175], v[196:199], v[100:103]\n"
"s_mov_b32 s52, 339968\n"
"buffer_load_dwordx4 v30, s[28:31], s52 offen offset:0 sc1 lds\n"
"v_mfma_f32_16x16x32_bf16 v[100:103], a[164:167], v[192:195], v[100:103]\n"
"s_waitcnt vmcnt(10)\n"
"ds_read_b128 v[188:191], v2 offset:28672\n"
"v_mfma_f32_16x16x32_bf16 v[32:35], a[192:195], v[160:163], v[32:35]\n"
"s_waitcnt vmcnt(9)\n"
"v_readfirstlane_b32 s54, v253\n"
"s_nop 0\n"
"s_cmp_lt_u32 s54, 8\n"
"s_cbranch_scc1 .LPO_SLOW\n"
".LPO_BACK:\n"
"v_mfma_f32_16x16x32_bf16 v[32:35], a[200:203], v[164:167], v[32:35]\n"
"s_waitcnt lgkmcnt(2)\n"
"ds_read_b128 v[192:195], v2 offset:2048\n"
"v_mfma_f32_16x16x32_bf16 v[32:35], a[208:211], v[168:171], v[32:35]\n"
"s_mov_b32 s53, 73728\n"
"buffer_load_dwordx4 a[128:131], v3, s[44:47], s53 offen offset:0 sc1\n"
"v_mfma_f32_16x16x32_bf16 v[32:35], a[216:219], v[172:175], v[32:35]\n"
"ds_read_b128 v[196:199], v2 offset:11264\n"
"v_mfma_f32_16x16x32_bf16 v[36:39], a[220:223], v[172:175], v[36:39]\n"
"v_mfma_f32_16x16x32_bf16 v[36:39], a[212:215], v[168:171], v[36:39]\n"
"ds_read_b128 v[200:203], v2 offset:20480\n"
"v_mfma_f32_16x16x32_bf16 v[36:39], a[204:207], v[164:167], v[36:39]\n"
"buffer_load_dwordx4 a[132:135], v3, s[44:47], s53 offen offset:1024 sc1\n"
"v_mfma_f32_16x16x32_bf16 v[36:39], a[196:199], v[160:163], v[36:39]\n"
"ds_read_b128 v[204:207], v2 offset:29696\n"
"v_mfma_f32_16x16x32_bf16 v[44:47], a[196:199], v[176:179], v[44:47]\n"
"v_mfma_f32_16x16x32_bf16 v[44:47], a[204:207], v[180:183], v[44:47]\n"
"s_waitcnt lgkmcnt(2)\n"
"ds_read_b128 v[160:163], v2 offset:3072\n"
"v_mfma_f32_16x16x32_bf16 v[44:47], a[212:215], v[184:187], v[44:47]\n"
"buffer_load_dwordx4 a[136:139], v3, s[44:47], s53 offen offset:2048 sc1\n"
"v_mfma_f32_16x16x32_bf16 v[44:47], a[220:223], v[188:191], v[44:47]\n"
"ds_read_b128 v[164:167], v2 offset:12288\n"
"v_mfma_f32_16x16x32_bf16 v[40:43], a[216:219], v[188:191], v[40:43]\n"
"v_mfma_f32_16x16x32_bf16 v[40:43], a[208:211], v[184:187], v[40:43]\n"
"ds_read_b128 v[168:171], v2 offset:21504\n"
"v_mfma_f32_16x16x32_bf16 v[40:43], a[200:203], v[180:183], v[40:43]\n"
"buffer_load_dwordx4 a[140:143], v3, s[44:47], s53 offen offset:3072 sc1\n"
"v_mfma_f32_16x16x32_bf16 v[40:43], a[192:195], v[176:179], v[40:43]\n"
"ds_read_b128 v[172:175], v2 offset:30720\n"
"v_mfma_f32_16x16x32_bf16 v[48:51], a[192:195], v[192:195], v[48:51]\n"
"v_mfma_f32_16x16x32_bf16 v[48:51], a[200:203], v[196:199], v[48:51]\n"
"s_waitcnt lgkmcnt(2)\n"
"ds_read_b128 v[176:179], v2 offset:4096\n"
"v_mfma_f32_16x16x32_bf16 v[48:51], a[208:211], v[200:203], v[48:51]\n"
"s_mov_b32 s53, 77824\n"
"buffer_load_dwordx4 a[144:147], v3, s[44:47], s53 offen offset:0 sc1\n"
"v_mfma_f32_16x16x32_bf16 v[48:51], a[216:219], v[204:207], v[48:51]\n"
"ds_read_b128 v[180:183], v2 offset:13312\n"
"v_mfma_f32_16x16x32_bf16 v[52:55], a[220:223], v[204:207], v[52:55]\n"
"v_mfma_f32_16x16x32_bf16 v[52:55], a[212:215], v[200:203], v[52:55]\n"
"ds_read_b128 v[184:187], v2 offset:22528\n"
"v_mfma_f32_16x16x32_bf16 v[52:55], a[204:207], v[196:199], v[52:55]\n"
"buffer_load_dwordx4 a[148:151], v3, s[44:47], s53 offen offset:1024 sc1\n"
"v_mfma_f32_16x16x32_bf16 v[52:55], a[196:199], v[192:195], v[52:55]\n"
"ds_read_b128 v[188:191], v2 offset:31744\n"
"v_mfma_f32_16x16x32_bf16 v[60:63], a[196:199], v[160:163], v[60:63]\n"
"v_mfma_f32_16x16x32_bf16 v[60:63], a[204:207], v[164:167], v[60:63]\n"
"s_waitcnt lgkmcnt(2)\n"
"ds_read_b128 v[192:195], v2 offset:5120\n"
"v_mfma_f32_16x16x32_bf16 v[60:63], a[212:215], v[168:171], v[60:63]\n"
"buffer_load_dwordx4 a[152:155], v3, s[44:47], s53 offen offset:2048 sc1\n"
"v_mfma_f32_16x16x32_bf16 v[60:63], a[220:223], v[172:175], v[60:63]\n"
"ds_read_b128 v[196:199], v2 offset:14336\n"
"v_mfma_f32_16x16x32_bf16 v[56:59], a[216:219], v[172:175], v[56:59]\n"
"v_mfma_f32_16x16x32_bf16 v[56:59], a[208:211], v[168:171], v[56:59]\n"
"ds_read_b128 v[200:203], v2 offset:23552\n"
"v_mfma_f32_16x16x32_bf16 v[56:59], a[200:203], v[164:167], v[56:59]\n"
"buffer_load_dwordx4 a[156:159], v3, s[44:47], s53 offen offset:3072 sc1\n"
"v_mfma_f32_16x16x32_bf16 v[56:59], a[192:195], v[160:163], v[56:59]\n"
"ds_read_b128 v[204:207], v2 offset:32768\n"
"v_mfma_f32_16x16x32_bf16 v[64:67], a[192:195], v[176:179], v[64:67]\n"
"v_mfma_f32_16x16x32_bf16 v[64:67], a[200:203], v[180:183], v[64:67]\n"
"s_waitcnt lgkmcnt(2)\n"
"ds_read_b128 v[160:163], v2 offset:6144\n"
"v_mfma_f32_16x16x32_bf16 v[64:67], a[208:211], v[184:187], v[64:67]\n"
"buffer_load_dword v253, off, s[24:27], s8 sc1\n"
"v_mfma_f32_16x16x32_bf16 v[64:67], a[216:219], v[188:191], v[64:67]\n"
"ds_read_b128 v[164:167], v2 offset:15360\n"
"v_mfma_f32_16x16x32_bf16 v[68:71], a[220:223], v[188:191], v[68:71]\n"
"v_mfma_f32_16x16x32_bf16 v[68:71], a[212:215], v[184:187], v[68:71]\n"
"s_add_u32 m0, s84, 0\n"
"ds_read_b128 v[168:171], v2 offset:24576\n"
"v_mfma_f32_16x16x32_bf16 v[68:71], a[204:207], v[180:183], v[68:71]\n"
"s_mov_b32 s52, 368640\n"
"buffer_load_dwordx4 v30, s[28:31], s52 offen offset:0 sc1 lds\n"
"v_mfma_f32_16x16x32_bf16 v[68:71], a[196:199], v[176:179], v[68:71]\n"
"ds_read_b128 v[172:175], v2 offset:33792\n"
"v_mfma_f32_16x16x32_bf16 v[76:79], a[196:199], v[192:195], v[76:79]\n"
"v_mfma_f32_16x16x32_bf16 v[76:79], a[204:207], v[196:199], v[76:79]\n"
"s_waitcnt lgkmcnt(2)\n"
"ds_read_b128 v[176:179], v2 offset:7168\n"
"v_mfma_f32_16x16x32_bf16 v[76:79], a[212:215], v[200:203], v[76:79]\n"
"buffer_load_dwordx4 v30, s[28:31], s52 offen offset:1024 sc1 lds\n"
"v_mfma_f32_16x16x32_bf16 v[76:79], a[220:223], v[204:207], v[76:79]\n"
"ds_read_b128 v[180:183], v2 offset:16384\n"
"v_mfma_f32_16x16x32_bf16 v[72:75], a[216:219], v[204:207], v[72:75]\n"
"v_mfma_f32_16x16x32_bf16 v[72:75], a[208:211], v[200:203], v[72:75]\n"
"ds_read_b128 v[184:187], v2 offset:25600\n"
"v_mfma_f32_16x16x32_bf16 v[72:75], a[200:203], v[196:199], v[72:75]\n"
".LPAD14:\n"
"s_cbranch_execz .LPAD17\n"
"buffer_load_dwordx4 v30, s[28:31], s52 offen offset:2048 sc1 lds\n"
"v_mfma_f32_16x16x32_bf16 v[72:75], a[192:195], v[192:195], v[72:75]\n"
"ds_read_b128 v[188:191], v2 offset:34816\n"
"v_mfma_f32_16x16x32_bf16 v[80:83], a[192:195], v[160:163], v[80:83]\n"
"v_mfma_f32_16x16x32_bf16 v[80:83], a[200:203], v[164:167], v[80:83]\n"
"s_waitcnt lgkmcnt(2)\n"
"ds_read_b128 v[192:195], v2 offset:8192\n"
"v_mfma_f32_16x16x32_bf16 v[80:83], a[208:211], v[168:171], v[80:83]\n"
"buffer_load_dwordx4 v30, s[28:31], s52 offen offset:3072 sc1 lds\n"
"v_mfma_f32_16x16x32_bf16 v[80:83], a[216:219], v[172:175], v[80:83]\n"
"ds_read_b128 v[196:199], v2 offset:17408\n"
"v_mfma_f32_16x16x32_bf16 v[84:87], a[220:223], v[172:175], v[84:87]\n"
"v_mfma_f32_16x16x32_bf16 v[84:87], a[212:215], v[168:171], v[84:87]\n"
"s_add_u32 m0, s84, 4096\n"
"ds_read_b128 v[200:203], v2 offset:26624\n"
"v_mfma_f32_16x16x32_bf16 v[84:87], a[204:207], v[164:167], v[84:87]\n"
"s_mov_b32 s52, 372736\n"
"buffer_load_dwordx4 v30, s[28:31], s52 offen offset:0 sc1 lds\n"
"v_mfma_f32_16x16x32_bf16 v[84:87], a[196:199], v[160:163], v[84:87]\n"
"ds_read_b128 v[204:207], v2 offset:35840\n"
"v_add_u32 v2, 0x9000, v2\n"
"v_mfma_f32_16x16x32_bf16 v[92:95], a[196:199], v[176:179], v[92:95]\n"
"s_waitcnt vmcnt(14)\n"
"s_barrier\n"
"v_mfma_f32_16x16x32_bf16 v[92:95], a[204:207], v[180:183], v[92:95]\n"
"s_waitcnt lgkmcnt(2)\n"
"ds_read_b128 v[160:163], v2 offset:0\n"
"v_mfma_f32_16x16x32_bf16 v[92:95], a[212:215], v[184:187], v[92:95]\n"
"buffer_load_dwordx4 v30, s[28:31], s52 offen offset:1024 sc1 lds\n"
"v_mfma_f32_16x16x32_bf16 v[92:95], a[220:223], v[188:191], v[92:95]\n"
"ds_read_b128 v[164:167], v2 offset:9216\n"
"v_mfma_f32_16x16x32_bf16 v[88:91], a[216:219], v[188:191], v[88:91]\n"
"v_mfma_f32_16x16x32_bf16 v[88:91], a[208:211], v[184:187], v[88:91]\n"
"ds_read_b128 v[168:171], v2 offset:18432\n"
"v_mfma_f32_16x16x32_bf16 v[88:91], a[200:203], v[180:183], v[88:91]\n"
"buffer_load_dwordx4 v30, s[28:31], s52 offen offset:2048 sc1 lds\n"
"v_mfma_f32_16x16x32_bf16 v[88:91], a[192:195], v[176:179], v[88:91]\n"
"ds_read_b128 v[172:175], v2 offset:27648\n"
"v_mfma_f32_16x16x32_bf16 v[96:99], a[192:195], v[192:195], v[96:99]\n"
"v_mfma_f32_16x16x32_bf16 v[96:99], a[200:203], v[196:199], v[96:99]\n"
"s_waitcnt lgkmcnt(2)\n"
"ds_read_b128 v[176:179], v2 offset:1024\n"
"v_mfma_f32_16x16x32_bf16 v[96:99], a[208:211], v[200:203], v[96:99]\n"
"buffer_load_dwordx4 v30, s[28:31], s52 offen offset:3072 sc1 lds\n"
"v_mfma_f32_16x16x32_bf16 v[96:99], a[216:219], v[204:207], v[96:99]\n"
"ds_read_b128 v[180:183], v2 offset:10240\n"
"v_mfma_f32_16x16x32_bf16 v[100:103], a[220:223], v[204:207], v[100:103]\n"
"v_mfma_f32_16x16x32_bf16 v[100:103], a[212:215], v[200:203], v[100:103]\n"
"s_add_u32 m0, s84, 8192\n"
"ds_read_b128 v[184:187], v2 offset:19456\n"
"v_mfma_f32_16x16x32_bf16 v[100:103], a[204:207], v[196:199], v[100:103]\n"
"s_mov_b32 s52, 376832\n"
"buffer_load_dwordx4 v30, s[28:31], s52 offen offset:0 sc1 lds\n"
"v_mfma_f32_16x16x32_bf16 v[100:103], a[196:199], v[192:195], v[100:103]\n"
"s_waitcnt vmcnt(10)\n"
"ds_read_b128 v[188:191], v2 offset:28672\n"
"v_mfma_f32_16x16x32_bf16 v[104:107], a[128:131], v[160:163], 0\n"
"s_waitcnt vmcnt(9)\n"
"v_readfirstlane_b32 s54, v253\n"
"s_nop 0\n"
"s_cmp_lt_u32 s54, 4\n"
"s_cbranch_scc1 .LPG_SLOW\n"
".LPG_BACK:\n"
"v_mfma_f32_16x16x32_bf16 v[104:107], a[136:139], v[164:167], v[104:107]\n"
"s_waitcnt lgkmcnt(2)\n"
"ds_read_b128 v[192:195], v2 offset:2048\n"
"v_mfma_f32_16x16x32_bf16 v[104:107], a[144:147], v[168:171], v[104:107]\n"
"s_mov_b32 s53, 81920\n"
"buffer_load_dwordx4 a[160:163], v3, s[44:47], s53 offen offset:0 sc1\n"
"v_mfma_f32_16x16x32_bf16 v[104:107], a[152:155], v[172:175], v[104:107]\n"
"ds_read_b128 v[196:199], v2 offset:11264\n"
"v_mfma_f32_16x16x32_bf16 v[108:111], a[156:159], v[172:175], 0\n"
"v_mfma_f32_16x16x32_bf16 v[108:111], a[148:151], v[168:171], v[108:111]\n"
"ds_read_b128 v[200:203], v2 offset:20480\n"
"v_cvt_pk_bf16_f32 v8, v33, v37\n"
"v_mfma_f32_16x16x32_bf16 v[108:111], a[140:143], v[164:167], v[108:111]\n"
"buffer_load_dwordx4 a[164:167], v3, s[44:47], s53 offen offset:1024 sc1\n"
"v_mfma_f32_16x16x32_bf16 v[108:111], a[132:135], v[160:163], v[108:111]\n"
"ds_read_b128 v[204:207], v2 offset:29696\n"
"v_cvt_pk_bf16_f32 v9, v41, v45\n"
"v_mfma_f32_16x16x32_bf16 v[116:119], a[132:135], v[176:179], 0\n"
"v_cvt_pk_bf16_f32 v10, v49, v53\n"
"v_cvt_pk_bf16_f32 v11, v57, v61\n"
"v_mfma_f32_16x16x32_bf16 v[116:119], a[140:143], v[180:183], v[116:119]\n"
"s_waitcnt lgkmcnt(2)\n"
"ds_read_b128 v[160:163], v2 offset:3072\n"
"v_cvt_pk_bf16_f32 v12, v65, v69\n"
"v_mfma_f32_16x16x32_bf16 v[116:119], a[148:151], v[184:187], v[116:119]\n"
"buffer_load_dwordx4 a[168:171], v3, s[44:47], s53 offen offset:2048 sc1\n"
"v_mfma_f32_16x16x32_bf16 v[116:119], a[156:159], v[188:191], v[116:119]\n"
"ds_read_b128 v[164:167], v2 offset:12288\n"
"v_cvt_pk_bf16_f32 v13, v73, v77\n"
"v_mfma_f32_16x16x32_bf16 v[112:115], a[152:155], v[188:191], 0\n"
"v_cvt_pk_bf16_f32 v14, v81, v85\n"
"v_cvt_pk_bf16_f32 v15, v89, v93\n"
"v_mfma_f32_16x16x32_bf16 v[112:115], a[144:147], v[184:187], v[112:115]\n"
"ds_read_b128 v[168:171], v2 offset:21504\n"
"v_cvt_pk_bf16_f32 v16, v97, v101\n"
"v_mfma_f32_16x16x32_bf16 v[112:115], a[136:139], v[180:183], v[112:115]\n"
"buffer_load_dwordx4 a[172:175], v3, s[44:47], s53 offen offset:3072 sc1\n"
"v_mfma_f32_16x16x32_bf16 v[112:115], a[128:131], v[176:179], v[112:115]\n"
"ds_read_b128 v[172:175], v2 offset:30720\n"
"v_cvt_pk_bf16_f32 v20, v34, v38\n"
"v_mfma_f32_16x16x32_bf16 v[120:123], a[128:131], v[192:195], 0\n"
"v_cvt_pk_bf16_f32 v21, v42, v46\n"
"v_cvt_pk_bf16_f32 v22, v50, v54\n"
"v_mfma_f32_16x16x32_bf16 v[120:123], a[136:139], v[196:199], v[120:123]\n"
"s_waitcnt lgkmcnt(2)\n"
"ds_read_b128 v[176:179], v2 offset:4096\n"
"v_cvt_pk_bf16_f32 v23, v58, v62\n"
"v_mfma_f32_16x16x32_bf16 v[120:123], a[144:147], v[200:203], v[120:123]\n"
"s_mov_b32 s53, 86016\n"
"buffer_load_dwordx4 a[176:179], v3, s[44:47], s53 offen offset:0 sc1\n"
"v_mfma_f32_16x16x32_bf16 v[120:123], a[152:155], v[204:207], v[120:123]\n"
"ds_read_b128 v[180:183], v2 offset:13312\n"
"v_cvt_pk_bf16_f32 v24, v66, v70\n"
"v_mfma_f32_16x16x32_bf16 v[124:127], a[156:159], v[204:207], 0\n"
"v_cvt_pk_bf16_f32 v25, v74, v78\n"
"v_cvt_pk_bf16_f32 v26, v82, v86\n"
"v_mfma_f32_16x16x32_bf16 v[124:127], a[148:151], v[200:203], v[124:127]\n"
"ds_read_b128 v[184:187], v2 offset:22528\n"
"v_cvt_pk_bf16_f32 v27, v90, v94\n"
"v_mfma_f32_16x16x32_bf16 v[124:127], a[140:143], v[196:199], v[124:127]\n"
"buffer_load_dwordx4 a[180:183], v3, s[44:47], s53 offen offset:1024 sc1\n"
"v_mfma_f32_16x16x32_bf16 v[124:127], a[132:135], v[192:195], v[124:127]\n"
"ds_read_b128 v[188:191], v2 offset:31744\n"
"v_cvt_pk_bf16_f32 v28, v98, v102\n"
"v_mfma_f32_16x16x32_bf16 v[132:135], a[132:135], v[160:163], 0\n"
"v_cvt_pk_bf16_f32 v224, v35, v39\n"
"v_cvt_pk_bf16_f32 v225, v43, v47\n"
"v_mfma_f32_16x16x32_bf16 v[132:135], a[140:143], v[164:167], v[132:135]\n"
"s_waitcnt lgkmcnt(2)\n"
"ds_read_b128 v[192:195], v2 offset:5120\n"
"v_cvt_pk_bf16_f32 v226, v51, v55\n"
"v_mfma_f32_16x16x32_bf16 v[132:135], a[148:151], v[168:171], v[132:135]\n"
"buffer_load_dwordx4 a[184:187], v3, s[44:47], s53 offen offset:2048 sc1\n"
"v_mfma_f32_16x16x32_bf16 v[132:135], a[156:159], v[172:175], v[132:135]\n"
"ds_read_b128 v[196:199], v2 offset:14336\n"
"v_cvt_pk_bf16_f32 v227, v59, v63\n"
"v_mfma_f32_16x16x32_bf16 v[128:131], a[152:155], v[172:175], 0\n"
"v_cvt_pk_bf16_f32 v228, v67, v71\n"
"v_cvt_pk_bf16_f32 v229, v75, v79\n"
"v_mfma_f32_16x16x32_bf16 v[128:131], a[144:147], v[168:171], v[128:131]\n"
"ds_read_b128 v[200:203], v2 offset:23552\n"
"v_cvt_pk_bf16_f32 v230, v83, v87\n"
"v_mfma_f32_16x16x32_bf16 v[128:131], a[136:139], v[164:167], v[128:131]\n"
"buffer_load_dwordx4 a[188:191], v3, s[44:47], s53 offen offset:3072 sc1\n"
"v_mfma_f32_16x16x32_bf16 v[128:131], a[128:131], v[160:163], v[128:131]\n"
"ds_read_b128 v[204:207], v2 offset:32768\n"
"v_cvt_pk_bf16_f32 v231, v91, v95\n"
"v_mfma_f32_16x16x32_bf16 v[136:139], a[128:131], v[176:179], 0\n"
"v_cvt_pk_bf16_f32 v232, v99, v103\n"
"v_mov_b32 v33, v36\n"
"v_mfma_f32_16x16x32_bf16 v[136:139], a[136:139], v[180:183], v[136:139]\n"
"s_waitcnt lgkmcnt(2)\n"
"ds_read_b128 v[160:163], v2 offset:6144\n"
"v_mov_b32 v34, v40\n"
"v_mfma_f32_16x16x32_bf16 v[136:139], a[144:147], v[184:187], v[136:139]\n"
"s_add_u32 s54, s6, .LP3_W9-.LK_START\n"
"buffer_load_dword a255, off, s[12:15], s54\n"
"v_mfma_f32_16x16x32_bf16 v[136:139], a[152:155], v[188:191], v[136:139]\n"
"ds_read_b128 v[164:167], v2 offset:15360\n"
"v_mov_b32 v35, v44\n"
"v_mfma_f32_16x16x32_bf16 v[140:143], a[156:159], v[188:191], 0\n"
"v_mov_b32 v36, v48\n"
"v_mov_b32 v37, v52\n"
"v_mfma_f32_16x16x32_bf16 v[140:143], a[148:151], v[184:187], v[140:143]\n"
"s_add_u32 m0, s84, 36864\n"
"ds_read_b128 v[168:171], v2 offset:24576\n"
"v_mov_b32 v38, v56\n"
"v_mfma_f32_16x16x32_bf16 v[140:143], a[140:143], v[180:183], v[140:143]\n"
"s_mov_b32 s52, 405504\n"
"buffer_load_dwordx4 v30, s[28:31], s52 offen offset:0 sc1 lds\n"
"v_mfma_f32_16x16x32_bf16 v[140:143], a[132:135], v[176:179], v[140:143]\n"
"ds_read_b128 v[172:175], v2 offset:33792\n"
"v_mov_b32 v39, v60\n"
"v_mfma_f32_16x16x32_bf16 v[148:151], a[132:135], v[192:195], 0\n"
"v_mov_b32 v40, v64\n"
"v_mov_b32 v41, v68\n"
"v_mfma_f32_16x16x32_bf16 v[148:151], a[140:143], v[196:199], v[148:151]\n"
"s_waitcnt lgkmcnt(2)\n"
"ds_read_b128 v[176:179], v2 offset:7168\n"
"v_mov_b32 v42, v72\n"
"v_mfma_f32_16x16x32_bf16 v[148:151], a[148:151], v[200:203], v[148:151]\n"
"buffer_load_dwordx4 v30, s[28:31], s52 offen offset:1024 sc1 lds\n"
"v_mfma_f32_16x16x32_bf16 v[148:151], a[156:159], v[204:207], v[148:151]\n"
"ds_read_b128 v[180:183], v2 offset:16384\n"
"v_mov_b32 v43, v76\n"
"v_mfma_f32_16x16x32_bf16 v[144:147], a[152:155], v[204:207], 0\n"
"v_mov_b32 v44, v80\n"
"v_mov_b32 v45, v84\n"
"v_mfma_f32_16x16x32_bf16 v[144:147], a[144:147], v[200:203], v[144:147]\n"
"ds_read_b128 v[184:187], v2 offset:25600\n"
"v_mov_b32 v46, v88\n"
"v_mfma_f32_16x16x32_bf16 v[144:147], a[136:139], v[196:199], v[144:147]\n"
"buffer_load_dwordx4 v30, s[28:31], s52 offen offset:2048 sc1 lds\n"
"v_mfma_f32_16x16x32_bf16 v[144:147], a[128:131], v[192:195], v[144:147]\n"
"ds_read_b128 v[188:191], v2 offset:34816\n"
"v_mov_b32 v47, v92\n"
"v_mfma_f32_16x16x32_bf16 v[152:155], a[128:131], v[160:163], 0\n"
"v_mov_b32 v48, v96\n"
"v_mov_b32 v49, v100\n"
"v_mfma_f32_16x16x32_bf16 v[152:155], a[136:139], v[164:167], v[152:155]\n"
"s_waitcnt lgkmcnt(2)\n"
"ds_read_b128 v[192:195], v2 offset:8192\n"
"v_mov_b32 v101, v16\n"
"v_mfma_f32_16x16x32_bf16 v[152:155], a[144:147], v[168:171], v[152:155]\n"
"buffer_load_dwordx4 v30, s[28:31], s52 offen offset:3072 sc1 lds\n"
"v_mfma_f32_16x16x32_bf16 v[152:155], a[152:155], v[172:175], v[152:155]\n"
"ds_read_b128 v[196:199], v2 offset:17408\n"
"v_mov_b32 v102, v28\n"
"v_mfma_f32_16x16x32_bf16 v[156:159], a[156:159], v[172:175], 0\n"
"v_mov_b32 v103, v232\n"
"v_mfma_f32_16x16x32_bf16 v[156:159], a[148:151], v[168:171], v[156:159]\n"
"s_add_u32 m0, s84, 40960\n"
"ds_read_b128 v[200:203], v2 offset:26624\n"
"v_mfma_f32_16x16x32_bf16 v[156:159], a[140:143], v[164:167], v[156:159]\n"
"s_mov_b32 s52, 409600\n"
"buffer_load_dwordx4 v30, s[28:31], s52 offen offset:0 sc1 lds\n"
"v_mfma_f32_16x16x32_bf16 v[156:159], a[132:135], v[160:163], v[156:159]\n"
"ds_read_b128 v[204:207], v2 offset:35840\n"
"v_add_u32 v2, 0xfffe1000, v2\n"
"v_mfma_f32_16x16x32_bf16 v[212:215], a[132:135], v[176:179], 0\n"
"s_waitcnt vmcnt(14)\n"
"s_barrier\n"
"v_mfma_f32_16x16x32_bf16 v[212:215], a[140:143], v[180:183], v[212:215]\n"
"s_waitcnt lgkmcnt(2)\n"
"ds_read_b128 v[160:163], v2 offset:0\n"
"v_permlane32_swap_b32 v100, v102\n"
"v_mfma_f32_16x16x32_bf16 v[212:215], a[148:151], v[184:187], v[212:215]\n"
"buffer_load_dwordx4 v30, s[28:31], s52 offen offset:1024 sc1 lds\n"
"v_mfma_f32_16x16x32_bf16 v[212:215], a[156:159], v[188:191], v[212:215]\n"
"ds_read_b128 v[164:167], v2 offset:9216\n"
"v_permlane32_swap_b32 v101, v103\n"
"v_mfma_f32_16x16x32_bf16 v[208:211], a[152:155], v[188:191], 0\n"
"v_mfma_f32_16x16x32_bf16 v[208:211], a[144:147], v[184:187], v[208:211]\n"
"ds_read_b128 v[168:171], v2 offset:18432\n"
"v_permlane16_swap_b32 v100, v101\n"
"v_mfma_f32_16x16x32_bf16 v[208:211], a[136:139], v[180:183], v[208:211]\n"
"buffer_load_dwordx4 v30, s[28:31], s52 offen offset:2048 sc1 lds\n"
"v_mfma_f32_16x16x32_bf16 v[208:211], a[128:131], v[176:179], v[208:211]\n"
"ds_read_b128 v[172:175], v2 offset:27648\n"
"v_permlane16_swap_b32 v102, v103\n"
"v_mfma_f32_16x16x32_bf16 v[216:219], a[128:131], v[192:195], 0\n"
"v_mfma_f32_16x16x32_bf16 v[216:219], a[136:139], v[196:199], v[216:219]\n"
"s_waitcnt lgkmcnt(2)\n"
"ds_read_b128 v[176:179], v2 offset:1024\n"
"v_mfma_f32_16x16x32_bf16 v[216:219], a[144:147], v[200:203], v[216:219]\n"
"buffer_load_dwordx4 v30, s[28:31], s52 offen offset:3072 sc1 lds\n"
"v_mfma_f32_16x16x32_bf16 v[216:219], a[152:155], v[204:207], v[216:219]\n"
"ds_read_b128 v[180:183], v2 offset:10240\n"
"v_mfma_f32_16x16x32_bf16 v[220:223], a[156:159], v[204:207], 0\n"
"v_mfma_f32_16x16x32_bf16 v[220:223], a[148:151], v[200:203], v[220:223]\n"
"s_add_u32 m0, s84, 45056\n"
"ds_read_b128 v[184:187], v2 offset:19456\n"
"v_mfma_f32_16x16x32_bf16 v[220:223], a[140:143], v[196:199], v[220:223]\n"
"s_mov_b32 s52, 413696\n"
"buffer_load_dwordx4 v30, s[28:31], s52 offen offset:0 sc1 lds\n"
"v_mfma_f32_16x16x32_bf16 v[220:223], a[132:135], v[192:195], v[220:223]\n"
"s_waitcnt vmcnt(10)\n"
"ds_read_b128 v[188:191], v2 offset:28672\n"
"v_mfma_f32_16x16x32_bf16 v[104:107], a[160:163], v[160:163], v[104:107]\n"
"buffer_store_dwordx4 v[8:11], v1, s[20:23], s67 offen offset:0 sc1\n"
"v_mfma_f32_16x16x32_bf16 v[104:107], a[168:171], v[164:167], v[104:107]\n"
"s_waitcnt lgkmcnt(2)\n"
"ds_read_b128 v[192:195], v2 offset:2048\n"
"v_mfma_f32_16x16x32_bf16 v[104:107], a[176:179], v[168:171], v[104:107]\n"
"s_mov_b32 s53, 90112\n"
"buffer_load_dwordx4 a[192:195], v3, s[44:47], s53 offen offset:0 sc1\n"
"v_mfma_f32_16x16x32_bf16 v[104:107], a[184:187], v[172:175], v[104:107]\n"
"ds_read_b128 v[196:199], v2 offset:11264\n"
"v_mfma_f32_16x16x32_bf16 v[108:111], a[188:191], v[172:175], v[108:111]\n"
"v_mfma_f32_16x16x32_bf16 v[108:111], a[180:183], v[168:171], v[108:111]\n"
"ds_read_b128 v[200:203], v2 offset:20480\n"
"v_mfma_f32_16x16x32_bf16 v[108:111], a[172:175], v[164:167], v[108:111]\n"
"buffer_load_dwordx4 a[196:199], v3, s[44:47], s53 offen offset:1024 sc1\n"
"v_mfma_f32_16x16x32_bf16 v[108:111], a[164:167], v[160:163], v[108:111]\n"
"ds_read_b128 v[204:207], v2 offset:29696\n"
"v_mfma_f32_16x16x32_bf16 v[116:119], a[164:167], v[176:179], v[116:119]\n"
"v_mfma_f32_16x16x32_bf16 v[116:119], a[172:175], v[180:183], v[116:119]\n"
"s_waitcnt lgkmcnt(2)\n"
"ds_read_b128 v[160:163], v2 offset:3072\n"
"v_mfma_f32_16x16x32_bf16 v[116:119], a[180:183], v[184:187], v[116:119]\n"
"buffer_load_dwordx4 a[200:203], v3, s[44:47], s53 offen offset:2048 sc1\n"
"v_mfma_f32_16x16x32_bf16 v[116:119], a[188:191], v[188:191], v[116:119]\n"
"ds_read_b128 v[164:167], v2 offset:12288\n"
"v_mfma_f32_16x16x32_bf16 v[112:115], a[184:187], v[188:191], v[112:115]\n"
"v_mfma_f32_16x16x32_bf16 v[112:115], a[176:179], v[184:187], v[112:115]\n"
"ds_read_b128 v[168:171], v2 offset:21504\n"
"v_mfma_f32_16x16x32_bf16 v[112:115], a[168:171], v[180:183], v[112:115]\n"
"buffer_load_dwordx4 a[204:207], v3, s[44:47], s53 offen offset:3072 sc1\n"
"v_mfma_f32_16x16x32_bf16 v[112:115], a[160:163], v[176:179], v[112:115]\n"
"ds_read_b128 v[172:175], v2 offset:30720\n"
"v_mfma_f32_16x16x32_bf16 v[120:123], a[160:163], v[192:195], v[120:123]\n"
"v_mfma_f32_16x16x32_bf16 v[120:123], a[168:171], v[196:199], v[120:123]\n"
"s_waitcnt lgkmcnt(2)\n"
"ds_read_b128 v[176:179], v2 offset:4096\n"
"v_mfma_f32_16x16x32_bf16 v[120:123], a[176:179], v[200:203], v[120:123]\n"
"s_mov_b32 s53, 94208\n"
"buffer_load_dwordx4 a[208:211], v3, s[44:47], s53 offen offset:0 sc1\n"
"v_mfma_f32_16x16x32_bf16 v[120:123], a[184:187], v[204:207], v[120:123]\n"
"ds_read_b128 v[180:183], v2 offset:13312\n"
"v_mfma_f32_16x16x32_bf16 v[124:127], a[188:191], v[204:207], v[124:127]\n"
"v_mfma_f32_16x16x32_bf16 v[124:127], a[180:183], v[200:203], v[124:127]\n"
"ds_read_b128 v[184:187], v2 offset:22528\n"
"v_mfma_f32_16x16x32_bf16 v[124:127], a[172:175], v[196:199], v[124:127]\n"
"buffer_load_dwordx4 a[212:215], v3, s[44:47], s53 offen offset:1024 sc1\n"
"v_mfma_f32_16x16x32_bf16 v[124:127], a[164:167], v[192:195], v[124:127]\n"
"ds_read_b128 v[188:191], v2 offset:31744\n"
"v_mfma_f32_16x16x32_bf16 v[132:135], a[164:167], v[160:163], v[132:135]\n"
"buffer_store_dwordx4 v[20:23], v1, s[20:23], s68 offen offset:0 sc1\n"
"v_mfma_f32_16x16x32_bf16 v[132:135], a[172:175], v[164:167], v[132:135]\n"
"s_waitcnt lgkmcnt(2)\n"
"ds_read_b128 v[192:195], v2 offset:5120\n"
"v_mfma_f32_16x16x32_bf16 v[132:135], a[180:183], v[168:171], v[132:135]\n"
"buffer_load_dwordx4 a[216:219], v3, s[44:47], s53 offen offset:2048 sc1\n"
"v_mfma_f32_16x16x32_bf16 v[132:135], a[188:191], v[172:175], v[132:135]\n"
"ds_read_b128 v[196:199], v2 offset:14336\n"
"v_mfma_f32_16x16x32_bf16 v[128:131], a[184:187], v[172:175], v[128:131]\n"
"v_mfma_f32_16x16x32_bf16 v[128:131], a[176:179], v[168:171], v[128:131]\n"
"ds_read_b128 v[200:203], v2 offset:23552\n"
"v_mfma_f32_16x16x32_bf16 v[128:131], a[168:171], v[164:167], v[128:131]\n"
"buffer_load_dwordx4 a[220:223], v3, s[44:47], s53 offen offset:3072 sc1\n"
"v_mfma_f32_16x16x32_bf16 v[128:131], a[160:163], v[160:163], v[128:131]\n"
"ds_read_b128 v[204:207], v2 offset:32768\n"
"v_mfma_f32_16x16x32_bf16 v[136:139], a[160:163], v[176:179], v[136:139]\n"
"v_mfma_f32_16x16x32_bf16 v[136:139], a[168:171], v[180:183], v[136:139]\n"
"s_waitcnt lgkmcnt(2)\n"
"s_add_u32 m0, s84, 90112\n"
"ds_read_b128 v[160:163], v2 offset:6144\n"
"v_mfma_f32_16x16x32_bf16 v[136:139], a[176:179], v[184:187], v[136:139]\n"
"s_mov_b32 s52, 442368\n"
"buffer_load_dwordx4 v30, s[28:31], s52 offen offset:0 sc1 lds\n"
"v_mfma_f32_16x16x32_bf16 v[136:139], a[184:187], v[188:191], v[136:139]\n"
"ds_read_b128 v[164:167], v2 offset:15360\n"
"v_mfma_f32_16x16x32_bf16 v[140:143], a[188:191], v[188:191], v[140:143]\n"
"v_mfma_f32_16x16x32_bf16 v[140:143], a[180:183], v[184:187], v[140:143]\n"
"ds_read_b128 v[168:171], v2 offset:24576\n"
"v_mfma_f32_16x16x32_bf16 v[140:143], a[172:175], v[180:183], v[140:143]\n"
"buffer_load_dwordx4 v30, s[28:31], s52 offen offset:1024 sc1 lds\n"
"v_mfma_f32_16x16x32_bf16 v[140:143], a[164:167], v[176:179], v[140:143]\n"
"ds_read_b128 v[172:175], v2 offset:33792\n"
"v_mfma_f32_16x16x32_bf16 v[148:151], a[164:167], v[192:195], v[148:151]\n"
"v_mfma_f32_16x16x32_bf16 v[148:151], a[172:175], v[196:199], v[148:151]\n"
"s_waitcnt lgkmcnt(2)\n"
"ds_read_b128 v[176:179], v2 offset:7168\n"
"v_mfma_f32_16x16x32_bf16 v[148:151], a[180:183], v[200:203], v[148:151]\n"
"buffer_load_dwordx4 v30, s[28:31], s52 offen offset:2048 sc1 lds\n"
"v_mfma_f32_16x16x32_bf16 v[148:151], a[188:191], v[204:207], v[148:151]\n"
"ds_read_b128 v[180:183], v2 offset:16384\n"
"v_mfma_f32_16x16x32_bf16 v[144:147], a[184:187], v[204:207], v[144:147]\n"
"v_mfma_f32_16x16x32_bf16 v[144:147], a[176:179], v[200:203], v[144:147]\n"
"ds_read_b128 v[184:187], v2 offset:25600\n"
"v_mfma_f32_16x16x32_bf16 v[144:147], a[168:171], v[196:199], v[144:147]\n"
"buffer_load_dwordx4 v30, s[28:31], s52 offen offset:3072 sc1 lds\n"
"v_mfma_f32_16x16x32_bf16 v[144:147], a[160:163], v[192:195], v[144:147]\n"
"ds_read_b128 v[188:191], v2 offset:34816\n"
"v_mfma_f32_16x16x32_bf16 v[152:155], a[160:163], v[160:163], v[152:155]\n"
"buffer_store_dwordx4 v[224:227], v1, s[20:23], s69 offen offset:0 sc1\n"
"v_mfma_f32_16x16x32_bf16 v[152:155], a[168:171], v[164:167], v[152:155]\n"
"s_waitcnt lgkmcnt(2)\n"
"s_add_u32 m0, s84, 94208\n"
"ds_read_b128 v[192:195], v2 offset:8192\n"
"v_mfma_f32_16x16x32_bf16 v[152:155], a[176:179], v[168:171], v[152:155]\n"
"s_mov_b32 s52, 446464\n"
"buffer_load_dwordx4 v30, s[28:31], s52 offen offset:0 sc1 lds\n"
"v_mfma_f32_16x16x32_bf16 v[152:155], a[184:187], v[172:175], v[152:155]\n"
"ds_read_b128 v[196:199], v2 offset:17408\n"
"v_mfma_f32_16x16x32_bf16 v[156:159], a[188:191], v[172:175], v[156:159]\n"
"v_mfma_f32_16x16x32_bf16 v[156:159], a[180:183], v[168:171], v[156:159]\n"
"ds_read_b128 v[200:203], v2 offset:26624\n"
"v_mfma_f32_16x16x32_bf16 v[156:159], a[172:175], v[164:167], v[156:159]\n"
"buffer_load_dwordx4 v30, s[28:31], s52 offen offset:1024 sc1 lds\n"
"v_mfma_f32_16x16x32_bf16 v[156:159], a[164:167], v[160:163], v[156:159]\n"
"ds_read_b128 v[204:207], v2 offset:35840\n"
"v_add_u32 v2, 0x9000, v2\n"
"v_mfma_f32_16x16x32_bf16 v[212:215], a[164:167], v[176:179], v[212:215]\n"
"s_waitcnt vmcnt(17)\n"
"s_barrier\n"
"v_mfma_f32_16x16x32_bf16 v[212:215], a[172:175], v[180:183], v[212:215]\n"
"s_waitcnt lgkmcnt(2)\n"
"ds_read_b128 v[160:163], v2 offset:0\n"
"v_mfma_f32_16x16x32_bf16 v[212:215], a[180:183], v[184:187], v[212:215]\n"
"buffer_load_dwordx4 v30, s[28:31], s52 offen offset:2048 sc1 lds\n"
"v_mfma_f32_16x16x32_bf16 v[212:215], a[188:191], v[188:191], v[212:215]\n"
"ds_read_b128 v[164:167], v2 offset:9216\n"
"v_mfma_f32_16x16x32_bf16 v[208:211], a[184:187], v[188:191], v[208:211]\n"
"v_mfma_f32_16x16x32_bf16 v[208:211], a[176:179], v[184:187], v[208:211]\n"
"ds_read_b128 v[168:171], v2 offset:18432\n"
"v_mfma_f32_16x16x32_bf16 v[208:211], a[168:171], v[180:183], v[208:211]\n"
"buffer_load_dwordx4 v30, s[28:31], s52 offen offset:3072 sc1 lds\n"
"v_mfma_f32_16x16x32_bf16 v[208:211], a[160:163], v[176:179], v[208:211]\n"
"ds_read_b128 v[172:175], v2 offset:27648\n"
"v_mfma_f32_16x16x32_bf16 v[216:219], a[160:163], v[192:195], v[216:219]\n"
"v_mfma_f32_16x16x32_bf16 v[216:219], a[168:171], v[196:199], v[216:219]\n"
"s_waitcnt lgkmcnt(2)\n"
"s_add_u32 m0, s84, 98304\n"
"ds_read_b128 v[176:179], v2 offset:1024\n"
"v_mfma_f32_16x16x32_bf16 v[216:219], a[176:179], v[200:203], v[216:219]\n"
"s_mov_b32 s52, 450560\n"
"buffer_load_dwordx4 v30, s[28:31], s52 offen offset:0 sc1 lds\n"
"v_mfma_f32_16x16x32_bf16 v[216:219], a[184:187], v[204:207], v[216:219]\n"
"ds_read_b128 v[180:183], v2 offset:10240\n"
"v_mfma_f32_16x16x32_bf16 v[220:223], a[188:191], v[204:207], v[220:223]\n"
"v_mfma_f32_16x16x32_bf16 v[220:223], a[180:183], v[200:203], v[220:223]\n"
"ds_read_b128 v[184:187], v2 offset:19456\n"
"v_mfma_f32_16x16x32_bf16 v[220:223], a[172:175], v[196:199], v[220:223]\n"
"v_mfma_f32_16x16x32_bf16 v[220:223], a[164:167], v[192:195], v[220:223]\n"
"s_waitcnt vmcnt(10)\n"
"ds_read_b128 v[188:191], v2 offset:28672\n"
"v_mfma_f32_16x16x32_bf16 v[104:107], a[192:195], v[160:163], v[104:107]\n"
"buffer_store_dwordx4 v[12:15], v1, s[20:23], s67 offen offset:1024 sc1\n"
"v_mfma_f32_16x16x32_bf16 v[104:107], a[200:203], v[164:167], v[104:107]\n"
"s_waitcnt lgkmcnt(2)\n"
"ds_read_b128 v[192:195], v2 offset:2048\n"
"v_mfma_f32_16x16x32_bf16 v[104:107], a[208:211], v[168:171], v[104:107]\n"
"s_mov_b32 s53, 98304\n"
"buffer_load_dwordx4 a[128:131], v3, s[44:47], s53 offen offset:0 sc1\n"
"v_mfma_f32_16x16x32_bf16 v[104:107], a[216:219], v[172:175], v[104:107]\n"
"ds_read_b128 v[196:199], v2 offset:11264\n"
".LP3_W9:\n"
"v_mfma_f32_16x16x32_bf16 v[108:111], a[220:223], v[172:175], v[108:111]\n"
"v_mfma_f32_16x16x32_bf16 v[108:111], a[212:215], v[168:171], v[108:111]\n"
"ds_read_b128 v[200:203], v2 offset:20480\n"
"v_mfma_f32_16x16x32_bf16 v[108:111], a[204:207], v[164:167], v[108:111]\n"
"buffer_load_dwordx4 a[132:135], v3, s[44:47], s53 offen offset:1024 sc1\n"
"v_mfma_f32_16x16x32_bf16 v[108:111], a[196:199], v[160:163], v[108:111]\n"
"ds_read_b128 v[204:207], v2 offset:29696\n"
"v_mfma_f32_16x16x32_bf16 v[116:119], a[196:199], v[176:179], v[116:119]\n"
"v_mfma_f32_16x16x32_bf16 v[116:119], a[204:207], v[180:183], v[116:119]\n"
"s_waitcnt lgkmcnt(2)\n"
"ds_read_b128 v[160:163], v2 offset:3072\n"
"v_mfma_f32_16x16x32_bf16 v[116:119], a[212:215], v[184:187], v[116:119]\n"
"buffer_load_dwordx4 a[136:139], v3, s[44:47], s53 offen offset:2048 sc1\n"
"v_mfma_f32_16x16x32_bf16 v[116:119], a[220:223], v[188:191], v[116:119]\n"
"ds_read_b128 v[164:167], v2 offset:12288\n"
"v_mfma_f32_16x16x32_bf16 v[112:115], a[216:219], v[188:191], v[112:115]\n"
"v_mfma_f32_16x16x32_bf16 v[112:115], a[208:211], v[184:187], v[112:115]\n"
"ds_read_b128 v[168:171], v2 offset:21504\n"
"v_mfma_f32_16x16x32_bf16 v[112:115], a[200:203], v[180:183], v[112:115]\n"
"buffer_load_dwordx4 a[140:143], v3, s[44:47], s53 offen offset:3072 sc1\n"
"v_mfma_f32_16x16x32_bf16 v[112:115], a[192:195], v[176:179], v[112:115]\n"
"ds_read_b128 v[172:175], v2 offset:30720\n"
"v_mfma_f32_16x16x32_bf16 v[120:123], a[192:195], v[192:195], v[120:123]\n"
"v_mfma_f32_16x16x32_bf16 v[120:123], a[200:203], v[196:199], v[120:123]\n"
"s_waitcnt lgkmcnt(2)\n"
"ds_read_b128 v[176:179], v2 offset:4096\n"
"v_mfma_f32_16x16x32_bf16 v[120:123], a[208:211], v[200:203], v[120:123]\n"
"s_mov_b32 s53, 102400\n"
"buffer_load_dwordx4 a[144:147], v3, s[44:47], s53 offen offset:0 sc1\n"
"v_mfma_f32_16x16x32_bf16 v[120:123], a[216:219], v[204:207], v[120:123]\n"
"ds_read_b128 v[180:183], v2 offset:13312\n"
"v_mfma_f32_16x16x32_bf16 v[124:127], a[220:223], v[204:207], v[124:127]\n"
"v_mfma_f32_16x16x32_bf16 v[124:127], a[212:215], v[200:203], v[124:127]\n"
"ds_read_b128 v[184:187], v2 offset:22528\n"
"v_mfma_f32_16x16x32_bf16 v[124:127], a[204:207], v[196:199], v[124:127]\n"
"buffer_load_dwordx4 a[148:151], v3, s[44:47], s53 offen offset:1024 sc1\n"
"v_mfma_f32_16x16x32_bf16 v[124:127], a[196:199], v[192:195], v[124:127]\n"
"ds_read_b128 v[188:191], v2 offset:31744\n"
"v_mfma_f32_16x16x32_bf16 v[132:135], a[196:199], v[160:163], v[132:135]\n"
"buffer_store_dwordx4 v[24:27], v1, s[20:23], s68 offen offset:1024 sc1\n"
"v_mfma_f32_16x16x32_bf16 v[132:135], a[204:207], v[164:167], v[132:135]\n"
"s_waitcnt lgkmcnt(2)\n"
"ds_read_b128 v[192:195], v2 offset:5120\n"
"v_mfma_f32_16x16x32_bf16 v[132:135], a[212:215], v[168:171], v[132:135]\n"
"buffer_load_dwordx4 a[152:155], v3, s[44:47], s53 offen offset:2048 sc1\n"
"v_mfma_f32_16x16x32_bf16 v[132:135], a[220:223], v[172:175], v[132:135]\n"
"ds_read_b128 v[196:199], v2 offset:14336\n"
"v_mfma_f32_16x16x32_bf16 v[128:131], a[216:219], v[172:175], v[128:131]\n"
"v_mfma_f32_16x16x32_bf16 v[128:131], a[208:211], v[168:171], v[128:131]\n"
"ds_read_b128 v[200:203], v2 offset:23552\n"
"v_mfma_f32_16x16x32_bf16 v[128:131], a[200:203], v[164:167], v[128:131]\n"
"buffer_load_dwordx4 a[156:159], v3, s[44:47], s53 offen offset:3072 sc1\n"
"v_mfma_f32_16x16x32_bf16 v[128:131], a[192:195], v[160:163], v[128:131]\n"
"ds_read_b128 v[204:207], v2 offset:32768\n"
"v_mfma_f32_16x16x32_bf16 v[136:139], a[192:195], v[176:179], v[136:139]\n"
"v_mfma_f32_16x16x32_bf16 v[136:139], a[200:203], v[180:183], v[136:139]\n"
"s_waitcnt lgkmcnt(2)\n"
"s_add_u32 m0, s84, 126976\n"
"ds_read_b128 v[160:163], v2 offset:6144\n"
"v_mfma_f32_16x16x32_bf16 v[136:139], a[208:211], v[184:187], v[136:139]\n"
"s_mov_b32 s52, 479232\n"
"buffer_load_dwordx4 v30, s[28:31], s52 offen offset:0 sc1 lds\n"
"v_mfma_f32_16x16x32_bf16 v[136:139], a[216:219], v[188:191], v[136:139]\n"
"ds_read_b128 v[164:167], v2 offset:15360\n"
"v_mfma_f32_16x16x32_bf16 v[140:143], a[220:223], v[188:191], v[140:143]\n"
"v_mfma_f32_16x16x32_bf16 v[140:143], a[212:215], v[184:187], v[140:143]\n"
"ds_read_b128 v[168:171], v2 offset:24576\n"
"v_mfma_f32_16x16x32_bf16 v[140:143], a[204:207], v[180:183], v[140:143]\n"
"buffer_load_dwordx4 v30, s[28:31], s52 offen offset:1024 sc1 lds\n"
"v_mfma_f32_16x16x32_bf16 v[140:143], a[196:199], v[176:179], v[140:143]\n"
"ds_read_b128 v[172:175], v2 offset:33792\n"
"v_mfma_f32_16x16x32_bf16 v[148:151], a[196:199], v[192:195], v[148:151]\n"
"v_mfma_f32_16x16x32_bf16 v[148:151], a[204:207], v[196:199], v[148:151]\n"
"s_waitcnt lgkmcnt(2)\n"
"ds_read_b128 v[176:179], v2 offset:7168\n"
"v_mfma_f32_16x16x32_bf16 v[148:151], a[212:215], v[200:203], v[148:151]\n"
"buffer_load_dwordx4 v30, s[28:31], s52 offen offset:2048 sc1 lds\n"
"v_mfma_f32_16x16x32_bf16 v[148:151], a[220:223], v[204:207], v[148:151]\n"
"ds_read_b128 v[180:183], v2 offset:16384\n"
"v_mfma_f32_16x16x32_bf16 v[144:147], a[216:219], v[204:207], v[144:147]\n"
"v_mfma_f32_16x16x32_bf16 v[144:147], a[208:211], v[200:203], v[144:147]\n"
"ds_read_b128 v[184:187], v2 offset:25600\n"
"v_mfma_f32_16x16x32_bf16 v[144:147], a[200:203], v[196:199], v[144:147]\n"
"buffer_load_dwordx4 v30, s[28:31], s52 offen offset:3072 sc1 lds\n"
"v_mfma_f32_16x16x32_bf16 v[144:147], a[192:195], v[192:195], v[144:147]\n"
"ds_read_b128 v[188:191], v2 offset:34816\n"
"v_mfma_f32_16x16x32_bf16 v[152:155], a[192:195], v[160:163], v[152:155]\n"
"buffer_store_dwordx4 v[228:231], v1, s[20:23], s69 offen offset:1024 sc1\n"
"v_mfma_f32_16x16x32_bf16 v[152:155], a[200:203], v[164:167], v[152:155]\n"
"s_waitcnt lgkmcnt(2)\n"
"s_add_u32 m0, s84, 131072\n"
"ds_read_b128 v[192:195], v2 offset:8192\n"
"v_mfma_f32_16x16x32_bf16 v[152:155], a[208:211], v[168:171], v[152:155]\n"
"s_mov_b32 s52, 483328\n"
"buffer_load_dwordx4 v30, s[28:31], s52 offen offset:0 sc1 lds\n"
"v_mfma_f32_16x16x32_bf16 v[152:155], a[216:219], v[172:175], v[152:155]\n"
"ds_read_b128 v[196:199], v2 offset:17408\n"
"v_mfma_f32_16x16x32_bf16 v[156:159], a[220:223], v[172:175], v[156:159]\n"
"v_mfma_f32_16x16x32_bf16 v[156:159], a[212:215], v[168:171], v[156:159]\n"
"ds_read_b128 v[200:203], v2 offset:26624\n"
"v_mfma_f32_16x16x32_bf16 v[156:159], a[204:207], v[164:167], v[156:159]\n"
"buffer_load_dwordx4 v30, s[28:31], s52 offen offset:1024 sc1 lds\n"
"v_mfma_f32_16x16x32_bf16 v[156:159], a[196:199], v[160:163], v[156:159]\n"
"ds_read_b128 v[204:207], v2 offset:35840\n"
"v_add_u32 v2, 0xd000, v2\n"
"v_mfma_f32_16x16x32_bf16 v[212:215], a[196:199], v[176:179], v[212:215]\n"
"s_waitcnt vmcnt(17)\n"
"s_barrier\n"
"v_mfma_f32_16x16x32_bf16 v[212:215], a[204:207], v[180:183], v[212:215]\n"
"s_waitcnt lgkmcnt(2)\n"
"ds_read_b128 v[160:163], v2 offset:0\n"
"v_mfma_f32_16x16x32_bf16 v[212:215], a[212:215], v[184:187], v[212:215]\n"
"buffer_load_dwordx4 v30, s[28:31], s52 offen offset:2048 sc1 lds\n"
"v_mfma_f32_16x16x32_bf16 v[212:215], a[220:223], v[188:191], v[212:215]\n"
"ds_read_b128 v[164:167], v2 offset:9216\n"
"v_mfma_f32_16x16x32_bf16 v[208:211], a[216:219], v[188:191], v[208:211]\n"
"v_mfma_f32_16x16x32_bf16 v[208:211], a[208:211], v[184:187], v[208:211]\n"
"ds_read_b128 v[168:171], v2 offset:18432\n"
"v_mfma_f32_16x16x32_bf16 v[208:211], a[200:203], v[180:183], v[208:211]\n"
"buffer_load_dwordx4 v30, s[28:31], s52 offen offset:3072 sc1 lds\n"
"v_mfma_f32_16x16x32_bf16 v[208:211], a[192:195], v[176:179], v[208:211]\n"
"ds_read_b128 v[172:175], v2 offset:27648\n"
"v_mfma_f32_16x16x32_bf16 v[216:219], a[192:195], v[192:195], v[216:219]\n"
"v_mfma_f32_16x16x32_bf16 v[216:219], a[200:203], v[196:199], v[216:219]\n"
"s_waitcnt lgkmcnt(2)\n"
"s_add_u32 m0, s84, 135168\n"
"ds_read_b128 v[176:179], v2 offset:1024\n"
"v_mfma_f32_16x16x32_bf16 v[216:219], a[208:211], v[200:203], v[216:219]\n"
"s_mov_b32 s52, 487424\n"
"buffer_load_dwordx4 v30, s[28:31], s52 offen offset:0 sc1 lds\n"
"v_mfma_f32_16x16x32_bf16 v[216:219], a[216:219], v[204:207], v[216:219]\n"
".LPAD15:\n"
"s_cbranch_execz .LPAD18\n"
"ds_read_b128 v[180:183], v2 offset:10240\n"
"v_mfma_f32_16x16x32_bf16 v[220:223], a[220:223], v[204:207], v[220:223]\n"
"v_mfma_f32_16x16x32_bf16 v[220:223], a[212:215], v[200:203], v[220:223]\n"
"ds_read_b128 v[184:187], v2 offset:19456\n"
"v_mfma_f32_16x16x32_bf16 v[220:223], a[204:207], v[196:199], v[220:223]\n"
"v_mfma_f32_16x16x32_bf16 v[220:223], a[196:199], v[192:195], v[220:223]\n"
"s_waitcnt vmcnt(10)\n"
"ds_read_b128 v[188:191], v2 offset:28672\n"
"v_mfma_f32_16x16x32_bf16 v[104:107], a[128:131], v[160:163], v[104:107]\n"
"buffer_store_dwordx4 v[100:103], v254, s[20:23], 0 offen sc1\n"
"v_mfma_f32_16x16x32_bf16 v[104:107], a[136:139], v[164:167], v[104:107]\n"
"s_waitcnt lgkmcnt(2)\n"
"ds_read_b128 v[192:195], v2 offset:2048\n"
"v_mfma_f32_16x16x32_bf16 v[104:107], a[144:147], v[168:171], v[104:107]\n"
"s_mov_b32 s53, 106496\n"
"buffer_load_dwordx4 a[160:163], v3, s[44:47], s53 offen offset:0 sc1\n"
"v_mfma_f32_16x16x32_bf16 v[104:107], a[152:155], v[172:175], v[104:107]\n"
"ds_read_b128 v[196:199], v2 offset:11264\n"
"v_mfma_f32_16x16x32_bf16 v[108:111], a[156:159], v[172:175], v[108:111]\n"
"v_mfma_f32_16x16x32_bf16 v[108:111], a[148:151], v[168:171], v[108:111]\n"
"ds_read_b128 v[200:203], v2 offset:20480\n"
"v_mfma_f32_16x16x32_bf16 v[108:111], a[140:143], v[164:167], v[108:111]\n"
"buffer_load_dwordx4 a[164:167], v3, s[44:47], s53 offen offset:1024 sc1\n"
"v_mfma_f32_16x16x32_bf16 v[108:111], a[132:135], v[160:163], v[108:111]\n"
"ds_read_b128 v[204:207], v2 offset:29696\n"
"v_mfma_f32_16x16x32_bf16 v[116:119], a[132:135], v[176:179], v[116:119]\n"
"v_mfma_f32_16x16x32_bf16 v[116:119], a[140:143], v[180:183], v[116:119]\n"
"s_waitcnt lgkmcnt(2)\n"
"ds_read_b128 v[160:163], v2 offset:3072\n"
"v_mfma_f32_16x16x32_bf16 v[116:119], a[148:151], v[184:187], v[116:119]\n"
"buffer_load_dwordx4 a[168:171], v3, s[44:47], s53 offen offset:2048 sc1\n"
"v_mfma_f32_16x16x32_bf16 v[116:119], a[156:159], v[188:191], v[116:119]\n"
"ds_read_b128 v[164:167], v2 offset:12288\n"
"v_mfma_f32_16x16x32_bf16 v[112:115], a[152:155], v[188:191], v[112:115]\n"
"v_mfma_f32_16x16x32_bf16 v[112:115], a[144:147], v[184:187], v[112:115]\n"
"ds_read_b128 v[168:171], v2 offset:21504\n"
"v_mfma_f32_16x16x32_bf16 v[112:115], a[136:139], v[180:183], v[112:115]\n"
"buffer_load_dwordx4 a[172:175], v3, s[44:47], s53 offen offset:3072 sc1\n"
"v_mfma_f32_16x16x32_bf16 v[112:115], a[128:131], v[176:179], v[112:115]\n"
"ds_read_b128 v[172:175], v2 offset:30720\n"
"v_mfma_f32_16x16x32_bf16 v[120:123], a[128:131], v[192:195], v[120:123]\n"
"v_mfma_f32_16x16x32_bf16 v[120:123], a[136:139], v[196:199], v[120:123]\n"
"s_waitcnt lgkmcnt(2)\n"
"ds_read_b128 v[176:179], v2 offset:4096\n"
"v_mfma_f32_16x16x32_bf16 v[120:123], a[144:147], v[200:203], v[120:123]\n"
"s_mov_b32 s53, 110592\n"
"buffer_load_dwordx4 a[176:179], v3, s[44:47], s53 offen offset:0 sc1\n"
"v_mfma_f32_16x16x32_bf16 v[120:123], a[152:155], v[204:207], v[120:123]\n"
"ds_read_b128 v[180:183], v2 offset:13312\n"
"v_mfma_f32_16x16x32_bf16 v[124:127], a[156:159], v[204:207], v[124:127]\n"
"v_mfma_f32_16x16x32_bf16 v[124:127], a[148:151], v[200:203], v[124:127]\n"
"ds_read_b128 v[184:187], v2 offset:22528\n"
"v_mfma_f32_16x16x32_bf16 v[124:127], a[140:143], v[196:199], v[124:127]\n"
"buffer_load_dwordx4 a[180:183], v3, s[44:47], s53 offen offset:1024 sc1\n"
"v_mfma_f32_16x16x32_bf16 v[124:127], a[132:135], v[192:195], v[124:127]\n"
"ds_read_b128 v[188:191], v2 offset:31744\n"
"v_mfma_f32_16x16x32_bf16 v[132:135], a[132:135], v[160:163], v[132:135]\n"
"v_mfma_f32_16x16x32_bf16 v[132:135], a[140:143], v[164:167], v[132:135]\n"
"s_waitcnt lgkmcnt(2)\n"
"s_add_u32 s67, s67, 6912\n"
"ds_read_b128 v[192:195], v2 offset:5120\n"
"v_mfma_f32_16x16x32_bf16 v[132:135], a[148:151], v[168:171], v[132:135]\n"
"s_add_u32 s68, s68, 6912\n"
"buffer_load_dwordx4 a[184:187], v3, s[44:47], s53 offen offset:2048 sc1\n"
"v_mfma_f32_16x16x32_bf16 v[132:135], a[156:159], v[172:175], v[132:135]\n"
"s_add_u32 s69, s69, 6912\n"
"ds_read_b128 v[196:199], v2 offset:14336\n"
"v_mfma_f32_16x16x32_bf16 v[128:131], a[152:155], v[172:175], v[128:131]\n"
"s_mov_b32 s55, 6912\n"
"v_mfma_f32_16x16x32_bf16 v[128:131], a[144:147], v[168:171], v[128:131]\n"
"ds_read_b128 v[200:203], v2 offset:23552\n"
"v_mfma_f32_16x16x32_bf16 v[128:131], a[136:139], v[164:167], v[128:131]\n"
"buffer_load_dwordx4 a[188:191], v3, s[44:47], s53 offen offset:3072 sc1\n"
"v_mfma_f32_16x16x32_bf16 v[128:131], a[128:131], v[160:163], v[128:131]\n"
"ds_read_b128 v[204:207], v2 offset:32768\n"
"v_mfma_f32_16x16x32_bf16 v[136:139], a[128:131], v[176:179], v[136:139]\n"
"v_mfma_f32_16x16x32_bf16 v[136:139], a[136:139], v[180:183], v[136:139]\n"
"s_waitcnt lgkmcnt(2)\n"
"s_add_u32 m0, s84, 0\n"
"ds_read_b128 v[160:163], v2 offset:6144\n"
"v_mfma_f32_16x16x32_bf16 v[136:139], a[144:147], v[184:187], v[136:139]\n"
"s_mov_b32 s52, 516096\n"
"buffer_load_dwordx4 v30, s[28:31], s52 offen offset:0 sc1 lds\n"
"v_mfma_f32_16x16x32_bf16 v[136:139], a[152:155], v[188:191], v[136:139]\n"
"ds_read_b128 v[164:167], v2 offset:15360\n"
"v_mfma_f32_16x16x32_bf16 v[140:143], a[156:159], v[188:191], v[140:143]\n"
"v_mfma_f32_16x16x32_bf16 v[140:143], a[148:151], v[184:187], v[140:143]\n"
"ds_read_b128 v[168:171], v2 offset:24576\n"
"v_mfma_f32_16x16x32_bf16 v[140:143], a[140:143], v[180:183], v[140:143]\n"
"buffer_load_dwordx4 v30, s[28:31], s52 offen offset:1024 sc1 lds\n"
"v_mfma_f32_16x16x32_bf16 v[140:143], a[132:135], v[176:179], v[140:143]\n"
"ds_read_b128 v[172:175], v2 offset:33792\n"
"v_mfma_f32_16x16x32_bf16 v[148:151], a[132:135], v[192:195], v[148:151]\n"
"v_mfma_f32_16x16x32_bf16 v[148:151], a[140:143], v[196:199], v[148:151]\n"
"s_waitcnt lgkmcnt(2)\n"
"ds_read_b128 v[176:179], v2 offset:7168\n"
"v_mfma_f32_16x16x32_bf16 v[148:151], a[148:151], v[200:203], v[148:151]\n"
"buffer_load_dwordx4 v30, s[28:31], s52 offen offset:2048 sc1 lds\n"
"v_mfma_f32_16x16x32_bf16 v[148:151], a[156:159], v[204:207], v[148:151]\n"
"ds_read_b128 v[180:183], v2 offset:16384\n"
"v_mfma_f32_16x16x32_bf16 v[144:147], a[152:155], v[204:207], v[144:147]\n"
"v_mfma_f32_16x16x32_bf16 v[144:147], a[144:147], v[200:203], v[144:147]\n"
"ds_read_b128 v[184:187], v2 offset:25600\n"
"v_mfma_f32_16x16x32_bf16 v[144:147], a[136:139], v[196:199], v[144:147]\n"
"buffer_load_dwordx4 v30, s[28:31], s52 offen offset:3072 sc1 lds\n"
"v_mfma_f32_16x16x32_bf16 v[144:147], a[128:131], v[192:195], v[144:147]\n"
"ds_read_b128 v[188:191], v2 offset:34816\n"
"v_mfma_f32_16x16x32_bf16 v[152:155], a[128:131], v[160:163], v[152:155]\n"
"v_mfma_f32_16x16x32_bf16 v[152:155], a[136:139], v[164:167], v[152:155]\n"
"s_waitcnt lgkmcnt(2)\n"
"s_add_u32 m0, s84, 4096\n"
"ds_read_b128 v[192:195], v2 offset:8192\n"
"v_mfma_f32_16x16x32_bf16 v[152:155], a[144:147], v[168:171], v[152:155]\n"
"s_mov_b32 s52, 520192\n"
"buffer_load_dwordx4 v30, s[28:31], s52 offen offset:0 sc1 lds\n"
"v_mfma_f32_16x16x32_bf16 v[152:155], a[152:155], v[172:175], v[152:155]\n"
"ds_read_b128 v[196:199], v2 offset:17408\n"
"v_mfma_f32_16x16x32_bf16 v[156:159], a[156:159], v[172:175], v[156:159]\n"
"v_mfma_f32_16x16x32_bf16 v[156:159], a[148:151], v[168:171], v[156:159]\n"
"ds_read_b128 v[200:203], v2 offset:26624\n"
"v_mfma_f32_16x16x32_bf16 v[156:159], a[140:143], v[164:167], v[156:159]\n"
"buffer_load_dwordx4 v30, s[28:31], s52 offen offset:1024 sc1 lds\n"
"v_mfma_f32_16x16x32_bf16 v[156:159], a[132:135], v[160:163], v[156:159]\n"
"ds_read_b128 v[204:207], v2 offset:35840\n"
"v_add_u32 v2, 0x9000, v2\n"
"v_mfma_f32_16x16x32_bf16 v[212:215], a[132:135], v[176:179], v[212:215]\n"
"s_waitcnt vmcnt(15)\n"
"s_barrier\n"
"v_mfma_f32_16x16x32_bf16 v[212:215], a[140:143], v[180:183], v[212:215]\n"
"s_waitcnt lgkmcnt(2)\n"
"ds_read_b128 v[160:163], v2 offset:0\n"
"v_mfma_f32_16x16x32_bf16 v[212:215], a[148:151], v[184:187], v[212:215]\n"
"buffer_load_dwordx4 v30, s[28:31], s52 offen offset:2048 sc1 lds\n"
"v_mfma_f32_16x16x32_bf16 v[212:215], a[156:159], v[188:191], v[212:215]\n"
"ds_read_b128 v[164:167], v2 offset:9216\n"
"v_mfma_f32_16x16x32_bf16 v[208:211], a[152:155], v[188:191], v[208:211]\n"
"v_mfma_f32_16x16x32_bf16 v[208:211], a[144:147], v[184:187], v[208:211]\n"
"ds_read_b128 v[168:171], v2 offset:18432\n"
"v_mfma_f32_16x16x32_bf16 v[208:211], a[136:139], v[180:183], v[208:211]\n"
"buffer_load_dwordx4 v30, s[28:31], s52 offen offset:3072 sc1 lds\n"
"v_mfma_f32_16x16x32_bf16 v[208:211], a[128:131], v[176:179], v[208:211]\n"
"ds_read_b128 v[172:175], v2 offset:27648\n"
"v_mfma_f32_16x16x32_bf16 v[216:219], a[128:131], v[192:195], v[216:219]\n"
"v_mfma_f32_16x16x32_bf16 v[216:219], a[136:139], v[196:199], v[216:219]\n"
"s_waitcnt lgkmcnt(2)\n"
"s_add_u32 m0, s84, 8192\n"
"ds_read_b128 v[176:179], v2 offset:1024\n"
"v_mfma_f32_16x16x32_bf16 v[216:219], a[144:147], v[200:203], v[216:219]\n"
"s_mov_b32 s52, 524288\n"
"buffer_load_dwordx4 v30, s[28:31], s52 offen offset:0 sc1 lds\n"
"v_mfma_f32_16x16x32_bf16 v[216:219], a[152:155], v[204:207], v[216:219]\n"
"ds_read_b128 v[180:183], v2 offset:10240\n"
"v_mfma_f32_16x16x32_bf16 v[220:223], a[156:159], v[204:207], v[220:223]\n"
"v_mfma_f32_16x16x32_bf16 v[220:223], a[148:151], v[200:203], v[220:223]\n"
"ds_read_b128 v[184:187], v2 offset:19456\n"
"v_mfma_f32_16x16x32_bf16 v[220:223], a[140:143], v[196:199], v[220:223]\n"
"v_mfma_f32_16x16x32_bf16 v[220:223], a[132:135], v[192:195], v[220:223]\n"
"s_waitcnt vmcnt(9)\n"
"ds_read_b128 v[188:191], v2 offset:28672\n"
"v_mfma_f32_16x16x32_bf16 v[104:107], a[160:163], v[160:163], v[104:107]\n"
"v_mfma_f32_16x16x32_bf16 v[104:107], a[168:171], v[164:167], v[104:107]\n"
"s_waitcnt lgkmcnt(2)\n"
"ds_read_b128 v[192:195], v2 offset:2048\n"
"v_mfma_f32_16x16x32_bf16 v[104:107], a[176:179], v[168:171], v[104:107]\n"
"s_mov_b32 s53, 114688\n"
"buffer_load_dwordx4 a[192:195], v3, s[44:47], s53 offen offset:0 sc1\n"
"v_mfma_f32_16x16x32_bf16 v[104:107], a[184:187], v[172:175], v[104:107]\n"
"ds_read_b128 v[196:199], v2 offset:11264\n"
"v_mfma_f32_16x16x32_bf16 v[108:111], a[188:191], v[172:175], v[108:111]\n"
"v_mfma_f32_16x16x32_bf16 v[108:111], a[180:183], v[168:171], v[108:111]\n"
"ds_read_b128 v[200:203], v2 offset:20480\n"
"v_mfma_f32_16x16x32_bf16 v[108:111], a[172:175], v[164:167], v[108:111]\n"
"buffer_load_dwordx4 a[196:199], v3, s[44:47], s53 offen offset:1024 sc1\n"
"v_mfma_f32_16x16x32_bf16 v[108:111], a[164:167], v[160:163], v[108:111]\n"
"ds_read_b128 v[204:207], v2 offset:29696\n"
"v_mfma_f32_16x16x32_bf16 v[116:119], a[164:167], v[176:179], v[116:119]\n"
"v_mfma_f32_16x16x32_bf16 v[116:119], a[172:175], v[180:183], v[116:119]\n"
"s_waitcnt lgkmcnt(2)\n"
"ds_read_b128 v[160:163], v2 offset:3072\n"
"v_mfma_f32_16x16x32_bf16 v[116:119], a[180:183], v[184:187], v[116:119]\n"
"buffer_load_dwordx4 a[200:203], v3, s[44:47], s53 offen offset:2048 sc1\n"
"v_mfma_f32_16x16x32_bf16 v[116:119], a[188:191], v[188:191], v[116:119]\n"
"ds_read_b128 v[164:167], v2 offset:12288\n"
"v_mfma_f32_16x16x32_bf16 v[112:115], a[184:187], v[188:191], v[112:115]\n"
"v_mfma_f32_16x16x32_bf16 v[112:115], a[176:179], v[184:187], v[112:115]\n"
"ds_read_b128 v[168:171], v2 offset:21504\n"
"v_mfma_f32_16x16x32_bf16 v[112:115], a[168:171], v[180:183], v[112:115]\n"
"buffer_load_dwordx4 a[204:207], v3, s[44:47], s53 offen offset:3072 sc1\n"
"v_mfma_f32_16x16x32_bf16 v[112:115], a[160:163], v[176:179], v[112:115]\n"
"ds_read_b128 v[172:175], v2 offset:30720\n"
"v_mfma_f32_16x16x32_bf16 v[120:123], a[160:163], v[192:195], v[120:123]\n"
"v_mfma_f32_16x16x32_bf16 v[120:123], a[168:171], v[196:199], v[120:123]\n"
"s_waitcnt lgkmcnt(2)\n"
"ds_read_b128 v[176:179], v2 offset:4096\n"
"v_mfma_f32_16x16x32_bf16 v[120:123], a[176:179], v[200:203], v[120:123]\n"
"s_mov_b32 s53, 118784\n"
"buffer_load_dwordx4 a[208:211], v3, s[44:47], s53 offen offset:0 sc1\n"
"v_mfma_f32_16x16x32_bf16 v[120:123], a[184:187], v[204:207], v[120:123]\n"
"ds_read_b128 v[180:183], v2 offset:13312\n"
"v_mfma_f32_16x16x32_bf16 v[124:127], a[188:191], v[204:207], v[124:127]\n"
"v_mfma_f32_16x16x32_bf16 v[124:127], a[180:183], v[200:203], v[124:127]\n"
"ds_read_b128 v[184:187], v2 offset:22528\n"
"v_mfma_f32_16x16x32_bf16 v[124:127], a[172:175], v[196:199], v[124:127]\n"
"buffer_load_dwordx4 a[212:215], v3, s[44:47], s53 offen offset:1024 sc1\n"
"v_mfma_f32_16x16x32_bf16 v[124:127], a[164:167], v[192:195], v[124:127]\n"
"ds_read_b128 v[188:191], v2 offset:31744\n"
"v_mfma_f32_16x16x32_bf16 v[132:135], a[164:167], v[160:163], v[132:135]\n"
"v_mfma_f32_16x16x32_bf16 v[132:135], a[172:175], v[164:167], v[132:135]\n"
"s_waitcnt lgkmcnt(2)\n"
"ds_read_b128 v[192:195], v2 offset:5120\n"
"v_mfma_f32_16x16x32_bf16 v[132:135], a[180:183], v[168:171], v[132:135]\n"
"buffer_load_dwordx4 a[216:219], v3, s[44:47], s53 offen offset:2048 sc1\n"
"v_mfma_f32_16x16x32_bf16 v[132:135], a[188:191], v[172:175], v[132:135]\n"
"ds_read_b128 v[196:199], v2 offset:14336\n"
"v_mfma_f32_16x16x32_bf16 v[128:131], a[184:187], v[172:175], v[128:131]\n"
"v_mfma_f32_16x16x32_bf16 v[128:131], a[176:179], v[168:171], v[128:131]\n"
"ds_read_b128 v[200:203], v2 offset:23552\n"
"v_mfma_f32_16x16x32_bf16 v[128:131], a[168:171], v[164:167], v[128:131]\n"
"buffer_load_dwordx4 a[220:223], v3, s[44:47], s53 offen offset:3072 sc1\n"
"v_mfma_f32_16x16x32_bf16 v[128:131], a[160:163], v[160:163], v[128:131]\n"
"ds_read_b128 v[204:207], v2 offset:32768\n"
"v_mfma_f32_16x16x32_bf16 v[136:139], a[160:163], v[176:179], v[136:139]\n"
"v_mfma_f32_16x16x32_bf16 v[136:139], a[168:171], v[180:183], v[136:139]\n"
"s_waitcnt lgkmcnt(2)\n"
"s_add_u32 m0, s84, 36864\n"
"ds_read_b128 v[160:163], v2 offset:6144\n"
"v_mfma_f32_16x16x32_bf16 v[136:139], a[176:179], v[184:187], v[136:139]\n"
"s_mov_b32 s52, 552960\n"
"buffer_load_dwordx4 v30, s[28:31], s52 offen offset:0 sc1 lds\n"
"v_mfma_f32_16x16x32_bf16 v[136:139], a[184:187], v[188:191], v[136:139]\n"
"ds_read_b128 v[164:167], v2 offset:15360\n"
"v_mfma_f32_16x16x32_bf16 v[140:143], a[188:191], v[188:191], v[140:143]\n"
"v_mfma_f32_16x16x32_bf16 v[140:143], a[180:183], v[184:187], v[140:143]\n"
"ds_read_b128 v[168:171], v2 offset:24576\n"
"v_mfma_f32_16x16x32_bf16 v[140:143], a[172:175], v[180:183], v[140:143]\n"
"buffer_load_dwordx4 v30, s[28:31], s52 offen offset:1024 sc1 lds\n"
"v_mfma_f32_16x16x32_bf16 v[140:143], a[164:167], v[176:179], v[140:143]\n"
"ds_read_b128 v[172:175], v2 offset:33792\n"
"v_mfma_f32_16x16x32_bf16 v[148:151], a[164:167], v[192:195], v[148:151]\n"
"v_mfma_f32_16x16x32_bf16 v[148:151], a[172:175], v[196:199], v[148:151]\n"
"s_waitcnt lgkmcnt(2)\n"
"ds_read_b128 v[176:179], v2 offset:7168\n"
"v_mfma_f32_16x16x32_bf16 v[148:151], a[180:183], v[200:203], v[148:151]\n"
"buffer_load_dwordx4 v30, s[28:31], s52 offen offset:2048 sc1 lds\n"
"v_mfma_f32_16x16x32_bf16 v[148:151], a[188:191], v[204:207], v[148:151]\n"
"ds_read_b128 v[180:183], v2 offset:16384\n"
"v_mfma_f32_16x16x32_bf16 v[144:147], a[184:187], v[204:207], v[144:147]\n"
"v_mfma_f32_16x16x32_bf16 v[144:147], a[176:179], v[200:203], v[144:147]\n"
"ds_read_b128 v[184:187], v2 offset:25600\n"
"v_mfma_f32_16x16x32_bf16 v[144:147], a[168:171], v[196:199], v[144:147]\n"
"buffer_load_dwordx4 v30, s[28:31], s52 offen offset:3072 sc1 lds\n"
"v_mfma_f32_16x16x32_bf16 v[144:147], a[160:163], v[192:195], v[144:147]\n"
"ds_read_b128 v[188:191], v2 offset:34816\n"
"v_mfma_f32_16x16x32_bf16 v[152:155], a[160:163], v[160:163], v[152:155]\n"
"v_mfma_f32_16x16x32_bf16 v[152:155], a[168:171], v[164:167], v[152:155]\n"
"s_waitcnt lgkmcnt(2)\n"
"s_add_u32 m0, s84, 40960\n"
"ds_read_b128 v[192:195], v2 offset:8192\n"
"v_mfma_f32_16x16x32_bf16 v[152:155], a[176:179], v[168:171], v[152:155]\n"
"s_mov_b32 s52, 557056\n"
"buffer_load_dwordx4 v30, s[28:31], s52 offen offset:0 sc1 lds\n"
"v_mfma_f32_16x16x32_bf16 v[152:155], a[184:187], v[172:175], v[152:155]\n"
"ds_read_b128 v[196:199], v2 offset:17408\n"
"v_mfma_f32_16x16x32_bf16 v[156:159], a[188:191], v[172:175], v[156:159]\n"
"v_mfma_f32_16x16x32_bf16 v[156:159], a[180:183], v[168:171], v[156:159]\n"
"ds_read_b128 v[200:203], v2 offset:26624\n"
"v_mfma_f32_16x16x32_bf16 v[156:159], a[172:175], v[164:167], v[156:159]\n"
"buffer_load_dwordx4 v30, s[28:31], s52 offen offset:1024 sc1 lds\n"
"v_mfma_f32_16x16x32_bf16 v[156:159], a[164:167], v[160:163], v[156:159]\n"
"ds_read_b128 v[204:207], v2 offset:35840\n"
"v_add_u32 v2, 0xfffe1000, v2\n"
"v_mfma_f32_16x16x32_bf16 v[212:215], a[164:167], v[176:179], v[212:215]\n"
"s_waitcnt vmcnt(14)\n"
"s_barrier\n"
"v_mfma_f32_16x16x32_bf16 v[212:215], a[172:175], v[180:183], v[212:215]\n"
"s_waitcnt lgkmcnt(2)\n"
"ds_read_b128 v[160:163], v2 offset:0\n"
"v_mfma_f32_16x16x32_bf16 v[212:215], a[180:183], v[184:187], v[212:215]\n"
"buffer_load_dwordx4 v30, s[28:31], s52 offen offset:2048 sc1 lds\n"
"v_mfma_f32_16x16x32_bf16 v[212:215], a[188:191], v[188:191], v[212:215]\n"
"ds_read_b128 v[164:167], v2 offset:9216\n"
"v_mfma_f32_16x16x32_bf16 v[208:211], a[184:187], v[188:191], v[208:211]\n"
"v_mfma_f32_16x16x32_bf16 v[208:211], a[176:179], v[184:187], v[208:211]\n"
"ds_read_b128 v[168:171], v2 offset:18432\n"
"v_mfma_f32_16x16x32_bf16 v[208:211], a[168:171], v[180:183], v[208:211]\n"
"buffer_load_dwordx4 v30, s[28:31], s52 offen offset:3072 sc1 lds\n"
"v_mfma_f32_16x16x32_bf16 v[208:211], a[160:163], v[176:179], v[208:211]\n"
"ds_read_b128 v[172:175], v2 offset:27648\n"
"v_mfma_f32_16x16x32_bf16 v[216:219], a[160:163], v[192:195], v[216:219]\n"
"v_mfma_f32_16x16x32_bf16 v[216:219], a[168:171], v[196:199], v[216:219]\n"
"s_waitcnt lgkmcnt(2)\n"
"s_add_u32 m0, s84, 45056\n"
"ds_read_b128 v[176:179], v2 offset:1024\n"
"v_mfma_f32_16x16x32_bf16 v[216:219], a[176:179], v[200:203], v[216:219]\n"
"s_mov_b32 s52, 561152\n"
"buffer_load_dwordx4 v30, s[28:31], s52 offen offset:0 sc1 lds\n"
"v_mfma_f32_16x16x32_bf16 v[216:219], a[184:187], v[204:207], v[216:219]\n"
"ds_read_b128 v[180:183], v2 offset:10240\n"
"v_mfma_f32_16x16x32_bf16 v[220:223], a[188:191], v[204:207], v[220:223]\n"
"v_mfma_f32_16x16x32_bf16 v[220:223], a[180:183], v[200:203], v[220:223]\n"
"ds_read_b128 v[184:187], v2 offset:19456\n"
"v_mfma_f32_16x16x32_bf16 v[220:223], a[172:175], v[196:199], v[220:223]\n"
"v_mfma_f32_16x16x32_bf16 v[220:223], a[164:167], v[192:195], v[220:223]\n"
"s_waitcnt vmcnt(9)\n"
"ds_read_b128 v[188:191], v2 offset:28672\n"
"v_mfma_f32_16x16x32_bf16 v[104:107], a[192:195], v[160:163], v[104:107]\n"
"v_mfma_f32_16x16x32_bf16 v[104:107], a[200:203], v[164:167], v[104:107]\n"
"s_waitcnt lgkmcnt(2)\n"
"ds_read_b128 v[192:195], v2 offset:2048\n"
"v_mfma_f32_16x16x32_bf16 v[104:107], a[208:211], v[168:171], v[104:107]\n"
"s_mov_b32 s53, 122880\n"
"buffer_load_dwordx4 a[128:131], v3, s[44:47], s53 offen offset:0 sc1\n"
"v_mfma_f32_16x16x32_bf16 v[104:107], a[216:219], v[172:175], v[104:107]\n"
"ds_read_b128 v[196:199], v2 offset:11264\n"
"v_mfma_f32_16x16x32_bf16 v[108:111], a[220:223], v[172:175], v[108:111]\n"
"v_mfma_f32_16x16x32_bf16 v[108:111], a[212:215], v[168:171], v[108:111]\n"
"ds_read_b128 v[200:203], v2 offset:20480\n"
"v_mfma_f32_16x16x32_bf16 v[108:111], a[204:207], v[164:167], v[108:111]\n"
"buffer_load_dwordx4 a[132:135], v3, s[44:47], s53 offen offset:1024 sc1\n"
"v_mfma_f32_16x16x32_bf16 v[108:111], a[196:199], v[160:163], v[108:111]\n"
"ds_read_b128 v[204:207], v2 offset:29696\n"
"v_mfma_f32_16x16x32_bf16 v[116:119], a[196:199], v[176:179], v[116:119]\n"
"v_mfma_f32_16x16x32_bf16 v[116:119], a[204:207], v[180:183], v[116:119]\n"
"s_waitcnt lgkmcnt(2)\n"
"ds_read_b128 v[160:163], v2 offset:3072\n"
"v_mfma_f32_16x16x32_bf16 v[116:119], a[212:215], v[184:187], v[116:119]\n"
"buffer_load_dwordx4 a[136:139], v3, s[44:47], s53 offen offset:2048 sc1\n"
"v_mfma_f32_16x16x32_bf16 v[116:119], a[220:223], v[188:191], v[116:119]\n"
"ds_read_b128 v[164:167], v2 offset:12288\n"
"v_mfma_f32_16x16x32_bf16 v[112:115], a[216:219], v[188:191], v[112:115]\n"
"v_mfma_f32_16x16x32_bf16 v[112:115], a[208:211], v[184:187], v[112:115]\n"
"ds_read_b128 v[168:171], v2 offset:21504\n"
"v_mfma_f32_16x16x32_bf16 v[112:115], a[200:203], v[180:183], v[112:115]\n"
"buffer_load_dwordx4 a[140:143], v3, s[44:47], s53 offen offset:3072 sc1\n"
"v_mfma_f32_16x16x32_bf16 v[112:115], a[192:195], v[176:179], v[112:115]\n"
"ds_read_b128 v[172:175], v2 offset:30720\n"
"v_mfma_f32_16x16x32_bf16 v[120:123], a[192:195], v[192:195], v[120:123]\n"
"v_mfma_f32_16x16x32_bf16 v[120:123], a[200:203], v[196:199], v[120:123]\n"
"s_waitcnt lgkmcnt(2)\n"
"ds_read_b128 v[176:179], v2 offset:4096\n"
"v_mfma_f32_16x16x32_bf16 v[120:123], a[208:211], v[200:203], v[120:123]\n"
"s_mov_b32 s53, 126976\n"
"buffer_load_dwordx4 a[144:147], v3, s[44:47], s53 offen offset:0 sc1\n"
"v_mfma_f32_16x16x32_bf16 v[120:123], a[216:219], v[204:207], v[120:123]\n"
"ds_read_b128 v[180:183], v2 offset:13312\n"
"v_mfma_f32_16x16x32_bf16 v[124:127], a[220:223], v[204:207], v[124:127]\n"
"v_mfma_f32_16x16x32_bf16 v[124:127], a[212:215], v[200:203], v[124:127]\n"
"ds_read_b128 v[184:187], v2 offset:22528\n"
"v_mfma_f32_16x16x32_bf16 v[124:127], a[204:207], v[196:199], v[124:127]\n"
"buffer_load_dwordx4 a[148:151], v3, s[44:47], s53 offen offset:1024 sc1\n"
"v_mfma_f32_16x16x32_bf16 v[124:127], a[196:199], v[192:195], v[124:127]\n"
"ds_read_b128 v[188:191], v2 offset:31744\n"
"v_mfma_f32_16x16x32_bf16 v[132:135], a[196:199], v[160:163], v[132:135]\n"
"v_mfma_f32_16x16x32_bf16 v[132:135], a[204:207], v[164:167], v[132:135]\n"
"s_waitcnt lgkmcnt(2)\n"
"ds_read_b128 v[192:195], v2 offset:5120\n"
"v_mfma_f32_16x16x32_bf16 v[132:135], a[212:215], v[168:171], v[132:135]\n"
"buffer_load_dwordx4 a[152:155], v3, s[44:47], s53 offen offset:2048 sc1\n"
"v_mfma_f32_16x16x32_bf16 v[132:135], a[220:223], v[172:175], v[132:135]\n"
"ds_read_b128 v[196:199], v2 offset:14336\n"
"v_mfma_f32_16x16x32_bf16 v[128:131], a[216:219], v[172:175], v[128:131]\n"
"v_mfma_f32_16x16x32_bf16 v[128:131], a[208:211], v[168:171], v[128:131]\n"
"ds_read_b128 v[200:203], v2 offset:23552\n"
"v_mfma_f32_16x16x32_bf16 v[128:131], a[200:203], v[164:167], v[128:131]\n"
"buffer_load_dwordx4 a[156:159], v3, s[44:47], s53 offen offset:3072 sc1\n"
"v_mfma_f32_16x16x32_bf16 v[128:131], a[192:195], v[160:163], v[128:131]\n"
"ds_read_b128 v[204:207], v2 offset:32768\n"
"v_mfma_f32_16x16x32_bf16 v[136:139], a[192:195], v[176:179], v[136:139]\n"
"v_mfma_f32_16x16x32_bf16 v[136:139], a[200:203], v[180:183], v[136:139]\n"
"s_waitcnt lgkmcnt(2)\n"
"s_add_u32 m0, s84, 90112\n"
"ds_read_b128 v[160:163], v2 offset:6144\n"
"v_mfma_f32_16x16x32_bf16 v[136:139], a[208:211], v[184:187], v[136:139]\n"
"s_mov_b32 s52, 589824\n"
"buffer_load_dwordx4 v30, s[28:31], s52 offen offset:0 sc1 lds\n"
"v_mfma_f32_16x16x32_bf16 v[136:139], a[216:219], v[188:191], v[136:139]\n"
"ds_read_b128 v[164:167], v2 offset:15360\n"
"v_mfma_f32_16x16x32_bf16 v[140:143], a[220:223], v[188:191], v[140:143]\n"
"v_mfma_f32_16x16x32_bf16 v[140:143], a[212:215], v[184:187], v[140:143]\n"
"ds_read_b128 v[168:171], v2 offset:24576\n"
"v_mfma_f32_16x16x32_bf16 v[140:143], a[204:207], v[180:183], v[140:143]\n"
"buffer_load_dwordx4 v30, s[28:31], s52 offen offset:1024 sc1 lds\n"
"v_mfma_f32_16x16x32_bf16 v[140:143], a[196:199], v[176:179], v[140:143]\n"
"ds_read_b128 v[172:175], v2 offset:33792\n"
"v_mfma_f32_16x16x32_bf16 v[148:151], a[196:199], v[192:195], v[148:151]\n"
"v_mfma_f32_16x16x32_bf16 v[148:151], a[204:207], v[196:199], v[148:151]\n"
"s_waitcnt lgkmcnt(2)\n"
"ds_read_b128 v[176:179], v2 offset:7168\n"
"v_mfma_f32_16x16x32_bf16 v[148:151], a[212:215], v[200:203], v[148:151]\n"
"buffer_load_dwordx4 v30, s[28:31], s52 offen offset:2048 sc1 lds\n"
"v_mfma_f32_16x16x32_bf16 v[148:151], a[220:223], v[204:207], v[148:151]\n"
"ds_read_b128 v[180:183], v2 offset:16384\n"
"v_mfma_f32_16x16x32_bf16 v[144:147], a[216:219], v[204:207], v[144:147]\n"
"v_mfma_f32_16x16x32_bf16 v[144:147], a[208:211], v[200:203], v[144:147]\n"
"ds_read_b128 v[184:187], v2 offset:25600\n"
"v_mfma_f32_16x16x32_bf16 v[144:147], a[200:203], v[196:199], v[144:147]\n"
"buffer_load_dwordx4 v30, s[28:31], s52 offen offset:3072 sc1 lds\n"
"v_mfma_f32_16x16x32_bf16 v[144:147], a[192:195], v[192:195], v[144:147]\n"
"ds_read_b128 v[188:191], v2 offset:34816\n"
"v_mfma_f32_16x16x32_bf16 v[152:155], a[192:195], v[160:163], v[152:155]\n"
"v_mfma_f32_16x16x32_bf16 v[152:155], a[200:203], v[164:167], v[152:155]\n"
"s_waitcnt lgkmcnt(2)\n"
"s_add_u32 m0, s84, 94208\n"
"ds_read_b128 v[192:195], v2 offset:8192\n"
"v_mfma_f32_16x16x32_bf16 v[152:155], a[208:211], v[168:171], v[152:155]\n"
"s_mov_b32 s52, 593920\n"
"buffer_load_dwordx4 v30, s[28:31], s52 offen offset:0 sc1 lds\n"
"v_mfma_f32_16x16x32_bf16 v[152:155], a[216:219], v[172:175], v[152:155]\n"
"ds_read_b128 v[196:199], v2 offset:17408\n"
"v_mfma_f32_16x16x32_bf16 v[156:159], a[220:223], v[172:175], v[156:159]\n"
"v_mfma_f32_16x16x32_bf16 v[156:159], a[212:215], v[168:171], v[156:159]\n"
"ds_read_b128 v[200:203], v2 offset:26624\n"
"v_mfma_f32_16x16x32_bf16 v[156:159], a[204:207], v[164:167], v[156:159]\n"
"buffer_load_dwordx4 v30, s[28:31], s52 offen offset:1024 sc1 lds\n"
"v_mfma_f32_16x16x32_bf16 v[156:159], a[196:199], v[160:163], v[156:159]\n"
"ds_read_b128 v[204:207], v2 offset:35840\n"
"v_add_u32 v2, 0x9000, v2\n"
"v_mfma_f32_16x16x32_bf16 v[212:215], a[196:199], v[176:179], v[212:215]\n"
"s_waitcnt vmcnt(14)\n"
"s_barrier\n"
"v_mfma_f32_16x16x32_bf16 v[212:215], a[204:207], v[180:183], v[212:215]\n"
"s_waitcnt lgkmcnt(2)\n"
"ds_read_b128 v[160:163], v2 offset:0\n"
"v_mfma_f32_16x16x32_bf16 v[212:215], a[212:215], v[184:187], v[212:215]\n"
"buffer_load_dwordx4 v30, s[28:31], s52 offen offset:2048 sc1 lds\n"
"v_mfma_f32_16x16x32_bf16 v[212:215], a[220:223], v[188:191], v[212:215]\n"
"ds_read_b128 v[164:167], v2 offset:9216\n"
"v_mfma_f32_16x16x32_bf16 v[208:211], a[216:219], v[188:191], v[208:211]\n"
"v_mfma_f32_16x16x32_bf16 v[208:211], a[208:211], v[184:187], v[208:211]\n"
"ds_read_b128 v[168:171], v2 offset:18432\n"
"v_mfma_f32_16x16x32_bf16 v[208:211], a[200:203], v[180:183], v[208:211]\n"
"buffer_load_dwordx4 v30, s[28:31], s52 offen offset:3072 sc1 lds\n"
"v_mfma_f32_16x16x32_bf16 v[208:211], a[192:195], v[176:179], v[208:211]\n"
"ds_read_b128 v[172:175], v2 offset:27648\n"
"v_mfma_f32_16x16x32_bf16 v[216:219], a[192:195], v[192:195], v[216:219]\n"
"v_mfma_f32_16x16x32_bf16 v[216:219], a[200:203], v[196:199], v[216:219]\n"
"s_waitcnt lgkmcnt(2)\n"
"s_add_u32 m0, s84, 98304\n"
"ds_read_b128 v[176:179], v2 offset:1024\n"
"v_mfma_f32_16x16x32_bf16 v[216:219], a[208:211], v[200:203], v[216:219]\n"
"s_mov_b32 s52, 598016\n"
"buffer_load_dwordx4 v30, s[28:31], s52 offen offset:0 sc1 lds\n"
"v_mfma_f32_16x16x32_bf16 v[216:219], a[216:219], v[204:207], v[216:219]\n"
"ds_read_b128 v[180:183], v2 offset:10240\n"
"v_mfma_f32_16x16x32_bf16 v[220:223], a[220:223], v[204:207], v[220:223]\n"
"v_mfma_f32_16x16x32_bf16 v[220:223], a[212:215], v[200:203], v[220:223]\n"
"ds_read_b128 v[184:187], v2 offset:19456\n"
"v_mfma_f32_16x16x32_bf16 v[220:223], a[204:207], v[196:199], v[220:223]\n"
"v_mfma_f32_16x16x32_bf16 v[220:223], a[196:199], v[192:195], v[220:223]\n"
"s_waitcnt vmcnt(9)\n"
"ds_read_b128 v[188:191], v2 offset:28672\n"
"v_mfma_f32_16x16x32_bf16 v[104:107], a[128:131], v[160:163], v[104:107]\n"
"v_mfma_f32_16x16x32_bf16 v[104:107], a[136:139], v[164:167], v[104:107]\n"
"s_waitcnt lgkmcnt(2)\n"
"ds_read_b128 v[192:195], v2 offset:2048\n"
"v_mfma_f32_16x16x32_bf16 v[104:107], a[144:147], v[168:171], v[104:107]\n"
"s_mov_b32 s53, 131072\n"
"buffer_load_dwordx4 a[160:163], v3, s[44:47], s53 offen offset:0 sc1\n"
"v_mfma_f32_16x16x32_bf16 v[104:107], a[152:155], v[172:175], v[104:107]\n"
"ds_read_b128 v[196:199], v2 offset:11264\n"
"v_mfma_f32_16x16x32_bf16 v[108:111], a[156:159], v[172:175], v[108:111]\n"
"v_mfma_f32_16x16x32_bf16 v[108:111], a[148:151], v[168:171], v[108:111]\n"
"ds_read_b128 v[200:203], v2 offset:20480\n"
"v_mfma_f32_16x16x32_bf16 v[108:111], a[140:143], v[164:167], v[108:111]\n"
"buffer_load_dwordx4 a[164:167], v3, s[44:47], s53 offen offset:1024 sc1\n"
"v_mfma_f32_16x16x32_bf16 v[108:111], a[132:135], v[160:163], v[108:111]\n"
"ds_read_b128 v[204:207], v2 offset:29696\n"
"v_mfma_f32_16x16x32_bf16 v[116:119], a[132:135], v[176:179], v[116:119]\n"
"v_mfma_f32_16x16x32_bf16 v[116:119], a[140:143], v[180:183], v[116:119]\n"
"s_waitcnt lgkmcnt(2)\n"
"ds_read_b128 v[160:163], v2 offset:3072\n"
"v_mfma_f32_16x16x32_bf16 v[116:119], a[148:151], v[184:187], v[116:119]\n"
"buffer_load_dwordx4 a[168:171], v3, s[44:47], s53 offen offset:2048 sc1\n"
"v_mfma_f32_16x16x32_bf16 v[116:119], a[156:159], v[188:191], v[116:119]\n"
"ds_read_b128 v[164:167], v2 offset:12288\n"
"v_mfma_f32_16x16x32_bf16 v[112:115], a[152:155], v[188:191], v[112:115]\n"
"v_mfma_f32_16x16x32_bf16 v[112:115], a[144:147], v[184:187], v[112:115]\n"
"ds_read_b128 v[168:171], v2 offset:21504\n"
"v_mfma_f32_16x16x32_bf16 v[112:115], a[136:139], v[180:183], v[112:115]\n"
"buffer_load_dwordx4 a[172:175], v3, s[44:47], s53 offen offset:3072 sc1\n"
"v_mfma_f32_16x16x32_bf16 v[112:115], a[128:131], v[176:179], v[112:115]\n"
"ds_read_b128 v[172:175], v2 offset:30720\n"
"v_mfma_f32_16x16x32_bf16 v[120:123], a[128:131], v[192:195], v[120:123]\n"
"v_mfma_f32_16x16x32_bf16 v[120:123], a[136:139], v[196:199], v[120:123]\n"
"s_waitcnt lgkmcnt(2)\n"
"ds_read_b128 v[176:179], v2 offset:4096\n"
"v_mfma_f32_16x16x32_bf16 v[120:123], a[144:147], v[200:203], v[120:123]\n"
"s_mov_b32 s53, 135168\n"
"buffer_load_dwordx4 a[176:179], v3, s[44:47], s53 offen offset:0 sc1\n"
"v_mfma_f32_16x16x32_bf16 v[120:123], a[152:155], v[204:207], v[120:123]\n"
"ds_read_b128 v[180:183], v2 offset:13312\n"
"v_mfma_f32_16x16x32_bf16 v[124:127], a[156:159], v[204:207], v[124:127]\n"
"v_mfma_f32_16x16x32_bf16 v[124:127], a[148:151], v[200:203], v[124:127]\n"
"ds_read_b128 v[184:187], v2 offset:22528\n"
"v_mfma_f32_16x16x32_bf16 v[124:127], a[140:143], v[196:199], v[124:127]\n"
"buffer_load_dwordx4 a[180:183], v3, s[44:47], s53 offen offset:1024 sc1\n"
"v_mfma_f32_16x16x32_bf16 v[124:127], a[132:135], v[192:195], v[124:127]\n"
"ds_read_b128 v[188:191], v2 offset:31744\n"
"v_mfma_f32_16x16x32_bf16 v[132:135], a[132:135], v[160:163], v[132:135]\n"
"v_mfma_f32_16x16x32_bf16 v[132:135], a[140:143], v[164:167], v[132:135]\n"
"s_waitcnt lgkmcnt(2)\n"
"ds_read_b128 v[192:195], v2 offset:5120\n"
"v_mfma_f32_16x16x32_bf16 v[132:135], a[148:151], v[168:171], v[132:135]\n"
"buffer_load_dwordx4 a[184:187], v3, s[44:47], s53 offen offset:2048 sc1\n"
"v_mfma_f32_16x16x32_bf16 v[132:135], a[156:159], v[172:175], v[132:135]\n"
"ds_read_b128 v[196:199], v2 offset:14336\n"
"v_mfma_f32_16x16x32_bf16 v[128:131], a[152:155], v[172:175], v[128:131]\n"
"v_mfma_f32_16x16x32_bf16 v[128:131], a[144:147], v[168:171], v[128:131]\n"
"ds_read_b128 v[200:203], v2 offset:23552\n"
"v_mfma_f32_16x16x32_bf16 v[128:131], a[136:139], v[164:167], v[128:131]\n"
"buffer_load_dwordx4 a[188:191], v3, s[44:47], s53 offen offset:3072 sc1\n"
"v_mfma_f32_16x16x32_bf16 v[128:131], a[128:131], v[160:163], v[128:131]\n"
"ds_read_b128 v[204:207], v2 offset:32768\n"
"v_mfma_f32_16x16x32_bf16 v[136:139], a[128:131], v[176:179], v[136:139]\n"
"v_mfma_f32_16x16x32_bf16 v[136:139], a[136:139], v[180:183], v[136:139]\n"
"s_waitcnt lgkmcnt(2)\n"
"ds_read_b128 v[160:163], v2 offset:6144\n"
"v_mfma_f32_16x16x32_bf16 v[136:139], a[144:147], v[184:187], v[136:139]\n"
"v_mfma_f32_16x16x32_bf16 v[136:139], a[152:155], v[188:191], v[136:139]\n"
"s_add_u32 m0, s48, 126976\n"
"ds_read_b128 v[164:167], v2 offset:15360\n"
"v_mfma_f32_16x16x32_bf16 v[140:143], a[156:159], v[188:191], v[140:143]\n"
"buffer_load_dwordx4 v4, s[32:35], s50 offen lds\n"
"v_mfma_f32_16x16x32_bf16 v[140:143], a[148:151], v[184:187], v[140:143]\n"
"s_add_u32 m0, s48, 128000\n"
"ds_read_b128 v[168:171], v2 offset:24576\n"
"v_mfma_f32_16x16x32_bf16 v[140:143], a[140:143], v[180:183], v[140:143]\n"
"buffer_load_dwordx4 v4, s[32:35], s49 offen lds\n"
"v_mfma_f32_16x16x32_bf16 v[140:143], a[132:135], v[176:179], v[140:143]\n"
".LPAD16:\n"
"s_cbranch_execz .LPAD19\n"
"ds_read_b128 v[172:175], v2 offset:33792\n"
"v_mfma_f32_16x16x32_bf16 v[148:151], a[132:135], v[192:195], v[148:151]\n"
"s_add_u32 m0, s48, 129024\n"
"s_nop 0\n"
"buffer_load_dwordx4 v17, s[32:35], s82 offen lds\n"
"v_mfma_f32_16x16x32_bf16 v[148:151], a[140:143], v[196:199], v[148:151]\n"
"s_waitcnt lgkmcnt(2)\n"
"ds_read_b128 v[176:179], v2 offset:7168\n"
"v_mfma_f32_16x16x32_bf16 v[148:151], a[148:151], v[200:203], v[148:151]\n"
"v_mfma_f32_16x16x32_bf16 v[148:151], a[156:159], v[204:207], v[148:151]\n"
"ds_read_b128 v[180:183], v2 offset:16384\n"
"v_mfma_f32_16x16x32_bf16 v[144:147], a[152:155], v[204:207], v[144:147]\n"
"v_mfma_f32_16x16x32_bf16 v[144:147], a[144:147], v[200:203], v[144:147]\n"
"ds_read_b128 v[184:187], v2 offset:25600\n"
"v_mfma_f32_16x16x32_bf16 v[144:147], a[136:139], v[196:199], v[144:147]\n"
"v_mfma_f32_16x16x32_bf16 v[144:147], a[128:131], v[192:195], v[144:147]\n"
"ds_read_b128 v[188:191], v2 offset:34816\n"
"v_mfma_f32_16x16x32_bf16 v[152:155], a[128:131], v[160:163], v[152:155]\n"
"s_mul_i32 s54, s58, 55296\n"
"v_mfma_f32_16x16x32_bf16 v[152:155], a[136:139], v[164:167], v[152:155]\n"
"s_waitcnt lgkmcnt(2)\n"
"s_add_u32 s54, s54, 0\n"
"ds_read_b128 v[192:195], v2 offset:8192\n"
"v_mfma_f32_16x16x32_bf16 v[152:155], a[144:147], v[168:171], v[152:155]\n"
"buffer_load_dwordx4 v[52:55], v1, s[20:23], s54 offen sc1\n"
"v_mfma_f32_16x16x32_bf16 v[152:155], a[152:155], v[172:175], v[152:155]\n"
"ds_read_b128 v[196:199], v2 offset:17408\n"
"v_mfma_f32_16x16x32_bf16 v[156:159], a[156:159], v[172:175], v[156:159]\n"
"buffer_load_dwordx4 v[56:59], v1, s[20:23], s54 offen offset:1024 sc1\n"
"v_mfma_f32_16x16x32_bf16 v[156:159], a[148:151], v[168:171], v[156:159]\n"
"ds_read_b128 v[200:203], v2 offset:26624\n"
"v_mfma_f32_16x16x32_bf16 v[156:159], a[140:143], v[164:167], v[156:159]\n"
"buffer_load_dword v60, v5, s[20:23], s54 offen offset:2048 sc1\n"
"v_mfma_f32_16x16x32_bf16 v[156:159], a[132:135], v[160:163], v[156:159]\n"
"ds_read_b128 v[204:207], v2 offset:35840\n"
"v_add_u32 v2, 0xd000, v2\n"
"v_mfma_f32_16x16x32_bf16 v[212:215], a[132:135], v[176:179], v[212:215]\n"
"s_waitcnt vmcnt(14)\n"
"s_barrier\n"
"v_mfma_f32_16x16x32_bf16 v[212:215], a[140:143], v[180:183], v[212:215]\n"
"s_waitcnt lgkmcnt(2)\n"
"s_mul_i32 s54, s58, 55296\n"
"ds_read_b128 v[160:163], v2 offset:0\n"
"v_mfma_f32_16x16x32_bf16 v[212:215], a[148:151], v[184:187], v[212:215]\n"
"s_add_u32 s54, s54, 2304\n"
"buffer_load_dwordx4 v[64:67], v1, s[20:23], s54 offen sc1\n"
"v_mfma_f32_16x16x32_bf16 v[212:215], a[156:159], v[188:191], v[212:215]\n"
"ds_read_b128 v[164:167], v2 offset:9216\n"
"v_mfma_f32_16x16x32_bf16 v[208:211], a[152:155], v[188:191], v[208:211]\n"
"buffer_load_dwordx4 v[68:71], v1, s[20:23], s54 offen offset:1024 sc1\n"
"v_mfma_f32_16x16x32_bf16 v[208:211], a[144:147], v[184:187], v[208:211]\n"
"ds_read_b128 v[168:171], v2 offset:18432\n"
"v_mfma_f32_16x16x32_bf16 v[208:211], a[136:139], v[180:183], v[208:211]\n"
"buffer_load_dword v72, v5, s[20:23], s54 offen offset:2048 sc1\n"
"v_mfma_f32_16x16x32_bf16 v[208:211], a[128:131], v[176:179], v[208:211]\n"
"ds_read_b128 v[172:175], v2 offset:27648\n"
"v_mfma_f32_16x16x32_bf16 v[216:219], a[128:131], v[192:195], v[216:219]\n"
"s_mul_i32 s54, s58, 55296\n"
"v_mfma_f32_16x16x32_bf16 v[216:219], a[136:139], v[196:199], v[216:219]\n"
"s_waitcnt lgkmcnt(2)\n"
"s_add_u32 s54, s54, 4608\n"
"ds_read_b128 v[176:179], v2 offset:1024\n"
"v_mfma_f32_16x16x32_bf16 v[216:219], a[144:147], v[200:203], v[216:219]\n"
"buffer_load_dwordx4 v[76:79], v1, s[20:23], s54 offen sc1\n"
"v_mfma_f32_16x16x32_bf16 v[216:219], a[152:155], v[204:207], v[216:219]\n"
"ds_read_b128 v[180:183], v2 offset:10240\n"
"v_mfma_f32_16x16x32_bf16 v[220:223], a[156:159], v[204:207], v[220:223]\n"
"buffer_load_dwordx4 v[80:83], v1, s[20:23], s54 offen offset:1024 sc1\n"
"v_mfma_f32_16x16x32_bf16 v[220:223], a[148:151], v[200:203], v[220:223]\n"
"ds_read_b128 v[184:187], v2 offset:19456\n"
"v_mfma_f32_16x16x32_bf16 v[220:223], a[140:143], v[196:199], v[220:223]\n"
"buffer_load_dword v84, v5, s[20:23], s54 offen offset:2048 sc1\n"
"v_mfma_f32_16x16x32_bf16 v[220:223], a[132:135], v[192:195], v[220:223]\n"
"s_waitcnt vmcnt(12)\n"
"ds_read_b128 v[188:191], v2 offset:28672\n"
"v_mfma_f32_16x16x32_bf16 v[104:107], a[160:163], v[160:163], v[104:107]\n"
"v_mfma_f32_16x16x32_bf16 v[104:107], a[168:171], v[164:167], v[104:107]\n"
"s_waitcnt lgkmcnt(2)\n"
"ds_read_b128 v[192:195], v2 offset:2048\n"
"v_mfma_f32_16x16x32_bf16 v[104:107], a[176:179], v[168:171], v[104:107]\n"
"v_mfma_f32_16x16x32_bf16 v[104:107], a[184:187], v[172:175], v[104:107]\n"
"ds_read_b128 v[196:199], v2 offset:11264\n"
"v_mfma_f32_16x16x32_bf16 v[108:111], a[188:191], v[172:175], v[108:111]\n"
"v_mfma_f32_16x16x32_bf16 v[108:111], a[180:183], v[168:171], v[108:111]\n"
"ds_read_b128 v[200:203], v2 offset:20480\n"
"v_mfma_f32_16x16x32_bf16 v[108:111], a[172:175], v[164:167], v[108:111]\n"
"v_mfma_f32_16x16x32_bf16 v[108:111], a[164:167], v[160:163], v[108:111]\n"
"ds_read_b128 v[204:207], v2 offset:29696\n"
"v_mfma_f32_16x16x32_bf16 v[116:119], a[164:167], v[176:179], v[116:119]\n"
"v_mfma_f32_16x16x32_bf16 v[116:119], a[172:175], v[180:183], v[116:119]\n"
"s_waitcnt lgkmcnt(2)\n"
"ds_read_b128 v[160:163], v2 offset:3072\n"
"v_mfma_f32_16x16x32_bf16 v[116:119], a[180:183], v[184:187], v[116:119]\n"
"v_mfma_f32_16x16x32_bf16 v[116:119], a[188:191], v[188:191], v[116:119]\n"
"ds_read_b128 v[164:167], v2 offset:12288\n"
"v_mfma_f32_16x16x32_bf16 v[112:115], a[184:187], v[188:191], v[112:115]\n"
"v_cvt_pk_bf16_f32 v8, v105, v109\n"
"v_cvt_pk_bf16_f32 v20, v106, v110\n"
"v_mfma_f32_16x16x32_bf16 v[112:115], a[176:179], v[184:187], v[112:115]\n"
"ds_read_b128 v[168:171], v2 offset:21504\n"
"v_cvt_pk_bf16_f32 v224, v107, v111\n"
"v_mfma_f32_16x16x32_bf16 v[112:115], a[168:171], v[180:183], v[112:115]\n"
"v_mfma_f32_16x16x32_bf16 v[112:115], a[160:163], v[176:179], v[112:115]\n"
"ds_read_b128 v[172:175], v2 offset:30720\n"
"v_mfma_f32_16x16x32_bf16 v[120:123], a[160:163], v[192:195], v[120:123]\n"
"v_mfma_f32_16x16x32_bf16 v[120:123], a[168:171], v[196:199], v[120:123]\n"
"s_waitcnt lgkmcnt(2)\n"
"ds_read_b128 v[176:179], v2 offset:4096\n"
"v_mfma_f32_16x16x32_bf16 v[120:123], a[176:179], v[200:203], v[120:123]\n"
"v_mfma_f32_16x16x32_bf16 v[120:123], a[184:187], v[204:207], v[120:123]\n"
"ds_read_b128 v[180:183], v2 offset:13312\n"
"v_mfma_f32_16x16x32_bf16 v[124:127], a[188:191], v[204:207], v[124:127]\n"
"v_cvt_pk_bf16_f32 v9, v113, v117\n"
"v_cvt_pk_bf16_f32 v21, v114, v118\n"
"v_mfma_f32_16x16x32_bf16 v[124:127], a[180:183], v[200:203], v[124:127]\n"
"ds_read_b128 v[184:187], v2 offset:22528\n"
"v_cvt_pk_bf16_f32 v225, v115, v119\n"
"v_mfma_f32_16x16x32_bf16 v[124:127], a[172:175], v[196:199], v[124:127]\n"
"v_mfma_f32_16x16x32_bf16 v[124:127], a[164:167], v[192:195], v[124:127]\n"
"ds_read_b128 v[188:191], v2 offset:31744\n"
"v_mfma_f32_16x16x32_bf16 v[132:135], a[164:167], v[160:163], v[132:135]\n"
"v_mfma_f32_16x16x32_bf16 v[132:135], a[172:175], v[164:167], v[132:135]\n"
"s_waitcnt lgkmcnt(2)\n"
"ds_read_b128 v[192:195], v2 offset:5120\n"
"v_mfma_f32_16x16x32_bf16 v[132:135], a[180:183], v[168:171], v[132:135]\n"
"v_mfma_f32_16x16x32_bf16 v[132:135], a[188:191], v[172:175], v[132:135]\n"
"ds_read_b128 v[196:199], v2 offset:14336\n"
"v_mfma_f32_16x16x32_bf16 v[128:131], a[184:187], v[172:175], v[128:131]\n"
"v_cvt_pk_bf16_f32 v10, v121, v125\n"
"v_cvt_pk_bf16_f32 v22, v122, v126\n"
"v_mfma_f32_16x16x32_bf16 v[128:131], a[176:179], v[168:171], v[128:131]\n"
"ds_read_b128 v[200:203], v2 offset:23552\n"
"v_cvt_pk_bf16_f32 v226, v123, v127\n"
"v_mfma_f32_16x16x32_bf16 v[128:131], a[168:171], v[164:167], v[128:131]\n"
"v_mfma_f32_16x16x32_bf16 v[128:131], a[160:163], v[160:163], v[128:131]\n"
"ds_read_b128 v[204:207], v2 offset:32768\n"
"v_mfma_f32_16x16x32_bf16 v[136:139], a[160:163], v[176:179], v[136:139]\n"
"v_mfma_f32_16x16x32_bf16 v[136:139], a[168:171], v[180:183], v[136:139]\n"
"s_waitcnt lgkmcnt(2)\n"
"ds_read_b128 v[160:163], v2 offset:6144\n"
"v_mfma_f32_16x16x32_bf16 v[136:139], a[176:179], v[184:187], v[136:139]\n"
"v_mfma_f32_16x16x32_bf16 v[136:139], a[184:187], v[188:191], v[136:139]\n"
"ds_read_b128 v[164:167], v2 offset:15360\n"
"v_mfma_f32_16x16x32_bf16 v[140:143], a[188:191], v[188:191], v[140:143]\n"
"v_cvt_pk_bf16_f32 v11, v129, v133\n"
"v_cvt_pk_bf16_f32 v23, v130, v134\n"
"v_mfma_f32_16x16x32_bf16 v[140:143], a[180:183], v[184:187], v[140:143]\n"
"ds_read_b128 v[168:171], v2 offset:24576\n"
"v_cvt_pk_bf16_f32 v227, v131, v135\n"
"v_mfma_f32_16x16x32_bf16 v[140:143], a[172:175], v[180:183], v[140:143]\n"
"buffer_store_dwordx4 v[8:11], v1, s[20:23], s67 offen offset:0 sc1\n"
"v_mfma_f32_16x16x32_bf16 v[140:143], a[164:167], v[176:179], v[140:143]\n"
"ds_read_b128 v[172:175], v2 offset:33792\n"
"v_mfma_f32_16x16x32_bf16 v[148:151], a[164:167], v[192:195], v[148:151]\n"
"buffer_store_dwordx4 v[20:23], v1, s[20:23], s68 offen offset:0 sc1\n"
"v_mfma_f32_16x16x32_bf16 v[148:151], a[172:175], v[196:199], v[148:151]\n"
"s_waitcnt lgkmcnt(2)\n"
"ds_read_b128 v[176:179], v2 offset:7168\n"
"v_mfma_f32_16x16x32_bf16 v[148:151], a[180:183], v[200:203], v[148:151]\n"
"buffer_store_dwordx4 v[224:227], v1, s[20:23], s69 offen offset:0 sc1\n"
"v_mfma_f32_16x16x32_bf16 v[148:151], a[188:191], v[204:207], v[148:151]\n"
"ds_read_b128 v[180:183], v2 offset:16384\n"
"v_mfma_f32_16x16x32_bf16 v[144:147], a[184:187], v[204:207], v[144:147]\n"
"v_cvt_pk_bf16_f32 v12, v137, v141\n"
"v_cvt_pk_bf16_f32 v24, v138, v142\n"
"v_mfma_f32_16x16x32_bf16 v[144:147], a[176:179], v[200:203], v[144:147]\n"
"ds_read_b128 v[184:187], v2 offset:25600\n"
"v_cvt_pk_bf16_f32 v228, v139, v143\n"
"v_mfma_f32_16x16x32_bf16 v[144:147], a[168:171], v[196:199], v[144:147]\n"
"v_mfma_f32_16x16x32_bf16 v[144:147], a[160:163], v[192:195], v[144:147]\n"
"ds_read_b128 v[188:191], v2 offset:34816\n"
"v_mfma_f32_16x16x32_bf16 v[152:155], a[160:163], v[160:163], v[152:155]\n"
"v_mfma_f32_16x16x32_bf16 v[152:155], a[168:171], v[164:167], v[152:155]\n"
"s_waitcnt lgkmcnt(2)\n"
"ds_read_b128 v[192:195], v2 offset:8192\n"
"v_mfma_f32_16x16x32_bf16 v[152:155], a[176:179], v[168:171], v[152:155]\n"
"v_mfma_f32_16x16x32_bf16 v[152:155], a[184:187], v[172:175], v[152:155]\n"
"ds_read_b128 v[196:199], v2 offset:17408\n"
"v_mfma_f32_16x16x32_bf16 v[156:159], a[188:191], v[172:175], v[156:159]\n"
"v_cvt_pk_bf16_f32 v13, v145, v149\n"
"v_cvt_pk_bf16_f32 v25, v146, v150\n"
"v_mfma_f32_16x16x32_bf16 v[156:159], a[180:183], v[168:171], v[156:159]\n"
"ds_read_b128 v[200:203], v2 offset:26624\n"
"v_cvt_pk_bf16_f32 v229, v147, v151\n"
"v_mfma_f32_16x16x32_bf16 v[156:159], a[172:175], v[164:167], v[156:159]\n"
"v_mfma_f32_16x16x32_bf16 v[156:159], a[164:167], v[160:163], v[156:159]\n"
"ds_read_b128 v[204:207], v2 offset:35840\n"
"v_mfma_f32_16x16x32_bf16 v[212:215], a[164:167], v[176:179], v[212:215]\n"
"v_mfma_f32_16x16x32_bf16 v[212:215], a[172:175], v[180:183], v[212:215]\n"
"s_waitcnt lgkmcnt(2)\n"
"v_mfma_f32_16x16x32_bf16 v[212:215], a[180:183], v[184:187], v[212:215]\n"
"v_mfma_f32_16x16x32_bf16 v[212:215], a[188:191], v[188:191], v[212:215]\n"
"v_mfma_f32_16x16x32_bf16 v[208:211], a[184:187], v[188:191], v[208:211]\n"
"v_mfma_f32_16x16x32_bf16 v[208:211], a[176:179], v[184:187], v[208:211]\n"
"v_mfma_f32_16x16x32_bf16 v[208:211], a[168:171], v[180:183], v[208:211]\n"
"v_mfma_f32_16x16x32_bf16 v[208:211], a[160:163], v[176:179], v[208:211]\n"
"v_cvt_pk_bf16_f32 v14, v153, v157\n"
"v_cvt_pk_bf16_f32 v26, v154, v158\n"
"v_mfma_f32_16x16x32_bf16 v[216:219], a[160:163], v[192:195], v[216:219]\n"
"v_cvt_pk_bf16_f32 v230, v155, v159\n"
"v_mfma_f32_16x16x32_bf16 v[216:219], a[168:171], v[196:199], v[216:219]\n"
"s_waitcnt lgkmcnt(0)\n"
"v_mfma_f32_16x16x32_bf16 v[216:219], a[176:179], v[200:203], v[216:219]\n"
"v_mfma_f32_16x16x32_bf16 v[216:219], a[184:187], v[204:207], v[216:219]\n"
"v_mfma_f32_16x16x32_bf16 v[220:223], a[188:191], v[204:207], v[220:223]\n"
"v_mfma_f32_16x16x32_bf16 v[220:223], a[180:183], v[200:203], v[220:223]\n"
"v_mfma_f32_16x16x32_bf16 v[220:223], a[172:175], v[196:199], v[220:223]\n"
"v_mfma_f32_16x16x32_bf16 v[220:223], a[164:167], v[192:195], v[220:223]\n"
"v_cvt_pk_bf16_f32 v15, v209, v213\n"
"v_cvt_pk_bf16_f32 v27, v210, v214\n"
"v_cvt_pk_bf16_f32 v231, v211, v215\n"
"buffer_store_dwordx4 v[12:15], v1, s[20:23], s67 offen offset:1024 sc1\n"
"buffer_store_dwordx4 v[24:27], v1, s[20:23], s68 offen offset:1024 sc1\n"
"buffer_store_dwordx4 v[228:231], v1, s[20:23], s69 offen offset:1024 sc1\n"
"s_nop 3\n"
"v_cvt_pk_bf16_f32 v102, v218, v222\n"
"v_cvt_pk_bf16_f32 v101, v217, v221\n"
"v_cvt_pk_bf16_f32 v103, v219, v223\n"
"v_permlane32_swap_b32 v100, v102\n"
"s_nop 0\n"
"v_permlane32_swap_b32 v101, v103\n"
"s_nop 1\n"
"v_permlane16_swap_b32 v100, v101\n"
"v_permlane16_swap_b32 v102, v103\n"
"buffer_store_dwordx4 v[100:103], v254, s[20:23], s55 offen sc1\n"
"s_waitcnt vmcnt(7)\n"
"ds_read_u16_d16_hi v233, v18 offset:0\n"
"ds_read_u16_d16_hi v234, v18 offset:1024\n"
"ds_read_u16_d16_hi v235, v18 offset:32\n"
"ds_read_u16_d16_hi v236, v18 offset:1056\n"
"ds_read_u16_d16_hi v237, v18 offset:64\n"
"ds_read_u16_d16_hi v238, v18 offset:1088\n"
"ds_read_u16_d16_hi v239, v18 offset:96\n"
"ds_read_u16_d16_hi v240, v18 offset:1120\n"
"ds_read_u16_d16_hi v241, v18 offset:128\n"
"ds_read_u16_d16_hi v242, v18 offset:1152\n"
"ds_read_u16_d16_hi v243, v18 offset:160\n"
"ds_read_u16_d16_hi v244, v18 offset:1184\n"
"ds_read_u16_d16_hi v245, v18 offset:192\n"
"ds_read_u16_d16_hi v246, v18 offset:1216\n"
"ds_read_u16_d16_hi v247, v18 offset:224\n"
"ds_read_u16_d16_hi v248, v18 offset:1248\n"
"ds_read_u16_d16_hi v249, v19 offset:2048\n"
"ds_read_u16_d16_hi v250, v19 offset:2176\n"
"v_max3_u32 v6, v52, v53, v54\n"
"v_max3_u32 v7, v55, v56, v57\n"
"v_max3_u32 v7, v7, v58, v59\n"
"v_max3_u32 v6, v6, v60, v64\n"
"v_max3_u32 v7, v7, v65, v66\n"
"v_max3_u32 v6, v6, v67, v68\n"
"v_max3_u32 v7, v7, v69, v70\n"
"v_max3_u32 v6, v6, v71, v72\n"
"v_max3_u32 v7, v7, v76, v77\n"
"v_max3_u32 v6, v6, v78, v79\n"
"v_max3_u32 v7, v7, v80, v81\n"
"v_max3_u32 v6, v6, v82, v83\n"
"v_max3_u32 v6, v6, v7, v84\n"
"v_cmp_eq_u32 vcc, -1, v6\n"
"s_nop 0\n"
"s_cmp_eq_u64 vcc, 0\n"
"s_cbranch_scc0 .LGX_SLOW\n"
".LGX_BACK:\n"
"v_lshlrev_b32 v88, 16, v52\n"
"v_and_b32 v89, 0xffff0000, v52\n"
"v_lshlrev_b32 v90, 16, v64\n"
"v_and_b32 v91, 0xffff0000, v64\n"
"v_lshlrev_b32 v92, 16, v76\n"
"v_and_b32 v93, 0xffff0000, v76\n"
"v_pk_add_f32 v[32:33], v[32:33], v[88:89]\n"
"v_pk_add_f32 v[90:91], v[90:91], v[92:93]\n"
"v_pk_add_f32 v[32:33], v[32:33], v[90:91]\n"
"v_lshlrev_b32 v96, 16, v53\n"
"v_and_b32 v97, 0xffff0000, v53\n"
"v_lshlrev_b32 v98, 16, v65\n"
"v_and_b32 v99, 0xffff0000, v65\n"
"v_lshlrev_b32 v100, 16, v77\n"
"v_and_b32 v101, 0xffff0000, v77\n"
"v_pk_add_f32 v[34:35], v[34:35], v[96:97]\n"
"v_pk_add_f32 v[98:99], v[98:99], v[100:101]\n"
"v_pk_add_f32 v[34:35], v[34:35], v[98:99]\n"
"v_lshlrev_b32 v88, 16, v54\n"
"v_and_b32 v89, 0xffff0000, v54\n"
"v_lshlrev_b32 v90, 16, v66\n"
"v_and_b32 v91, 0xffff0000, v66\n"
"v_lshlrev_b32 v92, 16, v78\n"
"v_and_b32 v93, 0xffff0000, v78\n"
"v_pk_add_f32 v[36:37], v[36:37], v[88:89]\n"
"v_pk_add_f32 v[90:91], v[90:91], v[92:93]\n"
"v_pk_add_f32 v[36:37], v[36:37], v[90:91]\n"
"v_lshlrev_b32 v96, 16, v55\n"
"v_and_b32 v97, 0xffff0000, v55\n"
"v_lshlrev_b32 v98, 16, v67\n"
"v_and_b32 v99, 0xffff0000, v67\n"
"v_lshlrev_b32 v100, 16, v79\n"
"v_and_b32 v101, 0xffff0000, v79\n"
"v_pk_add_f32 v[38:39], v[38:39], v[96:97]\n"
"v_pk_add_f32 v[98:99], v[98:99], v[100:101]\n"
"v_pk_add_f32 v[38:39], v[38:39], v[98:99]\n"
"v_lshlrev_b32 v88, 16, v56\n"
"v_and_b32 v89, 0xffff0000, v56\n"
"v_lshlrev_b32 v90, 16, v68\n"
"v_and_b32 v91, 0xffff0000, v68\n"
"v_lshlrev_b32 v92, 16, v80\n"
"v_and_b32 v93, 0xffff0000, v80\n"
"v_pk_add_f32 v[40:41], v[40:41], v[88:89]\n"
"v_pk_add_f32 v[90:91], v[90:91], v[92:93]\n"
"v_pk_add_f32 v[40:41], v[40:41], v[90:91]\n"
"v_lshlrev_b32 v96, 16, v57\n"
"v_and_b32 v97, 0xffff0000, v57\n"
"v_lshlrev_b32 v98, 16, v69\n"
"v_and_b32 v99, 0xffff0000, v69\n"
"v_lshlrev_b32 v100, 16, v81\n"
"v_and_b32 v101, 0xffff0000, v81\n"
"v_pk_add_f32 v[42:43], v[42:43], v[96:97]\n"
"v_pk_add_f32 v[98:99], v[98:99], v[100:101]\n"
"v_pk_add_f32 v[42:43], v[42:43], v[98:99]\n"
"v_lshlrev_b32 v88, 16, v58\n"
"v_and_b32 v89, 0xffff0000, v58\n"
"v_lshlrev_b32 v90, 16, v70\n"
"v_and_b32 v91, 0xffff0000, v70\n"
"v_lshlrev_b32 v92, 16, v82\n"
"v_and_b32 v93, 0xffff0000, v82\n"
"v_pk_add_f32 v[44:45], v[44:45], v[88:89]\n"
"v_pk_add_f32 v[90:91], v[90:91], v[92:93]\n"
"v_pk_add_f32 v[44:45], v[44:45], v[90:91]\n"
"v_lshlrev_b32 v96, 16, v59\n"
"v_and_b32 v97, 0xffff0000, v59\n"
"v_lshlrev_b32 v98, 16, v71\n"
"v_and_b32 v99, 0xffff0000, v71\n"
"v_lshlrev_b32 v100, 16, v83\n"
"v_and_b32 v101, 0xffff0000, v83\n"
"v_pk_add_f32 v[46:47], v[46:47], v[96:97]\n"
"v_pk_add_f32 v[98:99], v[98:99], v[100:101]\n"
"v_pk_add_f32 v[46:47], v[46:47], v[98:99]\n"
"v_lshlrev_b32 v88, 16, v60\n"
"v_and_b32 v89, 0xffff0000, v60\n"
"v_lshlrev_b32 v90, 16, v72\n"
"v_and_b32 v91, 0xffff0000, v72\n"
"v_lshlrev_b32 v92, 16, v84\n"
"v_and_b32 v93, 0xffff0000, v84\n"
"v_pk_add_f32 v[48:49], v[48:49], v[88:89]\n"
"v_pk_add_f32 v[90:91], v[90:91], v[92:93]\n"
"v_pk_add_f32 v[48:49], v[48:49], v[90:91]\n"
"v_mov_b32 v6, 0xc13504f3\n"
"v_mov_b32 v7, 0x40faf233\n"
"v_pk_mul_f32 v[52:53], v[32:33], v[6:7] op_sel_hi:[1,0]\n"
"v_pk_mul_f32 v[54:55], v[34:35], v[6:7] op_sel_hi:[1,0]\n"
"v_pk_mul_f32 v[56:57], v[36:37], v[6:7] op_sel_hi:[1,0]\n"
"v_pk_mul_f32 v[58:59], v[38:39], v[6:7] op_sel_hi:[1,0]\n"
"v_pk_mul_f32 v[60:61], v[40:41], v[6:7] op_sel_hi:[1,0]\n"
"v_pk_mul_f32 v[62:63], v[42:43], v[6:7] op_sel_hi:[1,0]\n"
"v_pk_mul_f32 v[64:65], v[44:45], v[6:7] op_sel_hi:[1,0]\n"
"v_pk_mul_f32 v[66:67], v[46:47], v[6:7] op_sel_hi:[1,0]\n"
"v_pk_mul_f32 v[68:69], v[48:49], v[6:7] op_sel_hi:[1,0]\n"
"v_exp_f32 v52, v52\n"
"v_exp_f32 v53, v53\n"
"v_exp_f32 v54, v54\n"
"v_exp_f32 v55, v55\n"
"v_exp_f32 v56, v56\n"
"v_exp_f32 v57, v57\n"
"v_exp_f32 v58, v58\n"
"v_exp_f32 v59, v59\n"
"v_exp_f32 v60, v60\n"
"v_exp_f32 v61, v61\n"
"v_exp_f32 v62, v62\n"
"v_exp_f32 v63, v63\n"
"s_mul_i32 s54, s58, 55296\n"
"s_add_u32 s54, s54, 6912\n"
"buffer_load_dwordx4 v[160:163], v1, s[20:23], s54 offen sc1\n"
"buffer_load_dwordx4 v[164:167], v1, s[20:23], s54 offen offset:1024 sc1\n"
"buffer_load_dword v168, v5, s[20:23], s54 offen offset:2048 sc1\n"
"s_mul_i32 s54, s58, 55296\n"
"s_add_u32 s54, s54, 9216\n"
"buffer_load_dwordx4 v[172:175], v1, s[20:23], s54 offen sc1\n"
"buffer_load_dwordx4 v[176:179], v1, s[20:23], s54 offen offset:1024 sc1\n"
"buffer_load_dword v180, v5, s[20:23], s54 offen offset:2048 sc1\n"
"s_mul_i32 s54, s58, 55296\n"
"s_add_u32 s54, s54, 11520\n"
"buffer_load_dwordx4 v[184:187], v1, s[20:23], s54 offen sc1\n"
"buffer_load_dwordx4 v[188:191], v1, s[20:23], s54 offen offset:1024 sc1\n"
"buffer_load_dword v192, v5, s[20:23], s54 offen offset:2048 sc1\n"
"v_exp_f32 v64, v64\n"
"v_exp_f32 v65, v65\n"
"v_exp_f32 v66, v66\n"
"v_exp_f32 v67, v67\n"
"v_exp_f32 v68, v68\n"
"v_exp_f32 v69, v69\n"
"v_pk_mul_f32 v[32:33], v[32:33], v[6:7] op_sel:[0,1]\n"
"v_pk_mul_f32 v[34:35], v[34:35], v[6:7] op_sel:[0,1]\n"
"v_pk_mul_f32 v[36:37], v[36:37], v[6:7] op_sel:[0,1]\n"
"v_pk_mul_f32 v[38:39], v[38:39], v[6:7] op_sel:[0,1]\n"
"v_pk_mul_f32 v[40:41], v[40:41], v[6:7] op_sel:[0,1]\n"
"v_pk_mul_f32 v[42:43], v[42:43], v[6:7] op_sel:[0,1]\n"
"v_pk_mul_f32 v[44:45], v[44:45], v[6:7] op_sel:[0,1]\n"
"v_pk_mul_f32 v[46:47], v[46:47], v[6:7] op_sel:[0,1]\n"
"v_pk_mul_f32 v[48:49], v[48:49], v[6:7] op_sel:[0,1]\n"
"v_pk_add_f32 v[52:53], v[52:53], 1.0 op_sel_hi:[1,0]\n"
"v_pk_add_f32 v[54:55], v[54:55], 1.0 op_sel_hi:[1,0]\n"
"v_pk_add_f32 v[56:57], v[56:57], 1.0 op_sel_hi:[1,0]\n"
"v_pk_add_f32 v[58:59], v[58:59], 1.0 op_sel_hi:[1,0]\n"
"v_pk_add_f32 v[60:61], v[60:61], 1.0 op_sel_hi:[1,0]\n"
"v_pk_add_f32 v[62:63], v[62:63], 1.0 op_sel_hi:[1,0]\n"
"v_pk_add_f32 v[64:65], v[64:65], 1.0 op_sel_hi:[1,0]\n"
"v_pk_add_f32 v[66:67], v[66:67], 1.0 op_sel_hi:[1,0]\n"
"v_pk_add_f32 v[68:69], v[68:69], 1.0 op_sel_hi:[1,0]\n"
"v_rcp_f32 v52, v52\n"
"v_rcp_f32 v53, v53\n"
"v_rcp_f32 v54, v54\n"
"v_rcp_f32 v55, v55\n"
"v_rcp_f32 v56, v56\n"
"v_rcp_f32 v57, v57\n"
"v_rcp_f32 v58, v58\n"
"v_rcp_f32 v59, v59\n"
"v_rcp_f32 v60, v60\n"
"v_rcp_f32 v61, v61\n"
"v_rcp_f32 v62, v62\n"
"v_rcp_f32 v63, v63\n"
"v_rcp_f32 v64, v64\n"
"v_rcp_f32 v65, v65\n"
"v_rcp_f32 v66, v66\n"
"v_rcp_f32 v67, v67\n"
"v_rcp_f32 v68, v68\n"
"v_rcp_f32 v69, v69\n"
"v_pk_mul_f32 v[32:33], v[32:33], v[52:53]\n"
"v_pk_mul_f32 v[34:35], v[34:35], v[54:55]\n"
"v_pk_mul_f32 v[36:37], v[36:37], v[56:57]\n"
"v_pk_mul_f32 v[38:39], v[38:39], v[58:59]\n"
"v_pk_mul_f32 v[40:41], v[40:41], v[60:61]\n"
"v_pk_mul_f32 v[42:43], v[42:43], v[62:63]\n"
"v_pk_mul_f32 v[44:45], v[44:45], v[64:65]\n"
"v_pk_mul_f32 v[46:47], v[46:47], v[66:67]\n"
"v_pk_mul_f32 v[48:49], v[48:49], v[68:69]\n"
"s_waitcnt lgkmcnt(0)\n"
"v_fmac_f32 v233, v32, v104\n"
"v_fmac_f32 v234, v33, v108\n"
"v_fmac_f32 v235, v34, v112\n"
"v_fmac_f32 v236, v35, v116\n"
"v_fmac_f32 v237, v36, v120\n"
"v_fmac_f32 v238, v37, v124\n"
"v_fmac_f32 v239, v38, v128\n"
"v_fmac_f32 v240, v39, v132\n"
"v_fmac_f32 v241, v40, v136\n"
"v_fmac_f32 v242, v41, v140\n"
"v_fmac_f32 v243, v42, v144\n"
"v_fmac_f32 v244, v43, v148\n"
"v_fmac_f32 v245, v44, v152\n"
"v_fmac_f32 v246, v45, v156\n"
"v_fmac_f32 v247, v46, v208\n"
"v_fmac_f32 v248, v47, v212\n"
"v_fmac_f32 v249, v48, v216\n"
"v_fmac_f32 v250, v49, v220\n"
"s_waitcnt vmcnt(0)\n"
"v_max3_u32 v6, v160, v161, v162\n"
"v_max3_u32 v7, v163, v164, v165\n"
"v_max3_u32 v7, v7, v166, v167\n"
"v_max3_u32 v6, v6, v168, v172\n"
"v_max3_u32 v7, v7, v173, v174\n"
"v_max3_u32 v6, v6, v175, v176\n"
"v_max3_u32 v7, v7, v177, v178\n"
"v_max3_u32 v6, v6, v179, v180\n"
"v_max3_u32 v7, v7, v184, v185\n"
"v_max3_u32 v6, v6, v186, v187\n"
"v_max3_u32 v7, v7, v188, v189\n"
"v_max3_u32 v6, v6, v190, v191\n"
"v_max3_u32 v6, v6, v7, v192\n"
"v_cmp_eq_u32 vcc, -1, v6\n"
"s_nop 0\n"
"s_cmp_eq_u64 vcc, 0\n"
"s_cbranch_scc0 .LSX_SLOW\n"
".LSX_BACK:\n"
"v_lshlrev_b32 v88, 16, v160\n"
"v_and_b32 v89, 0xffff0000, v160\n"
"v_lshlrev_b32 v90, 16, v172\n"
"v_and_b32 v91, 0xffff0000, v172\n"
"v_lshlrev_b32 v92, 16, v184\n"
"v_and_b32 v93, 0xffff0000, v184\n"
"v_pk_add_f32 v[88:89], v[88:89], v[90:91]\n"
"v_pk_add_f32 v[88:89], v[88:89], v[92:93]\n"
"v_fmac_f32 v233, v32, v88\n"
"v_fmac_f32 v234, v33, v89\n"
"v_cvt_pk_bf16_f32 v233, v233, v234\n"
"v_lshlrev_b32 v96, 16, v161\n"
"v_and_b32 v97, 0xffff0000, v161\n"
"v_lshlrev_b32 v98, 16, v173\n"
"v_and_b32 v99, 0xffff0000, v173\n"
"v_lshlrev_b32 v100, 16, v185\n"
"v_and_b32 v101, 0xffff0000, v185\n"
"v_pk_add_f32 v[96:97], v[96:97], v[98:99]\n"
"v_pk_add_f32 v[96:97], v[96:97], v[100:101]\n"
"v_fmac_f32 v235, v34, v96\n"
"v_fmac_f32 v236, v35, v97\n"
"v_cvt_pk_bf16_f32 v235, v235, v236\n"
"v_lshlrev_b32 v88, 16, v162\n"
"v_and_b32 v89, 0xffff0000, v162\n"
"v_lshlrev_b32 v90, 16, v174\n"
"v_and_b32 v91, 0xffff0000, v174\n"
"v_lshlrev_b32 v92, 16, v186\n"
"v_and_b32 v93, 0xffff0000, v186\n"
"v_pk_add_f32 v[88:89], v[88:89], v[90:91]\n"
"v_pk_add_f32 v[88:89], v[88:89], v[92:93]\n"
"v_fmac_f32 v237, v36, v88\n"
"v_fmac_f32 v238, v37, v89\n"
"v_cvt_pk_bf16_f32 v237, v237, v238\n"
"v_lshlrev_b32 v96, 16, v163\n"
"v_and_b32 v97, 0xffff0000, v163\n"
"v_lshlrev_b32 v98, 16, v175\n"
"v_and_b32 v99, 0xffff0000, v175\n"
"v_lshlrev_b32 v100, 16, v187\n"
"v_and_b32 v101, 0xffff0000, v187\n"
"v_pk_add_f32 v[96:97], v[96:97], v[98:99]\n"
"v_pk_add_f32 v[96:97], v[96:97], v[100:101]\n"
"v_fmac_f32 v239, v38, v96\n"
"v_fmac_f32 v240, v39, v97\n"
"v_cvt_pk_bf16_f32 v239, v239, v240\n"
"v_lshlrev_b32 v88, 16, v164\n"
"v_and_b32 v89, 0xffff0000, v164\n"
"v_lshlrev_b32 v90, 16, v176\n"
"v_and_b32 v91, 0xffff0000, v176\n"
"v_lshlrev_b32 v92, 16, v188\n"
"v_and_b32 v93, 0xffff0000, v188\n"
"v_pk_add_f32 v[88:89], v[88:89], v[90:91]\n"
"v_pk_add_f32 v[88:89], v[88:89], v[92:93]\n"
"v_fmac_f32 v241, v40, v88\n"
"v_fmac_f32 v242, v41, v89\n"
"v_cvt_pk_bf16_f32 v241, v241, v242\n"
"v_lshlrev_b32 v96, 16, v165\n"
"v_and_b32 v97, 0xffff0000, v165\n"
"v_lshlrev_b32 v98, 16, v177\n"
"v_and_b32 v99, 0xffff0000, v177\n"
"v_lshlrev_b32 v100, 16, v189\n"
"v_and_b32 v101, 0xffff0000, v189\n"
"v_pk_add_f32 v[96:97], v[96:97], v[98:99]\n"
"v_pk_add_f32 v[96:97], v[96:97], v[100:101]\n"
"v_fmac_f32 v243, v42, v96\n"
"v_fmac_f32 v244, v43, v97\n"
"v_cvt_pk_bf16_f32 v243, v243, v244\n"
"v_lshlrev_b32 v88, 16, v166\n"
"v_and_b32 v89, 0xffff0000, v166\n"
"v_lshlrev_b32 v90, 16, v178\n"
"v_and_b32 v91, 0xffff0000, v178\n"
"v_lshlrev_b32 v92, 16, v190\n"
"v_and_b32 v93, 0xffff0000, v190\n"
"v_pk_add_f32 v[88:89], v[88:89], v[90:91]\n"
"v_pk_add_f32 v[88:89], v[88:89], v[92:93]\n"
"v_fmac_f32 v245, v44, v88\n"
"v_fmac_f32 v246, v45, v89\n"
"v_cvt_pk_bf16_f32 v245, v245, v246\n"
"v_lshlrev_b32 v96, 16, v167\n"
"v_and_b32 v97, 0xffff0000, v167\n"
"v_lshlrev_b32 v98, 16, v179\n"
"v_and_b32 v99, 0xffff0000, v179\n"
"v_lshlrev_b32 v100, 16, v191\n"
"v_and_b32 v101, 0xffff0000, v191\n"
"v_pk_add_f32 v[96:97], v[96:97], v[98:99]\n"
"v_pk_add_f32 v[96:97], v[96:97], v[100:101]\n"
"v_fmac_f32 v247, v46, v96\n"
"v_fmac_f32 v248, v47, v97\n"
"v_cvt_pk_bf16_f32 v247, v247, v248\n"
"v_lshlrev_b32 v88, 16, v168\n"
"v_and_b32 v89, 0xffff0000, v168\n"
"v_lshlrev_b32 v90, 16, v180\n"
"v_and_b32 v91, 0xffff0000, v180\n"
"v_lshlrev_b32 v92, 16, v192\n"
"v_and_b32 v93, 0xffff0000, v192\n"
"v_pk_add_f32 v[88:89], v[88:89], v[90:91]\n"
"v_pk_add_f32 v[88:89], v[88:89], v[92:93]\n"
"v_fmac_f32 v249, v48, v88\n"
"v_fmac_f32 v250, v49, v89\n"
"v_cvt_pk_bf16_f32 v249, v249, v250\n"
"ds_write_b16 v18, v233 offset:0\n"
"ds_write_b16_d16_hi v18, v233 offset:1024\n"
"ds_write_b16 v18, v235 offset:32\n"
"ds_write_b16_d16_hi v18, v235 offset:1056\n"
"ds_write_b16 v18, v237 offset:64\n"
"ds_write_b16_d16_hi v18, v237 offset:1088\n"
"ds_write_b16 v18, v239 offset:96\n"
"ds_write_b16_d16_hi v18, v239 offset:1120\n"
"ds_write_b16 v18, v241 offset:128\n"
"ds_write_b16_d16_hi v18, v241 offset:1152\n"
"ds_write_b16 v18, v243 offset:160\n"
"ds_write_b16_d16_hi v18, v243 offset:1184\n"
"ds_write_b16 v18, v245 offset:192\n"
"ds_write_b16_d16_hi v18, v245 offset:1216\n"
"ds_write_b16 v18, v247 offset:224\n"
"ds_write_b16_d16_hi v18, v247 offset:1248\n"
"ds_write_b16 v19, v249 offset:2048\n"
"ds_write_b16_d16_hi v19, v249 offset:2176\n"
"s_waitcnt lgkmcnt(0)\n"
"ds_read_b128 v[52:55], v251 offset:0\n"
"ds_read_b128 v[56:59], v251 offset:1024\n"
"ds_read_b128 v[60:63], v251 offset:2048\n"
"s_waitcnt lgkmcnt(2)\n"
"buffer_store_dwordx4 v[52:55], v4, s[36:39], s50 offen\n"
"s_waitcnt lgkmcnt(1)\n"
"buffer_store_dwordx4 v[56:59], v4, s[36:39], s49 offen\n"
"s_waitcnt lgkmcnt(0)\n"
"buffer_store_dwordx4 v[60:63], v17, s[36:39], s82 offen\n"
"s_endpgm\n"
".LPO_SLOW:\n"
"s_mov_b32 s11, 262144\n"
".LPO_L:\n"
"v_readfirstlane_b32 s54, v253\n"
"s_nop 0\n"
"s_cmp_ge_u32 s54, 8\n"
"s_cbranch_scc1 .LPO_X\n"
"s_sub_u32 s11, s11, 1\n"
"s_cbranch_scc1 .LPO_X\n"
"s_sleep 2\n"
"buffer_load_dword v253, off, s[24:27], s51 sc1\n"
"s_waitcnt vmcnt(0)\n"
"s_branch .LPO_L\n"
".LPO_X:\n"
"s_branch .LPO_BACK\n"
".LPG_SLOW:\n"
"s_mov_b32 s11, 262144\n"
".LPG_L:\n"
"v_readfirstlane_b32 s54, v253\n"
"s_nop 0\n"
"s_cmp_ge_u32 s54, 4\n"
"s_cbranch_scc1 .LPG_X\n"
"s_sub_u32 s11, s11, 1\n"
"s_cbranch_scc1 .LPG_X\n"
"s_sleep 2\n"
"buffer_load_dword v253, off, s[24:27], s8 sc1\n"
"s_waitcnt vmcnt(0)\n"
"s_branch .LPG_L\n"
".LPG_X:\n"
"s_branch .LPG_BACK\n"
".LGX_SLOW:\n"
"s_mov_b32 s11, 262144\n"
".LGXB_L:\n"
"v_max3_u32 v6, v52, v53, v54\n"
"v_max3_u32 v7, v55, v56, v57\n"
"v_max3_u32 v7, v7, v58, v59\n"
"v_max3_u32 v6, v6, v60, v64\n"
"v_max3_u32 v7, v7, v65, v66\n"
"v_max3_u32 v6, v6, v67, v68\n"
"v_max3_u32 v7, v7, v69, v70\n"
"v_max3_u32 v6, v6, v71, v72\n"
"v_max3_u32 v7, v7, v76, v77\n"
"v_max3_u32 v6, v6, v78, v79\n"
"v_max3_u32 v7, v7, v80, v81\n"
"v_max3_u32 v6, v6, v82, v83\n"
"v_max3_u32 v6, v6, v7, v84\n"
"v_cmp_eq_u32 vcc, -1, v6\n"
"s_nop 0\n"
"s_cmp_eq_u64 vcc, 0\n"
"s_cbranch_scc1 .LGXB_X\n"
"s_sub_u32 s11, s11, 1\n"
"s_cbranch_scc1 .LGXB_X\n"
"s_sleep 2\n"
"s_mul_i32 s54, s58, 55296\n"
"s_add_u32 s54, s54, 0\n"
"buffer_load_dwordx4 v[52:55], v1, s[20:23], s54 offen sc1\n"
"buffer_load_dwordx4 v[56:59], v1, s[20:23], s54 offen offset:1024 sc1\n"
"buffer_load_dword v60, v5, s[20:23], s54 offen offset:2048 sc1\n"
"s_mul_i32 s54, s58, 55296\n"
"s_add_u32 s54, s54, 2304\n"
"buffer_load_dwordx4 v[64:67], v1, s[20:23], s54 offen sc1\n"
"buffer_load_dwordx4 v[68:71], v1, s[20:23], s54 offen offset:1024 sc1\n"
"buffer_load_dword v72, v5, s[20:23], s54 offen offset:2048 sc1\n"
"s_mul_i32 s54, s58, 55296\n"
"s_add_u32 s54, s54, 4608\n"
"buffer_load_dwordx4 v[76:79], v1, s[20:23], s54 offen sc1\n"
"buffer_load_dwordx4 v[80:83], v1, s[20:23], s54 offen offset:1024 sc1\n"
"buffer_load_dword v84, v5, s[20:23], s54 offen offset:2048 sc1\n"
"s_waitcnt vmcnt(0)\n"
"s_branch .LGXB_L\n"
".LGXB_X:\n"
"s_branch .LGX_BACK\n"
".LSX_SLOW:\n"
"s_mov_b32 s11, 262144\n"
".LSXB_L:\n"
"v_max3_u32 v6, v160, v161, v162\n"
"v_max3_u32 v7, v163, v164, v165\n"
"v_max3_u32 v7, v7, v166, v167\n"
"v_max3_u32 v6, v6, v168, v172\n"
"v_max3_u32 v7, v7, v173, v174\n"
"v_max3_u32 v6, v6, v175, v176\n"
"v_max3_u32 v7, v7, v177, v178\n"
"v_max3_u32 v6, v6, v179, v180\n"
"v_max3_u32 v7, v7, v184, v185\n"
"v_max3_u32 v6, v6, v186, v187\n"
"v_max3_u32 v7, v7, v188, v189\n"
"v_max3_u32 v6, v6, v190, v191\n"
"v_max3_u32 v6, v6, v7, v192\n"
"v_cmp_eq_u32 vcc, -1, v6\n"
"s_nop 0\n"
"s_cmp_eq_u64 vcc, 0\n"
"s_cbranch_scc1 .LSXB_X\n"
"s_sub_u32 s11, s11, 1\n"
"s_cbranch_scc1 .LSXB_X\n"
"s_sleep 2\n"
"s_mul_i32 s54, s58, 55296\n"
"s_add_u32 s54, s54, 6912\n"
"buffer_load_dwordx4 v[160:163], v1, s[20:23], s54 offen sc1\n"
"buffer_load_dwordx4 v[164:167], v1, s[20:23], s54 offen offset:1024 sc1\n"
"buffer_load_dword v168, v5, s[20:23], s54 offen offset:2048 sc1\n"
"s_mul_i32 s54, s58, 55296\n"
"s_add_u32 s54, s54, 9216\n"
"buffer_load_dwordx4 v[172:175], v1, s[20:23], s54 offen sc1\n"
"buffer_load_dwordx4 v[176:179], v1, s[20:23], s54 offen offset:1024 sc1\n"
"buffer_load_dword v180, v5, s[20:23], s54 offen offset:2048 sc1\n"
"s_mul_i32 s54, s58, 55296\n"
"s_add_u32 s54, s54, 11520\n"
"buffer_load_dwordx4 v[184:187], v1, s[20:23], s54 offen sc1\n"
"buffer_load_dwordx4 v[188:191], v1, s[20:23], s54 offen offset:1024 sc1\n"
"buffer_load_dword v192, v5, s[20:23], s54 offen offset:2048 sc1\n"
"s_waitcnt vmcnt(0)\n"
"s_branch .LSXB_L\n"
".LSXB_X:\n"
"s_branch .LSX_BACK\n"
".LATT_SLOW:\n"
"s_mov_b32 s60, -1\n"
"s_waitcnt vmcnt(0) lgkmcnt(0)\n"
"s_barrier\n"
"s_mov_b32 s56, 2146435072\n"
"s_mov_b32 s57, 2146435072\n"
"s_mov_b32 s49, 2146435072\n"
"s_mov_b32 s75, 2146435072\n"
"s_mov_b32 s46, 0\n"
"s_mov_b32 s14, 0\n"
"s_mov_b32 s74, 98304\n"
"v_and_b32 v254, 7, v0\n"
"v_lshl_add_u32 v254, v254, 5, s63\n"
"v_lshlrev_b32 v254, 8, v254\n"
"v_lshrrev_b32 v255, 3, v0\n"
"v_lshl_add_u32 v255, v255, 12, v0\n"
"v_lshlrev_b32 v255, 8, v255\n"
"v_lshrrev_b32 v18, 4, v0\n"
"v_and_b32 v19, 15, v0\n"
"v_lshlrev_b32 v20, 8, v18\n"
"v_add_u32 v20, s48, v20\n"
"v_or_b32 v31, 0, v18\n"
"v_xor_b32 v31, v31, v19\n"
"v_lshl_add_u32 v12, v31, 4, v20\n"
"v_or_b32 v31, 4, v18\n"
"v_xor_b32 v31, v31, v19\n"
"v_lshl_add_u32 v13, v31, 4, v20\n"
"v_or_b32 v31, 8, v18\n"
"v_xor_b32 v31, v31, v19\n"
"v_lshl_add_u32 v14, v31, 4, v20\n"
"v_or_b32 v31, 12, v18\n"
"v_xor_b32 v31, v31, v19\n"
"v_lshl_add_u32 v15, v31, 4, v20\n"
"v_or_b32 v31, 0, v18\n"
"v_lshlrev_b32 v31, 1, v31\n"
"v_xor_b32 v31, v31, v19\n"
"v_lshl_add_u32 v16, v31, 4, v20\n"
"v_or_b32 v31, 4, v18\n"
"v_lshlrev_b32 v31, 1, v31\n"
"v_xor_b32 v31, v31, v19\n"
"v_lshl_add_u32 v17, v31, 4, v20\n"
"v_xor_b32 v31, v18, v19\n"
"v_lshlrev_b32 v19, 8, v19\n"
"v_lshl_add_u32 v18, v31, 4, v19\n"
"v_xor_b32 v19, 64, v18\n"
"v_xor_b32 v20, 128, v18\n"
"v_xor_b32 v21, 192, v18\n"
"v_lshrrev_b32 v23, 2, v0\n"
"v_and_b32 v24, 7, v23\n"
"v_and_b32 v25, 3, v0\n"
"v_lshlrev_b32 v23, 8, v23\n"
"v_lshl_add_u32 v23, v25, 3, v23\n"
"v_add_u32 v23, 49152, v23\n"
"v_lshl_add_u32 v22, v24, 5, v23\n"
"v_xor_b32 v23, 32, v22\n"
"v_xor_b32 v24, 64, v22\n"
"v_xor_b32 v25, 96, v22\n"
"v_xor_b32 v26, 128, v22\n"
"v_xor_b32 v27, 160, v22\n"
"v_xor_b32 v28, 192, v22\n"
"v_xor_b32 v29, 224, v22\n"
"s_add_u32 m0, s48, 0\n"
"s_mov_b32 s52, 0\n"
"buffer_load_dwordx4 v12, s[32:35], s52 offen offset:0 lds\n"
"buffer_load_dwordx4 v13, s[32:35], s52 offen offset:1024 lds\n"
"buffer_load_dwordx4 v14, s[32:35], s52 offen offset:2048 lds\n"
"buffer_load_dwordx4 v15, s[32:35], s52 offen offset:3072 lds\n"
"s_add_u32 m0, s48, 16384\n"
"s_mov_b32 s52, 16384\n"
"buffer_load_dwordx4 v12, s[32:35], s52 offen offset:0 lds\n"
"buffer_load_dwordx4 v13, s[32:35], s52 offen offset:1024 lds\n"
"buffer_load_dwordx4 v14, s[32:35], s52 offen offset:2048 lds\n"
"buffer_load_dwordx4 v15, s[32:35], s52 offen offset:3072 lds\n"
"s_add_u32 m0, s48, 49152\n"
"s_mov_b32 s52, 0\n"
"buffer_load_dwordx4 v16, s[36:39], s52 offen offset:0 lds\n"
"buffer_load_dwordx4 v17, s[36:39], s52 offen offset:1024 lds\n"
"buffer_load_dwordx4 v16, s[36:39], s52 offen offset:2048 lds\n"
"buffer_load_dwordx4 v17, s[36:39], s52 offen offset:3072 lds\n"
"v_and_b32 v31, 15, v0\n"
"v_lshlrev_b32 v31, 8, v31\n"
"v_lshrrev_b32 v249, 4, v0\n"
"v_lshl_add_u32 v31, v249, 4, v31\n"
"v_mov_b32 v245, 0xff800000\n"
"v_mov_b32 v246, 0xff800000\n"
"v_mov_b32 v247, 0xff800000\n"
"v_mov_b32 v248, 0xff800000\n"
"s_mov_b32 s52, 0\n"
".LATT_MAXL:\n"
"buffer_load_dwordx4 a[128:131], v31, s[32:35], s52 offen offset:0\n"
"buffer_load_dwordx4 a[132:135], v31, s[32:35], s52 offen offset:64\n"
"buffer_load_dwordx4 a[136:139], v31, s[32:35], s52 offen offset:128\n"
"buffer_load_dwordx4 a[140:143], v31, s[32:35], s52 offen offset:192\n"
"s_waitcnt vmcnt(0)\n"
"v_mfma_f32_16x16x32_bf16 v[48:51], a[128:131], v[32:35], 0\n"
"v_mfma_f32_16x16x32_bf16 v[48:51], a[132:135], v[64:67], v[48:51]\n"
"v_mfma_f32_16x16x32_bf16 v[48:51], a[136:139], v[96:99], v[48:51]\n"
"v_mfma_f32_16x16x32_bf16 v[48:51], a[140:143], v[128:131], v[48:51]\n"
"v_mfma_f32_16x16x32_bf16 v[52:55], a[128:131], v[36:39], 0\n"
"v_mfma_f32_16x16x32_bf16 v[52:55], a[132:135], v[68:71], v[52:55]\n"
"v_mfma_f32_16x16x32_bf16 v[52:55], a[136:139], v[100:103], v[52:55]\n"
"v_mfma_f32_16x16x32_bf16 v[52:55], a[140:143], v[132:135], v[52:55]\n"
"v_mfma_f32_16x16x32_bf16 v[56:59], a[128:131], v[40:43], 0\n"
"v_mfma_f32_16x16x32_bf16 v[56:59], a[132:135], v[72:75], v[56:59]\n"
"v_mfma_f32_16x16x32_bf16 v[56:59], a[136:139], v[104:107], v[56:59]\n"
"v_mfma_f32_16x16x32_bf16 v[56:59], a[140:143], v[136:139], v[56:59]\n"
"v_mfma_f32_16x16x32_bf16 v[60:63], a[128:131], v[44:47], 0\n"
"v_mfma_f32_16x16x32_bf16 v[60:63], a[132:135], v[76:79], v[60:63]\n"
"v_mfma_f32_16x16x32_bf16 v[60:63], a[136:139], v[108:111], v[60:63]\n"
"v_mfma_f32_16x16x32_bf16 v[60:63], a[140:143], v[140:143], v[60:63]\n"
"s_nop 7\n"
"v_max3_f32 v245, v245, v48, v49\n"
"v_max3_f32 v245, v245, v50, v51\n"
"v_max3_f32 v246, v246, v52, v53\n"
"v_max3_f32 v246, v246, v54, v55\n"
"v_max3_f32 v247, v247, v56, v57\n"
"v_max3_f32 v247, v247, v58, v59\n"
"v_max3_f32 v248, v248, v60, v61\n"
"v_max3_f32 v248, v248, v62, v63\n"
"s_add_u32 s52, s52, 4096\n"
"s_cmp_lt_u32 s52, 524288\n"
"s_cbranch_scc1 .LATT_MAXL\n"
"s_barrier\n"
"ds_read_b128 a[128:131], v18 offset:0\n"
"ds_read_b128 a[132:135], v19 offset:0\n"
"ds_read_b128 a[136:139], v20 offset:0\n"
"ds_read_b128 a[140:143], v21 offset:0\n"
"ds_read_b128 a[144:147], v18 offset:4096\n"
"ds_read_b128 a[148:151], v19 offset:4096\n"
"ds_read_b128 a[152:155], v20 offset:4096\n"
"ds_read_b128 a[156:159], v21 offset:4096\n"
"s_waitcnt lgkmcnt(0)\n"
"v_mfma_f32_16x16x32_bf16 v[48:51], a[128:131], v[32:35], 0\n"
"v_mfma_f32_16x16x32_bf16 v[52:55], a[128:131], v[36:39], 0\n"
"v_mfma_f32_16x16x32_bf16 v[56:59], a[128:131], v[40:43], 0\n"
"v_mfma_f32_16x16x32_bf16 v[60:63], a[128:131], v[44:47], 0\n"
"v_mfma_f32_16x16x32_bf16 v[48:51], a[132:135], v[64:67], v[48:51]\n"
"v_mfma_f32_16x16x32_bf16 v[52:55], a[132:135], v[68:71], v[52:55]\n"
"v_mfma_f32_16x16x32_bf16 v[56:59], a[132:135], v[72:75], v[56:59]\n"
"v_mfma_f32_16x16x32_bf16 v[60:63], a[132:135], v[76:79], v[60:63]\n"
"v_mfma_f32_16x16x32_bf16 v[48:51], a[136:139], v[96:99], v[48:51]\n"
"v_mfma_f32_16x16x32_bf16 v[52:55], a[136:139], v[100:103], v[52:55]\n"
"v_mfma_f32_16x16x32_bf16 v[56:59], a[136:139], v[104:107], v[56:59]\n"
"v_mfma_f32_16x16x32_bf16 v[60:63], a[136:139], v[108:111], v[60:63]\n"
"s_branch .LATT_ENTRY\n"
".LK_END:\n"
  :: "s"(__builtin_amdgcn_kernarg_segment_ptr()), "s"(blockIdx.x), "s"(__builtin_amdgcn_readfirstlane(threadIdx.x >> 6)), "s"((unsigned)(size_t)lds)
  : "v0","v1","v2","v3","v4","v5","v6","v7","v8","v9","v10","v11","v12","v13","v14","v15","v16","v17","v18","v19","v20","v21","v22","v23","v24","v25","v26","v27","v28","v29","v30","v31","v32","v33","v34","v35","v36","v37","v38","v39","v40","v41","v42","v43","v44","v45","v46","v47","v48","v49","v50","v51","v52","v53","v54","v55","v56","v57","v58","v59","v60","v61","v62","v63","v64","v65","v66","v67","v68","v69","v70","v71","v72","v73","v74","v75","v76","v77","v78","v79","v80","v81","v82","v83","v84","v85","v86","v87","v88","v89","v90","v91","v92","v93","v94","v95","v96","v97","v98","v99","v100","v101","v102","v103","v104","v105","v106","v107","v108","v109","v110","v111","v112","v113","v114","v115","v116","v117","v118","v119","v120","v121","v122","v123","v124","v125","v126","v127","v128","v129","v130","v131","v132","v133","v134","v135","v136","v137","v138","v139","v140","v141","v142","v143","v144","v145","v146","v147","v148","v149","v150","v151","v152","v153","v154","v155","v156","v157","v158","v159","v160","v161","v162","v163","v164","v165","v166","v167","v168","v169","v170","v171","v172","v173","v174","v175","v176","v177","v178","v179","v180","v181","v182","v183","v184","v185","v186","v187","v188","v189","v190","v191","v192","v193","v194","v195","v196","v197","v198","v199","v200","v201","v202","v203","v204","v205","v206","v207","v208","v209","v210","v211","v212","v213","v214","v215","v216","v217","v218","v219","v220","v221","v222","v223","v224","v225","v226","v227","v228","v229","v230","v231","v232","v233","v234","v235","v236","v237","v238","v239","v240","v241","v242","v243","v244","v245","v246","v247","v248","v249","v250","v251","v252","v253","v254","v255","a0","a1","a2","a3","a4","a5","a6","a7","a8","a9","a10","a11","a12","a13","a14","a15","a16","a17","a18","a19","a20","a21","a22","a23","a24","a25","a26","a27","a28","a29","a30","a31","a32","a33","a34","a35","a36","a37","a38","a39","a40","a41","a42","a43","a44","a45","a46","a47","a48","a49","a50","a51","a52","a53","a54","a55","a56","a57","a58","a59","a60","a61","a62","a63","a64","a65","a66","a67","a68","a69","a70","a71","a72","a73","a74","a75","a76","a77","a78","a79","a80","a81","a82","a83","a84","a85","a86","a87","a88","a89","a90","a91","a92","a93","a94","a95","a96","a97","a98","a99","a100","a101","a102","a103","a104","a105","a106","a107","a108","a109","a110","a111","a112","a113","a114","a115","a116","a117","a118","a119","a120","a121","a122","a123","a124","a125","a126","a127","a128","a129","a130","a131","a132","a133","a134","a135","a136","a137","a138","a139","a140","a141","a142","a143","a144","a145","a146","a147","a148","a149","a150","a151","a152","a153","a154","a155","a156","a157","a158","a159","a160","a161","a162","a163","a164","a165","a166","a167","a168","a169","a170","a171","a172","a173","a174","a175","a176","a177","a178","a179","a180","a181","a182","a183","a184","a185","a186","a187","a188","a189","a190","a191","a192","a193","a194","a195","a196","a197","a198","a199","a200","a201","a202","a203","a204","a205","a206","a207","a208","a209","a210","a211","a212","a213","a214","a215","a216","a217","a218","a219","a220","a221","a222","a223","a224","a225","a226","a227","a228","a229","a230","a231","a232","a233","a234","a235","a236","a237","a238","a239","a240","a241","a242","a243","a244","a245","a246","a247","a248","a249","a250","a251","a252","a253","a254","a255","s5","s6","s7","s8","s9","s10","s11","s12","s13","s14","s15","s16","s17","s18","s19","s20","s21","s22","s23","s24","s25","s26","s27","s28","s29","s30","s31","s33","s34","s35","s36","s37","s38","s39","s40","s41","s42","s43","s44","s45","s46","s47","s48","s49","s50","s51","s52","s53","s54","s55","s56","s57","s58","s59","s60","s61","s62","s63","s64","s65","s66","s67","s68","s69","s70","s71","s72","s73","s74","s75","s76","s77","s78","s79","s80","s81","s82","s83","s84","s85","s86","s87","s88","s89","s90","s91","s92","s93","s94","s95","s96","s97","s98","s99","vcc","scc","memory");
}

extern "C" void kernel_launch(void* const* d_in, const int* in_sizes, int n_in, void* d_out, int out_size, void* d_ws, size_t ws_size, hipStream_t stream) {
  k_fused<<<256, 256, 0, stream>>>(d_in[0], d_in[1], d_in[2], d_in[3], d_in[4], d_in[6], d_in[7], d_out, d_ws);
}
```
